```python
import torch

def _softmax(x, axis=-1):
    return torch.softmax(x, dim=axis)
def _gelu(x):
    return torch.nn.functional.gelu(x, approximate="tanh")
def _silu(x):
    return torch.nn.functional.silu(x)
def _sigmoid(x):
    return torch.sigmoid(x)
def _layernorm(x, axis=-1, eps=1e-5):
    m = x.mean(dim=axis, keepdim=True)
    v = x.var(dim=axis, keepdim=True, unbiased=False)
    return (x - m) / torch.sqrt(v + eps)
def _scatter_add(tgt, idx, src, axis=0):
    return tgt.clone().index_add_(axis, idx, src)
def _segment_sum(src, seg, n):
    out = torch.zeros((n,) + src.shape[1:], dtype=src.dtype)
    return out.index_add_(0, seg, src)
def _pad(x, pw):
    flat = tuple(v for pair in reversed(pw) for v in pair)
    return torch.nn.functional.pad(x, flat)

kernel_name = 'recurrent_scan__linear_attn_block__dense_activation_squared_relu__prompt'

def setup_inputs():

    _w = (lambda a: (lambda t: t.to(torch.bfloat16) if t.is_floating_point() else t)(torch.as_tensor(a)))
    def _A_setup():
        B, S, D = 1, 2048, 448
        return {
            "x": _w((lambda *s: torch.randn(*s, dtype=torch.bfloat16) * 0.05)(B, S, D)),
            "wqkv": _w((lambda *s: torch.randn(*s, dtype=torch.bfloat16) * 0.1)(D, 3 * D)),
            "wo": _w((lambda *s: torch.randn(*s, dtype=torch.bfloat16) * 0.05)(D, D)),
        }
    _A_in = _A_setup()

    def _B_setup():
        B, S, D, F = 1, 2048, 448, 1792
        return {
            "x": _w((lambda *s: torch.randn(*s, dtype=torch.bfloat16) * 0.05)(B, S, D)),
            "w1": _w((lambda *s: torch.randn(*s, dtype=torch.bfloat16) * 0.05)(D, F)),
            "b1": _w((lambda *s: torch.randn(*s, dtype=torch.bfloat16) * 0.2)(F)),
            "w2": _w((lambda *s: torch.randn(*s, dtype=torch.bfloat16) * 0.05)(F, D)),
            "b2": _w((lambda *s: torch.randn(*s, dtype=torch.bfloat16) * 0.8)(D)),
        }
    _B_in = _B_setup()

    return {
        "A_x": _A_in["x"],
        "A_wqkv": _A_in["wqkv"],
        "A_wo": _A_in["wo"],
        "B_x": _B_in["x"],
        "B_w1": _B_in["w1"],
        "B_b1": _B_in["b1"],
        "B_w2": _B_in["w2"],
        "B_b2": _B_in["b2"],
    }

def reference(A_x, A_wqkv, A_wo, B_x, B_w1, B_b1, B_w2, B_b2):
    def _A_ref(x, wqkv, wo):
        B, S, D = x.shape
        H = 16; Dh = D // H
        qkv = (lambda x, *d: x.permute(*d))((x @ wqkv).reshape(B, S, 3, H, Dh), 2, 0, 3, 1, 4)
        q, k, v = qkv[0], qkv[1], qkv[2]
        phi = lambda t: torch.where(t > 0, t + 1, torch.exp(t)) + 1e-6
        pq, pk = phi(q), phi(k)
        kv = torch.einsum("bhsd,bhse->bhde", pk, v)
        z = torch.einsum("bhsd->bhd", pk)
        num = torch.einsum("bhqd,bhde->bhqe", pq, kv)
        den = torch.einsum("bhqd,bhd->bhq", pq, z)[..., None] + 1e-6
        o = num / den
        return (lambda x, *d: x.permute(*d))(o, 0, 2, 1, 3).reshape(B, S, D) @ wo

    def _B_ref(x, w1, b1, w2, b2):
        h = torch.einsum("bsd,df->bsf", x, w1) + b1
        zero = torch.zeros_like(h)
        r = torch.where(h > zero, h, zero)
        h = r * r
        out = torch.einsum("bsf,fd->bsd", h, w2) + b2
        return out

    x = A_x
    xn = _layernorm(x, axis=-1)
    s = _A_ref(xn, A_wqkv, A_wo)
    g = _silu(_B_ref(xn, B_w1, B_b1, B_w2, B_b2))
    return x + s * g
```

```cpp
#include <hip/hip_runtime.h>
#include <hip/hip_bf16.h>
typedef unsigned short u16;
__device__ __forceinline__ float b2f(u16 v){ return __uint_as_float(((unsigned)v)<<16); }
__device__ __forceinline__ u16 f2b(float f){ __bf16 b = (__bf16)f; return __builtin_bit_cast(u16,b); }
__device__ __forceinline__ float rb(float f){ return b2f(f2b(f)); }
constexpr int S=2048,D=448,F=1792,H=16,DH=28;

__global__ void k_ln(const u16* x, float* xn){
  int r=blockIdx.x; int t=threadIdx.x;
  float v[7]; float s=0;
  for(int i=0;i<7;i++){ v[i]=b2f(x[r*D+t+64*i]); s+=v[i]; }
  for(int o=32;o;o>>=1) s+=__shfl_xor(s,o);
  float m=s/D; float q=0;
  for(int i=0;i<7;i++){ float d=v[i]-m; q+=d*d; }
  for(int o=32;o;o>>=1) q+=__shfl_xor(q,o);
  float var=q/D;
  float mb=rb(m), den=rb(sqrtf(rb(rb(var)+1e-5f)));
  for(int i=0;i<7;i++) xn[r*D+t+64*i]=rb(rb(v[i]-mb)/den);
}
__global__ void k_gemm(const float* A,const u16* W,float* C,int K,int N){
  int n=blockIdx.x*64+threadIdx.x; int r0=blockIdx.y*8;
  if(n>=N) return;
  float acc[8]={0,0,0,0,0,0,0,0};
  for(int k=0;k<K;k++){ float w=b2f(W[(size_t)k*N+n]);
    #pragma unroll
    for(int i=0;i<8;i++) acc[i]+=A[(size_t)(r0+i)*K+k]*w; }
  for(int i=0;i<8;i++) C[(size_t)(r0+i)*N+n]=rb(acc[i]);
}
__device__ float phi(float t){ return rb(rb(t>0? t+1.f: expf(t))+1e-6f); }
__global__ void k_kv(const float* qkv,float* kv,float* z){
  int h=blockIdx.x; int t=threadIdx.x;
  int d=t/32,e=t%32;
  if(d>=DH||e>DH) return;
  float acc=0;
  for(int s=0;s<S;s++){ float pk=phi(qkv[(size_t)s*3*D+D+h*DH+d]); float v= e<DH? qkv[(size_t)s*3*D+2*D+h*DH+e]:1.f; acc+=pk*v; }
  if(e<DH) kv[(h*DH+d)*DH+e]=rb(acc); else z[h*DH+d]=rb(acc);
}
__global__ void k_o(const float* qkv,const float* kv,const float* z,float* o){
  int s=blockIdx.x; int t=threadIdx.x;
  int h=t/DH,e=t%DH; float num=0,den=0;
  for(int d=0;d<DH;d++){ float pq=phi(qkv[(size_t)s*3*D+h*DH+d]); num+=pq*kv[(h*DH+d)*DH+e]; den+=pq*z[h*DH+d]; }
  o[s*D+t]=rb(rb(num)/rb(rb(den)+1e-6f));
}
__global__ void k_act(float* hbuf,const u16* b1){
  int i=blockIdx.x*256+threadIdx.x; int f=i%F; float h=rb(hbuf[i]+b2f(b1[f])); h=h>0?h:0; hbuf[i]=rb(h*h);
}
__global__ void k_fin(const u16* x,const float* sb,const float* fo,const u16* b2,u16* out){
  int i=blockIdx.x*256+threadIdx.x; int c=i%D; float t=rb(fo[i]+b2f(b2[c])); float g=rb(t/(1.f+expf(-t)));
  out[i]=f2b(b2f(x[i])+rb(sb[i]*g));
}
extern "C" void kernel_launch(void* const* d_in, const int* in_sizes, int n_in, void* d_out, int out_size, void* d_ws, size_t ws_size, hipStream_t stream){
  const u16* x=(const u16*)d_in[0]; const u16* wqkv=(const u16*)d_in[1]; const u16* wo=(const u16*)d_in[2];
  const u16* w1=(const u16*)d_in[4]; const u16* b1=(const u16*)d_in[5]; const u16* w2=(const u16*)d_in[6]; const u16* b2=(const u16*)d_in[7];
  float* ws=(float*)d_ws;
  float* xn=ws; float* qkv=xn+S*D; float* kv=qkv+S*3*D; float* z=kv+H*DH*DH; float* o=z+H*DH; float* sb=o+S*D; float* hb=sb+S*D; float* fo=hb+S*F;
  k_ln<<<S,64,0,stream>>>(x,xn);
  k_gemm<<<dim3(3*D/64,S/8),64,0,stream>>>(xn,wqkv,qkv,D,3*D);
  k_kv<<<H,1024,0,stream>>>(qkv,kv,z);
  k_o<<<S,D,0,stream>>>(qkv,kv,z,o);
  k_gemm<<<dim3(D/64,S/8),64,0,stream>>>(o,wo,sb,D,D);
  k_gemm<<<dim3(F/64,S/8),64,0,stream>>>(xn,w1,hb,D,F);
  k_act<<<S*F/256,256,0,stream>>>(hb,b1);
  k_gemm<<<dim3(D/64,S/8),64,0,stream>>>(hb,w2,fo,F,D);
  k_fin<<<S*D/256,256,0,stream>>>(x,sb,fo,b2,(u16*)d_out);
}
```

```cpp
#include <hip/hip_runtime.h>
#include <hip/hip_bf16.h>
#include <stdint.h>
#pragma clang diagnostic ignored "-Wint-to-pointer-cast"

typedef unsigned short u16;
typedef __bf16 bf16x8 __attribute__((ext_vector_type(8)));
typedef __bf16 bf16x2 __attribute__((ext_vector_type(2)));
typedef short s16x4 __attribute__((ext_vector_type(4)));
typedef float f32x4 __attribute__((ext_vector_type(4)));
typedef float f32x2 __attribute__((ext_vector_type(2)));
typedef unsigned u32x4 __attribute__((ext_vector_type(4)));
typedef unsigned u32x2 __attribute__((ext_vector_type(2)));
#define LDS __attribute__((address_space(3)))
#define DI __device__ __forceinline__

constexpr int S = 2048, D = 448, F = 1792;
constexpr size_t WS_H = 0;
constexpr size_t WS_PQ = WS_H + 128 * 56 * 1024;
constexpr size_t WS_KVP = WS_PQ + 128 * 16 * 1024;
constexpr size_t WS_KV = WS_KVP + 32 * 16 * 2048;
constexpr size_t WS_FLAG = WS_KV + 32 * 1024;
constexpr size_t WS_END = WS_FLAG + 32 * 128;

#define TS(n)
#define TSW(n, wv)

DI u32x4 mk_rsrc(const void* p, unsigned bytes) {
  uint64_t a = (uint64_t)p;
  u32x4 r; r.x = (unsigned)a; r.y = (unsigned)(a >> 32) & 0xffffu; r.z = bytes; r.w = 0x00020000u;
  return r;
}
DI void dma16(unsigned lds_addr, unsigned voff, u32x4 rsrc, unsigned soff) {
  asm volatile("s_mov_b32 m0, %0\n\ts_nop 0\n\tbuffer_load_dwordx4 %1, %2, %3 offen lds"
               :: "s"(lds_addr), "v"(voff), "s"(rsrc), "s"(soff) : "memory");
}
#define SB __builtin_amdgcn_sched_barrier(0)
#define WAIT_BAR(n) asm volatile("s_waitcnt vmcnt(" #n ") lgkmcnt(0)\n\ts_barrier" ::: "memory")
DI f32x4 mfma(bf16x8 a, bf16x8 b, f32x4 c) { return __builtin_amdgcn_mfma_f32_16x16x32_bf16(a, b, c, 0, 0, 0); }
DI bf16x8 ld_frag(unsigned a) { return *(LDS const bf16x8*)a; }
DI bf16x8 tr_frag(unsigned a0, unsigned a1) {
  s16x4 lo = __builtin_amdgcn_ds_read_tr16_b64_v4i16((LDS s16x4*)a0);
  s16x4 hi = __builtin_amdgcn_ds_read_tr16_b64_v4i16((LDS s16x4*)a1);
  return __builtin_bit_cast(bf16x8, __builtin_shufflevector(lo, hi, 0, 1, 2, 3, 4, 5, 6, 7));
}
DI unsigned pk2(float a, float b) { f32x2 v = {a, b}; return __builtin_bit_cast(unsigned, __builtin_convertvector(v, bf16x2)); }
DI float bflo(unsigned u) { return __uint_as_float(u << 16); }
DI float bfhi(unsigned u) { return __uint_as_float(u & 0xffff0000u); }
DI float rbf(float f) { return bflo(pk2(f, 0.f)); }
DI float phi(float t) { return (t > 0.f ? t + 1.f : __expf(t)) + 1e-6f; }

constexpr unsigned SLOT = 43008, OFF_W1 = 16384, OFF_WQ = 30720;
constexpr unsigned L1_DUMMY = 3 * SLOT, L1_ST = L1_DUMMY + 1024, L1_CS = L1_ST + 1024, L1_END = L1_CS + 1024;

__global__ __launch_bounds__(512, 2) void k1(const u16* __restrict__ x, const u16* __restrict__ wqkv,
                                             const u16* __restrict__ w1, const u16* __restrict__ b1, char* __restrict__ ws) {
  extern __shared__ __attribute__((aligned(16))) char smem[];
  const unsigned sb = (unsigned)(size_t)(LDS char*)smem;
  const int bid = blockIdx.x, tid = threadIdx.x, lane = tid & 63;
  const int w = __builtin_amdgcn_readfirstlane(tid >> 6);
  const int xc = bid & 7, ix = bid >> 3;
  const int r = (xc >> 1) * 4 + (ix >> 3), j = (xc & 1) * 8 + (ix & 7);
  const int i = lane & 15, g = lane >> 4, q = i >> 2, p = i & 3;
  const int rg = w & 3, role = w >> 2, odd = j & 1;
  TS(0);
  u32x4 rx = mk_rsrc(x, S * D * 2);
  const unsigned vx = (unsigned)(r * 128 + 16 * w + (lane >> 3)) * 896u + (unsigned)(((lane & 7) ^ (lane >> 3)) << 4);
  unsigned vA, vB;
  {
    int n = w * 64 + lane, rho = n / 14, c = n - rho * 14;
    int k = 8 * ((rho >> 2) & 3) + 4 * (rho >> 4) + (rho & 3);
    unsigned a1 = k * 3584 + j * 224 + c * 16;
    int pq_ = (w < 5) ? w : 5;
    n = pq_ * 64 + lane; rho = n / 12; c = n - rho * 12;
    k = 8 * ((rho >> 2) & 3) + 4 * (rho >> 4) + (rho & 3);
    int u = (c >> 1) ^ ((rho >> 2) & 1);
    unsigned a2 = k * 2688 + (u >> 1) * 896 + j * 56 + (u & 1) * 32 + (c & 1) * 16;
    vA = (w < 7) ? a1 : a2; vB = a2;
  }
  u32x4 rA = (w < 7) ? mk_rsrc(w1, D * F * 2) : mk_rsrc(wqkv, D * 3 * D * 2);
  u32x4 rB = mk_rsrc(wqkv, (w < 5) ? D * 3 * D * 2 : 0);
  const unsigned sA = (w < 7) ? 114688u : 86016u;
  const unsigned lA = (w < 7) ? OFF_W1 + w * 1024 : OFF_WQ + 5 * 1024, lAh = (w < 7) ? 7168u : 6144u;
  const unsigned lB = (w < 5) ? OFF_WQ + w * 1024 : L1_DUMMY, lBh = (w < 5) ? 6144u : 0u, lBm = (w < 5) ? ~0u : 0u;
  auto issue1 = [&](int n, int t, unsigned slot) {
    const unsigned hf = n >= 3;
    if (n % 3 == 0) dma16(sb + slot + w * 2048 + hf * 1024, vx, rx, t * 128 + hf * 7168);
    else if (n % 3 == 1) dma16(sb + slot + lA + hf * lAh, vA, rA, (2 * t + hf) * sA);
    else dma16(sb + (slot & lBm) + lB + hf * lBh, vB, rB, (2 * t + hf) * 86016u);
  };
  auto issue = [&](int t, unsigned slot) {
#pragma unroll
    for (int n = 0; n < 6; ++n) issue1(n, t, slot);
  };
  u32x2 bb1[7];
#pragma unroll
  for (int bb = 0; bb < 7; ++bb) { int cb = bb < 6 ? bb + odd : (odd ? 0 : 6); bb1[bb] = *(const u32x2*)(b1 + j * 112 + cb * 16 + 4 * g); }
  if (bid < 32 && tid == 0) *(unsigned*)(ws + WS_FLAG + bid * 128) = 0u;
  TS(7);
  issue(0, 0); issue(1, SLOT); issue(2, 2 * SLOT);
  TS(9);

  const unsigned xa0 = sb + (rg * 4 + (i >> 3)) * 1024 + (i & 7) * 128 + ((g ^ (i & 7)) << 4), xa1 = xa0 ^ 64u;
  bf16x8 ones = __builtin_bit_cast(bf16x8, (u32x4){0x3f803f80u, 0x3f803f80u, 0x3f803f80u, 0x3f803f80u});
  asm volatile("" : "+v"(ones));
  const f32x4 z4 = {0.f, 0.f, 0.f, 0.f};
  const int R0 = r * 8 + rg * 2;

  if (role == 0) {
    const unsigned wr = sb + OFF_W1 + (4 * g + q) * 224 + p * 8;
    const unsigned wa = wr + odd * 32, wa6 = wr + (odd ? 0 : 192);
    const int c1 = rg + 4 < 7 ? rg + 4 : 6;
    const unsigned ca0 = wr + rg * 32, ca1 = wr + c1 * 32;
    f32x4 acc[2][7], cs0 = z4, cs1 = z4;
#pragma unroll
    for (int a = 0; a < 2; ++a)
#pragma unroll
      for (int b = 0; b < 7; ++b) acc[a][b] = z4;
    bf16x8 X[2][2], W[2][9];
#define RD0(buf, n, so_, hf) do { \
      if ((n) < 2) X[buf][(n) & 1] = ld_frag(((hf) ? xa1 : xa0) + (so_) + ((n) & 1) * 2048); \
      else { const unsigned a_ = ((n) < 8 ? wa + ((n) - 2) * 32 : (n) == 8 ? wa6 : (n) == 9 ? ca0 : ca1) + (so_) + (hf) * 7168; \
             W[buf][(n) < 2 ? 0 : (n) - 2] = tr_frag(a_, a_ + 3584); } } while (0)
#define MM0(buf, m) do { \
      if ((m) < 14) acc[(m) & 1][(m) >> 1] = mfma(W[buf][(m) >> 1], X[buf][(m) & 1], acc[(m) & 1][(m) >> 1]); \
      else if ((m) == 14) cs0 = mfma(ones, W[buf][7], cs0); else cs1 = mfma(ones, W[buf][8], cs1); } while (0)
    WAIT_BAR(12);
#pragma unroll
    for (int n = 0; n < 11; ++n) RD0(0, n, 0u, 0);
    unsigned so = 0;
#pragma unroll 1
    for (int t = 0; t < 7; ++t) {
#pragma unroll
      for (int m = 0; m < 16; ++m) { MM0(0, m); SB; if (m < 11) RD0(1, m, so, 1); SB; }
      WAIT_BAR(6);
      const unsigned sn = (so == 2 * SLOT) ? 0 : so + SLOT;
      if (t == 4) { rx.z = 0; rA.z = 0; rB.z = 0; }
#pragma unroll
      for (int m = 0; m < 16; ++m) { MM0(1, m); SB; if (m < 11) RD0(0, m, sn, 0); if (m >= 10) issue1(m - 10, t + 3, so); SB; }
      so = sn;
    }
    TS(1);
    if (g == 0) { *(LDS float*)(sb + L1_CS + (rg * 16 + i) * 4) = cs0[0]; *(LDS float*)(sb + L1_CS + (c1 * 16 + i) * 4) = cs1[0]; }
    WAIT_BAR(0);
    char* hb = ws + WS_H;
    const int kap0 = (7 * j + odd) >> 1, kaps = (7 * j + (odd ? 0 : 6)) >> 1;
    f32x4 cs[7];
#pragma unroll
    for (int bb = 0; bb < 7; ++bb) { int cb = bb < 6 ? bb + odd : (odd ? 0 : 6); cs[bb] = *(LDS const f32x4*)(sb + L1_CS + (cb * 16 + 4 * g) * 4); }
#pragma unroll
    for (int rb = 0; rb < 2; ++rb) {
      const f32x2 st = *(LDS const f32x2*)(sb + L1_ST + (rg * 32 + rb * 16 + i) * 8);
      unsigned pk[14];
#pragma unroll
      for (int bb = 0; bb < 7; ++bb) {
        float bv[4] = {bflo(bb1[bb].x), bfhi(bb1[bb].x), bflo(bb1[bb].y), bfhi(bb1[bb].y)};
        float hv[4];
#pragma unroll
        for (int e = 0; e < 4; ++e) {
          float t = fmaf(acc[rb][bb][e], st.x, fmaf(st.y, cs[bb][e], bv[e]));
          t = fmaxf(t, 0.f); hv[e] = t * t;
        }
        pk[2 * bb] = pk2(hv[0], hv[1]); pk[2 * bb + 1] = pk2(hv[2], hv[3]);
      }
      char* row = hb + (size_t)(R0 + rb) * 56 * 1024;
#pragma unroll
      for (int pp = 0; pp < 3; ++pp)
        *(u32x4*)(row + (kap0 + pp) * 1024 + lane * 16) = (u32x4){pk[4 * pp], pk[4 * pp + 1], pk[4 * pp + 2], pk[4 * pp + 3]};
      *(u32x2*)(row + kaps * 1024 + odd * 512 + lane * 8) = (u32x2){pk[12], pk[13]};
    }
    TS(2);
  } else {
    const unsigned wr = sb + OFF_WQ + (4 * g + q) * 192 + p * 8;
    const unsigned qa0 = wr + (g & 1) * 32, qa1 = wr + ((g & 1) ^ 1) * 32;
    const int c1 = rg + 4 < 6 ? rg + 4 : 5;
    const unsigned ca0 = wr + ((rg ^ (g & 1)) * 32), ca1 = wr + ((c1 ^ (g & 1)) * 32);
    const bool padq = i >= 12;
    f32x4 acc[2][6], cs0 = z4, cs1 = z4, sq0 = z4, sq1 = z4;
#pragma unroll
    for (int a = 0; a < 2; ++a)
#pragma unroll
      for (int b = 0; b < 6; ++b) acc[a][b] = z4;
    bf16x8 X[2][2], W[2][8];
#define RD1(buf, n, so_, hf) do { \
      if ((n) < 2) X[buf][(n) & 1] = ld_frag(((hf) ? xa1 : xa0) + (so_) + ((n) & 1) * 2048); \
      else { const unsigned a_ = ((n) < 8 ? (((n) & 1) ? qa1 : qa0) + (((n) - 2) >> 1) * 64 : (n) == 8 ? ca0 : ca1) + (so_) + (hf) * 6144; \
             W[buf][(n) < 2 ? 0 : (n) - 2] = tr_frag(a_, a_ + 3072); } } while (0)
#define MM1(buf, m) do { \
      if ((m) == 0) sq0 = mfma(X[buf][0], X[buf][0], sq0); else if ((m) == 1) sq1 = mfma(X[buf][1], X[buf][1], sq1); \
      else if ((m) < 6) { if ((m) == 4) W[buf][1] = padq ? ones : W[buf][1]; \
                          acc[(m) & 1][((m) - 2) >> 1] = mfma(W[buf][((m) - 2) >> 1], X[buf][(m) & 1], acc[(m) & 1][((m) - 2) >> 1]); } \
      else if ((m) < 14) acc[(m) & 1][((m) - 2) >> 1] = mfma(X[buf][(m) & 1], W[buf][((m) - 2) >> 1], acc[(m) & 1][((m) - 2) >> 1]); \
      else if ((m) == 14) cs0 = mfma(ones, W[buf][6], cs0); else cs1 = mfma(ones, W[buf][7], cs1); } while (0)
    WAIT_BAR(12);
#pragma unroll
    for (int n = 0; n < 10; ++n) RD1(0, n, 0u, 0);
    unsigned so = 0;
#pragma unroll 1
    for (int t = 0; t < 7; ++t) {
#pragma unroll
      for (int m = 0; m < 16; ++m) { MM1(0, m); SB; if (m < 10) RD1(1, m, so, 1); SB; }
      WAIT_BAR(6);
      const unsigned sn = (so == 2 * SLOT) ? 0 : so + SLOT;
      if (t == 4) { rx.z = 0; rA.z = 0; rB.z = 0; }
#pragma unroll
      for (int m = 0; m < 16; ++m) { MM1(1, m); SB; if (m < 10) RD1(0, m, sn, 0); if (m >= 10) issue1(m - 10, t + 3, so); SB; }
      so = sn;
    }
    TSW(4, 4);
    if (g == 0) { *(LDS float*)(sb + L1_CS + (112 + rg * 16 + i) * 4) = cs0[0]; *(LDS float*)(sb + L1_CS + (112 + c1 * 16 + i) * 4) = cs1[0]; }
#pragma unroll
    for (int rb = 0; rb < 2; ++rb) {
      const f32x4 sq = rb ? sq1 : sq0;
      float s01 = (p & 1) ? sq[1] : sq[0], s23 = (p & 1) ? sq[3] : sq[2];
      float ss = (p & 2) ? s23 : s01;
      float sm = __shfl(acc[rb][1][0], 48 + i);
      float m = sm * (1.f / D);
      float var = fmaxf(ss * (1.f / D) - m * m, 0.f);
      float den = rbf(__builtin_amdgcn_sqrtf(rbf(rbf(var) + 1e-5f)));
      float rs = __builtin_amdgcn_rcpf(den);
      if (g == q) *(LDS f32x2*)(sb + L1_ST + (rg * 32 + rb * 16 + i) * 8) = (f32x2){rs, -rbf(m) * rs};
    }
    WAIT_BAR(0);
    TSW(5, 4);
    {
      const f32x4 cq0 = *(LDS const f32x4*)(sb + L1_CS + (112 + 4 * g) * 4), cq1 = *(LDS const f32x4*)(sb + L1_CS + (128 + 4 * g) * 4);
#pragma unroll
      for (int rb = 0; rb < 2; ++rb) {
        const f32x2 st = *(LDS const f32x2*)(sb + L1_ST + (rg * 32 + rb * 16 + i) * 8);
        float v0[4], v1[4];
#pragma unroll
        for (int e = 0; e < 4; ++e) {
          v0[e] = phi(fmaf(acc[rb][0][e], st.x, st.y * cq0[e]));
          v1[e] = phi(fmaf(acc[rb][1][e], st.x, st.y * cq1[e]));
        }
        u32x4 o = {pk2(v0[0], v0[1]), pk2(v0[2], v0[3]), pk2(v1[0], v1[1]), pk2(v1[2], v1[3])};
        if (g == 3) { o.z = 0; o.w = 0; }
        *(u32x4*)(ws + WS_PQ + ((size_t)(R0 + rb) * 16 + j) * 1024 + lane * 16) = o;
      }
    }
    {
      float ck[4];
#pragma unroll
      for (int b = 0; b < 4; ++b) ck[b] = *(LDS const float*)(sb + L1_CS + (144 + b * 16 + i) * 4);
      u32x4 fr[4];
#pragma unroll
      for (int rb = 0; rb < 2; ++rb) {
        const f32x4 s01 = *(LDS const f32x4*)(sb + L1_ST + (rg * 32 + rb * 16 + 4 * g) * 8);
        const f32x4 s23 = *(LDS const f32x4*)(sb + L1_ST + (rg * 32 + rb * 16 + 4 * g) * 8 + 16);
        const float rs[4] = {s01[0], s01[2], s23[0], s23[2]}, mr[4] = {s01[1], s01[3], s23[1], s23[3]};
#pragma unroll
        for (int b = 0; b < 4; ++b) {
          float v[4];
#pragma unroll
          for (int e = 0; e < 4; ++e) {
            float t = fmaf(acc[rb][2 + b][e], rs[e], mr[e] * ck[b]);
            v[e] = b < 2 ? phi(t) : t;
          }
          fr[b][2 * rb] = pk2(v[0], v[1]); fr[b][2 * rb + 1] = pk2(v[2], v[3]);
        }
      }
      if (i == 12) fr[3] = (u32x4){0x3f803f80u, 0x3f803f80u, 0x3f803f80u, 0x3f803f80u};
#pragma unroll
      for (int be = 0; be < 2; ++be)
#pragma unroll
        for (int bd = 0; bd < 2; ++bd) {
          f32x4 kv = mfma(__builtin_bit_cast(bf16x8, fr[bd]), __builtin_bit_cast(bf16x8, fr[2 + be]), z4);
          *(LDS f32x4*)(sb + rg * 4096 + be * 2048 + lane * 32 + bd * 16) = kv;
        }
      TSW(6, 4);
      WAIT_BAR(63);
      const int t = tid - 256;
      f32x4 s = *(LDS const f32x4*)(sb + t * 16);
#pragma unroll
      for (int a = 1; a < 4; ++a) s += *(LDS const f32x4*)(sb + a * 4096 + t * 16);
      *(f32x4*)(ws + WS_KVP + ((size_t)(j * 2 + (t >> 7)) * 16 + r) * 2048 + (t & 127) * 16) = s;
    }
    TSW(3, 4);
  }
}

constexpr unsigned L2_KV = 131072;
#define LD4(dst, voff, rsrc, soff, imm) asm volatile("buffer_load_dwordx4 %0, %1, %2, %3 offen offset:" #imm : "=v"(dst) : "v"(voff), "s"(rsrc), "s"(soff) : "memory")
#define LD2(dst, voff, rsrc, soff, imm) asm volatile("buffer_load_dwordx2 %0, %1, %2, %3 offen offset:" #imm : "=v"(dst) : "v"(voff), "s"(rsrc), "s"(soff) : "memory")

__global__ __launch_bounds__(512, 2) void k2(const u16* __restrict__ x, const u16* __restrict__ wo, const u16* __restrict__ w2,
                                             const u16* __restrict__ b2, u16* __restrict__ out, char* __restrict__ ws) {
  extern __shared__ __attribute__((aligned(16))) char smem[];
  const unsigned sb = (unsigned)(size_t)(LDS char*)smem;
  const int bid = blockIdx.x, tid = threadIdx.x, lane = tid & 63;
  const int w = __builtin_amdgcn_readfirstlane(tid >> 6);
  const int xc = bid & 7, ix = bid >> 3;
  TS(8);
  if (ix < 4) {
    const int id = xc * 4 + ix, t = tid & 127, gq = tid >> 7;
    const char* src = ws + WS_KVP + ((size_t)id * 16 + gq * 4) * 2048 + t * 16;
    f32x4 s = *(const f32x4*)src;
#pragma unroll
    for (int a = 1; a < 4; ++a) s += *(const f32x4*)(src + a * 2048);
    *(LDS f32x4*)(sb + gq * 2048 + t * 16) = s;
    __syncthreads();
    if (tid < 64) {
      f32x4 a = {0.f, 0.f, 0.f, 0.f}, b = a;
#pragma unroll
      for (int e = 0; e < 4; ++e) { a += *(LDS const f32x4*)(sb + e * 2048 + tid * 32); b += *(LDS const f32x4*)(sb + e * 2048 + tid * 32 + 16); }
      u32x4 o = {pk2(a[0], a[1]), pk2(a[2], a[3]), pk2(b[0], b[1]), pk2(b[2], b[3])};
      if (tid >= 48) { o.z = 0; o.w = 0; }
      const u32x4 rk = mk_rsrc(ws + WS_KV, 32768);
      asm volatile("buffer_store_dwordx4 %0, %1, %2, 0 offen sc1\n\ts_waitcnt vmcnt(0)" :: "v"(o), "v"(id * 1024 + tid * 16), "s"(rk) : "memory");
      if (tid == 0) __hip_atomic_store((unsigned*)(ws + WS_FLAG + id * 128), 1u, __ATOMIC_RELAXED, __HIP_MEMORY_SCOPE_AGENT);
    }
    return;
  }
  const int rt = xc * 4 + (ix - 4) / 7, ct = (ix - 4) % 7;
  const int i = lane & 15, g = lane >> 4, q = i >> 2, p = i & 3;
  const int rb = w & 3, kh = w >> 2;
  const int R = rt * 4 + rb;
  const u32x4 rw2 = mk_rsrc(w2, F * D * 2), rwo = mk_rsrc(wo, D * D * 2), rws = mk_rsrc(ws, (unsigned)WS_END);
  const unsigned swz = (unsigned)(((lane & 7) ^ (2 * ((lane >> 4) & 3))) << 4);
  const unsigned vw2 = (unsigned)(kh * 896 + rb * 8 + (lane >> 3)) * 896u + ct * 128 + swz;
  const unsigned vwo = (unsigned)(kh * 224 + rb * 8 + (lane >> 3)) * 896u + ct * 128 + swz;
  const unsigned v16 = lane * 16, v8 = lane * 8;
  const unsigned hs = (unsigned)WS_H + (unsigned)(R * 56 + kh * 28) * 1024u;
  u32x4 hf[28], pq[8], kvr[4];
  u32x2 hl[4], hh[4];
  unsigned flag;
#define HL(u) do { if ((u) % 7 == 3) { LD2(hl[(u) / 7], v8, rws, hs + (u) * 1024, 0); LD2(hh[(u) / 7], v8, rws, hs + (u) * 1024, 512); } \
                   else LD4(hf[u], v16, rws, hs + (u) * 1024, 0); } while (0)
#define GRP(bt, sl) do { _Pragma("unroll") for (int v = 0; v < 4; ++v) { \
      dma16(sb + (sl) * 32768 + v * 8192 + w * 1024, vw2, rw2, (unsigned)((bt) * 4 + v) * 28672u); HL((bt) * 4 + v); } } while (0)
#define GRPO(z, sl) do { _Pragma("unroll") for (int v = 0; v < 4; ++v) \
      dma16(sb + (sl) * 32768 + kh * 16384 + (v * 4 + rb) * 1024, vwo, rwo, (unsigned)((z) * 112 + v * 32) * 896u); } while (0)
  GRP(0, 0); GRP(1, 1); GRP(2, 2); GRP(3, 3);
  unsigned ra[2][4];
  {
    const int sw = ((4 * g + q) >> 1) & 3;
#pragma unroll
    for (int cb = 0; cb < 4; ++cb) {
      ra[0][cb] = sb + kh * 4096 + (4 * g + q) * 128 + ((cb ^ sw) << 5) + p * 8; ra[1][cb] = ra[0][cb] + 65536;
      asm volatile("" : "+v"(ra[0][cb]), "+v"(ra[1][cb]));
    }
  }
  const f32x4 z4 = {0.f, 0.f, 0.f, 0.f};
  f32x4 af[4] = {z4, z4, z4, z4}, as[4] = {z4, z4, z4, z4};
  bf16x8 W[2][4];
#define PIN4(a) asm volatile("" : "+v"(a))
#define PINH(u) do { if ((u) % 7 == 3) { PIN4(hl[(u) / 7]); PIN4(hh[(u) / 7]); hf[u] = (u32x4){hl[(u) / 7].x, hl[(u) / 7].y, hh[(u) / 7].x, hh[(u) / 7].y}; } else PIN4(hf[u]); } while (0)
#define RDW(buf, cb, sl, v) do { const unsigned a_ = ra[(sl) >> 1][cb] + ((sl) & 1) * 32768 + (v) * 8192; W[buf][cb] = tr_frag(a_, a_ + 2048); } while (0)
#define BATCH(bt, sl) do { PINH((bt) * 4); PINH((bt) * 4 + 1); PINH((bt) * 4 + 2); PINH((bt) * 4 + 3); \
      _Pragma("unroll") for (int cb = 0; cb < 4; ++cb) RDW(0, cb, sl, 0); SB; \
      _Pragma("unroll") for (int v = 0; v < 4; ++v) _Pragma("unroll") for (int cb = 0; cb < 4; ++cb) { \
        if (v < 3) RDW((v + 1) & 1, cb, sl, v + 1); SB; \
        af[cb] = mfma(W[v & 1][cb], __builtin_bit_cast(bf16x8, hf[(bt) * 4 + v]), af[cb]); SB; } } while (0)
  TS(14); WAIT_BAR(25); TS(15); BATCH(0, 0);
  WAIT_BAR(17); GRP(4, 0);
  asm volatile("buffer_load_dword %0, %1, %2, %3 offen sc1" : "=v"(flag) : "v"((lane & 3) * 128), "s"(rws), "s"((unsigned)WS_FLAG + w * 512) : "memory");
  {
    const unsigned ps = (unsigned)WS_PQ + (unsigned)(R * 16 + kh * 8) * 1024u, ps2 = ps + 4096;
    LD4(pq[0], v16, rws, ps, 0); LD4(pq[1], v16, rws, ps, 1024); LD4(pq[2], v16, rws, ps, 2048); LD4(pq[3], v16, rws, ps, 3072);
    LD4(pq[4], v16, rws, ps2, 0); LD4(pq[5], v16, rws, ps2, 1024); LD4(pq[6], v16, rws, ps2, 2048); LD4(pq[7], v16, rws, ps2, 3072);
  }
  BATCH(1, 1);
  WAIT_BAR(26); GRPO(0, 1);
  BATCH(2, 2);
  WAIT_BAR(22); GRPO(1, 2);
  BATCH(3, 3);
  WAIT_BAR(16);
  PIN4(flag);
  if (!__all(flag == 1u)) {
    unsigned spins = 0;
    do {
      __builtin_amdgcn_s_sleep(2);
      flag = __hip_atomic_load((unsigned*)(ws + WS_FLAG + w * 512 + (lane & 3) * 128), __ATOMIC_RELAXED, __HIP_MEMORY_SCOPE_AGENT);
    } while (!__all(flag == 1u) && ++spins < (1u << 20));
  }
  {
    const unsigned ks = (unsigned)WS_KV + w * 4096;
    asm volatile("buffer_load_dwordx4 %0, %4, %5, %6 offen sc1\n\tbuffer_load_dwordx4 %1, %4, %5, %6 offen offset:1024 sc1\n\t"
                 "buffer_load_dwordx4 %2, %4, %5, %6 offen offset:2048 sc1\n\tbuffer_load_dwordx4 %3, %4, %5, %6 offen offset:3072 sc1"
                 : "=&v"(kvr[0]), "=&v"(kvr[1]), "=&v"(kvr[2]), "=&v"(kvr[3]) : "v"(v16), "s"(rws), "s"(ks) : "memory");
  }
  GRP(5, 3);
  BATCH(4, 0);
  asm volatile("s_waitcnt vmcnt(8)" ::: "memory");
#pragma unroll
  for (int a = 0; a < 4; ++a) { PIN4(kvr[a]); *(LDS u32x4*)(sb + L2_KV + w * 4096 + a * 1024 + lane * 16) = kvr[a]; }
  WAIT_BAR(8);
  GRP(6, 0);
  u32x2 xr[2], br[2];
  {
    const u32x4 rx = mk_rsrc(x, S * D * 2), rb2 = mk_rsrc(b2, D * 2);
    const unsigned co = ct * 128 + kh * 64 + g * 8;
    const unsigned xo = (unsigned)(R * 16 + i) * 896u + co;
    LD2(xr[0], xo, rx, 0u, 0); LD2(xr[1], xo, rx, 0u, 32); LD2(br[0], co, rb2, 0u, 0); LD2(br[1], co, rb2, 0u, 32);
  }
  TS(12);
  {
#pragma unroll
    for (int a = 0; a < 8; ++a) PIN4(pq[a]);
    const unsigned ka = sb + L2_KV + kh * 16384 + lane * 16;
    f32x4 n0[8], n1[8];
#pragma unroll
    for (int a = 0; a < 8; ++a) {
      const bf16x8 pf = __builtin_bit_cast(bf16x8, pq[a]);
      n1[a] = mfma(ld_frag(ka + a * 2048 + 1024), pf, z4); n0[a] = mfma(ld_frag(ka + a * 2048), pf, z4);
    }
    SB;
    float rd[8];
#pragma unroll
    for (int a = 0; a < 8; ++a) rd[a] = __shfl(n1[a][0], 48 + i);
    SB;
    const unsigned km = g == 3 ? 0u : ~0u;
#pragma unroll
    for (int a = 0; a < 8; ++a) {
      const float r_ = __builtin_amdgcn_rcpf(rd[a] + 1e-6f);
      pq[a] = (u32x4){pk2(n0[a][0] * r_, n0[a][1] * r_), pk2(n0[a][2] * r_, n0[a][3] * r_), pk2(n1[a][0] * r_, n1[a][1] * r_) & km, pk2(n1[a][2] * r_, n1[a][3] * r_) & km};
    }
    unsigned rq[4];
#pragma unroll
    for (int cb = 0; cb < 4; ++cb) { rq[cb] = ra[0][cb] + kh * 12288 + 32768; PIN4(rq[cb]); }
#define RDO(buf, cb, a) do { const unsigned a_ = rq[((a) & 1) ? (cb) ^ 2 : (cb)] + ((a) >> 2) * 32768 + ((a) & 3) * 3584; W[buf][cb] = tr_frag(a_, a_ + 2048); } while (0)
#pragma unroll
    for (int cb = 0; cb < 4; ++cb) RDO(0, cb, 0);
    SB;
#pragma unroll
    for (int a = 0; a < 8; ++a)
#pragma unroll
      for (int cb = 0; cb < 4; ++cb) {
        if (a < 7) RDO((a + 1) & 1, cb, a + 1);
        SB;
        as[cb] = mfma(W[a & 1][cb], __builtin_bit_cast(bf16x8, pq[a]), as[cb]); SB;
      }
  }
  TS(13);
  WAIT_BAR(13);
  BATCH(5, 3);
  WAIT_BAR(0);
  TS(10);
  BATCH(6, 0);
  {
    const unsigned ex = sb + 32768 + w * 4096 + lane * 16;
    *(LDS f32x4*)(ex) = kh ? af[0] : af[2]; *(LDS f32x4*)(ex + 1024) = kh ? af[1] : af[3];
    *(LDS f32x4*)(ex + 2048) = kh ? as[0] : as[2]; *(LDS f32x4*)(ex + 3072) = kh ? as[1] : as[3];
    WAIT_BAR(0);
    const unsigned ey = sb + 32768 + (w ^ 4) * 4096 + lane * 16;
    PIN4(xr[0]); PIN4(xr[1]); PIN4(br[0]); PIN4(br[1]);
#pragma unroll
    for (int c = 0; c < 2; ++c) {
      f32x4 f = (kh ? af[2 + c] : af[c]) + *(LDS const f32x4*)(ey + c * 1024);
      f32x4 s = (kh ? as[2 + c] : as[c]) + *(LDS const f32x4*)(ey + 2048 + c * 1024);
      const float xv[4] = {bflo(xr[c].x), bfhi(xr[c].x), bflo(xr[c].y), bfhi(xr[c].y)};
      const float bv[4] = {bflo(br[c].x), bfhi(br[c].x), bflo(br[c].y), bfhi(br[c].y)};
      float o[4];
#pragma unroll
      for (int e = 0; e < 4; ++e) {
        float t = f[e] + bv[e];
        float gt = t * __builtin_amdgcn_rcpf(1.f + __expf(-t));
        o[e] = fmaf(s[e], gt, xv[e]);
      }
      *(u32x2*)((char*)out + (size_t)(R * 16 + i) * 896 + ct * 128 + kh * 64 + c * 32 + g * 8) = (u32x2){pk2(o[0], o[1]), pk2(o[2], o[3])};
    }
  }
  TS(11);
}

extern "C" void kernel_launch(void* const* d_in, const int* in_sizes, int n_in, void* d_out, int out_size, void* d_ws,
                              size_t ws_size, hipStream_t stream) {
  const u16* x = (const u16*)d_in[0]; const u16* wqkv = (const u16*)d_in[1]; const u16* wo = (const u16*)d_in[2];
  const u16* w1 = (const u16*)d_in[4]; const u16* b1 = (const u16*)d_in[5];
  const u16* w2 = (const u16*)d_in[6]; const u16* b2 = (const u16*)d_in[7];
  (void)hipFuncSetAttribute((const void*)k1, hipFuncAttributeMaxDynamicSharedMemorySize, L1_END);
  (void)hipFuncSetAttribute((const void*)k2, hipFuncAttributeMaxDynamicSharedMemorySize, 163840);
  k1<<<256, 512, L1_END, stream>>>(x, wqkv, w1, b1, (char*)d_ws);
  k2<<<256, 512, 163840, stream>>>(x, wo, w2, b2, (u16*)d_out, (char*)d_ws);
}
```

```cpp
#include <hip/hip_runtime.h>
#include <hip/hip_bf16.h>
#include <stdint.h>
#pragma clang diagnostic ignored "-Wint-to-pointer-cast"

typedef unsigned short u16;
typedef __bf16 bf16x8 __attribute__((ext_vector_type(8)));
typedef __bf16 bf16x2 __attribute__((ext_vector_type(2)));
typedef short s16x4 __attribute__((ext_vector_type(4)));
typedef float f32x4 __attribute__((ext_vector_type(4)));
typedef float f32x2 __attribute__((ext_vector_type(2)));
typedef unsigned u32x4 __attribute__((ext_vector_type(4)));
typedef unsigned u32x2 __attribute__((ext_vector_type(2)));
#define LDS __attribute__((address_space(3)))
#define DI __device__ __forceinline__

constexpr int S = 2048, D = 448, F = 1792;
constexpr size_t WS_H = 0;
constexpr size_t WS_PQ = WS_H + 128 * 56 * 1024;
constexpr size_t WS_KVP = WS_PQ + 128 * 16 * 1024;
constexpr size_t WS_KV = WS_KVP + 32 * 16 * 2048;
constexpr size_t WS_FLAG = WS_KV + 32 * 1024;
constexpr size_t WS_END = WS_FLAG + 32 * 128;

#define TS(n)
#define TSW(n, wv)

DI u32x4 mk_rsrc(const void* p, unsigned bytes) {
  uint64_t a = (uint64_t)p;
  u32x4 r; r.x = (unsigned)a; r.y = (unsigned)(a >> 32) & 0xffffu; r.z = bytes; r.w = 0x00020000u;
  return r;
}
DI void dma16(unsigned lds_addr, unsigned voff, u32x4 rsrc, unsigned soff) {
  asm volatile("s_mov_b32 m0, %0\n\ts_nop 0\n\tbuffer_load_dwordx4 %1, %2, %3 offen lds"
               :: "s"(lds_addr), "v"(voff), "s"(rsrc), "s"(soff) : "memory");
}
#define SB __builtin_amdgcn_sched_barrier(0)
#define WAIT_BAR(n) asm volatile("s_waitcnt vmcnt(" #n ") lgkmcnt(0)\n\ts_barrier" ::: "memory")
DI f32x4 mfma(bf16x8 a, bf16x8 b, f32x4 c) { return __builtin_amdgcn_mfma_f32_16x16x32_bf16(a, b, c, 0, 0, 0); }
DI bf16x8 ld_frag(unsigned a) { return *(LDS const bf16x8*)a; }
DI bf16x8 tr_frag(unsigned a0, unsigned a1) {
  s16x4 lo = __builtin_amdgcn_ds_read_tr16_b64_v4i16((LDS s16x4*)a0);
  s16x4 hi = __builtin_amdgcn_ds_read_tr16_b64_v4i16((LDS s16x4*)a1);
  return __builtin_bit_cast(bf16x8, __builtin_shufflevector(lo, hi, 0, 1, 2, 3, 4, 5, 6, 7));
}
DI unsigned pk2(float a, float b) { f32x2 v = {a, b}; return __builtin_bit_cast(unsigned, __builtin_convertvector(v, bf16x2)); }
DI float bflo(unsigned u) { return __uint_as_float(u << 16); }
DI float bfhi(unsigned u) { return __uint_as_float(u & 0xffff0000u); }
DI float rbf(float f) { return bflo(pk2(f, 0.f)); }
DI float phi(float t) { return (t > 0.f ? t + 1.f : __expf(t)) + 1e-6f; }

constexpr unsigned SLOT = 43008, OFF_W1 = 16384, OFF_WQ = 30720;
constexpr unsigned L1_DUMMY = 3 * SLOT, L1_ST = L1_DUMMY + 1024, L1_CS = L1_ST + 1024, L1_END = L1_CS + 1024;

__global__ __launch_bounds__(512, 2) void k1(const u16* __restrict__ x, const u16* __restrict__ wqkv,
                                             const u16* __restrict__ w1, const u16* __restrict__ b1, char* __restrict__ ws) {
  extern __shared__ __attribute__((aligned(16))) char smem[];
  const unsigned sb = (unsigned)(size_t)(LDS char*)smem;
  const int bid = blockIdx.x, tid = threadIdx.x, lane = tid & 63;
  const int w = __builtin_amdgcn_readfirstlane(tid >> 6);
  const int xc = bid & 7, ix = bid >> 3;
  const int r = (xc >> 1) * 4 + (ix >> 3), j = (xc & 1) * 8 + (ix & 7);
  const int i = lane & 15, g = lane >> 4, q = i >> 2, p = i & 3;
  const int rg = w & 3, role = w >> 2, odd = j & 1;
  TS(0);
  u32x4 rx = mk_rsrc(x, S * D * 2);
  const unsigned vx = (unsigned)(r * 128 + 16 * w + (lane >> 3)) * 896u + (unsigned)(((lane & 7) ^ (lane >> 3)) << 4);
  unsigned vA, vB;
  {
    int n = w * 64 + lane, rho = n / 14, c = n - rho * 14;
    int k = 8 * ((rho >> 2) & 3) + 4 * (rho >> 4) + (rho & 3);
    unsigned a1 = k * 3584 + j * 224 + c * 16;
    int pq_ = (w < 5) ? w : 5;
    n = pq_ * 64 + lane; rho = n / 12; c = n - rho * 12;
    k = 8 * ((rho >> 2) & 3) + 4 * (rho >> 4) + (rho & 3);
    int u = (c >> 1) ^ ((rho >> 2) & 1);
    unsigned a2 = k * 2688 + (u >> 1) * 896 + j * 56 + (u & 1) * 32 + (c & 1) * 16;
    vA = (w < 7) ? a1 : a2; vB = a2;
  }
  u32x4 rA = (w < 7) ? mk_rsrc(w1, D * F * 2) : mk_rsrc(wqkv, D * 3 * D * 2);
  u32x4 rB = mk_rsrc(wqkv, (w < 5) ? D * 3 * D * 2 : 0);
  const unsigned sA = (w < 7) ? 114688u : 86016u;
  const unsigned lA = (w < 7) ? OFF_W1 + w * 1024 : OFF_WQ + 5 * 1024, lAh = (w < 7) ? 7168u : 6144u;
  const unsigned lB = (w < 5) ? OFF_WQ + w * 1024 : L1_DUMMY, lBh = (w < 5) ? 6144u : 0u, lBm = (w < 5) ? ~0u : 0u;
  auto issue1 = [&](int n, int t, unsigned slot) {
    const unsigned hf = n >= 3;
    if (n % 3 == 0) dma16(sb + slot + w * 2048 + hf * 1024, vx, rx, t * 128 + hf * 7168);
    else if (n % 3 == 1) dma16(sb + slot + lA + hf * lAh, vA, rA, (2 * t + hf) * sA);
    else dma16(sb + (slot & lBm) + lB + hf * lBh, vB, rB, (2 * t + hf) * 86016u);
  };
  auto issue = [&](int t, unsigned slot) {
#pragma unroll
    for (int n = 0; n < 6; ++n) issue1(n, t, slot);
  };
  u32x2 bb1[7];
#pragma unroll
  for (int bb = 0; bb < 7; ++bb) { int cb = bb < 6 ? bb + odd : (odd ? 0 : 6); bb1[bb] = *(const u32x2*)(b1 + j * 112 + cb * 16 + 4 * g); }
  if (bid < 32 && tid == 0) *(unsigned*)(ws + WS_FLAG + bid * 128) = 0u;
  TS(7);
  issue(0, 0); issue(1, SLOT); issue(2, 2 * SLOT);
  TS(9);

  const unsigned xa0 = sb + (rg * 4 + (i >> 3)) * 1024 + (i & 7) * 128 + ((g ^ (i & 7)) << 4), xa1 = xa0 ^ 64u;
  bf16x8 ones = __builtin_bit_cast(bf16x8, (u32x4){0x3f803f80u, 0x3f803f80u, 0x3f803f80u, 0x3f803f80u});
  asm volatile("" : "+v"(ones));
  const f32x4 z4 = {0.f, 0.f, 0.f, 0.f};
  const int R0 = r * 8 + rg * 2;

  if (role == 0) {
    const unsigned wr = sb + OFF_W1 + (4 * g + q) * 224 + p * 8;
    const unsigned wa = wr + odd * 32, wa6 = wr + (odd ? 0 : 192);
    const int c1 = rg + 4 < 7 ? rg + 4 : 6;
    const unsigned ca0 = wr + rg * 32, ca1 = wr + c1 * 32;
    f32x4 acc[2][7], cs0 = z4, cs1 = z4;
#pragma unroll
    for (int a = 0; a < 2; ++a)
#pragma unroll
      for (int b = 0; b < 7; ++b) acc[a][b] = z4;
    bf16x8 X[2][2], W[2][9];
#define RD0(buf, n, so_, hf) do { \
      if ((n) < 2) X[buf][(n) & 1] = ld_frag(((hf) ? xa1 : xa0) + (so_) + ((n) & 1) * 2048); \
      else { const unsigned a_ = ((n) < 8 ? wa + ((n) - 2) * 32 : (n) == 8 ? wa6 : (n) == 9 ? ca0 : ca1) + (so_) + (hf) * 7168; \
             W[buf][(n) < 2 ? 0 : (n) - 2] = tr_frag(a_, a_ + 3584); } } while (0)
#define MM0(buf, m) do { \
      if ((m) < 14) acc[(m) & 1][(m) >> 1] = mfma(W[buf][(m) >> 1], X[buf][(m) & 1], acc[(m) & 1][(m) >> 1]); \
      else if ((m) == 14) cs0 = mfma(ones, W[buf][7], cs0); else cs1 = mfma(ones, W[buf][8], cs1); } while (0)
    WAIT_BAR(12);
#pragma unroll
    for (int n = 0; n < 11; ++n) RD0(0, n, 0u, 0);
    unsigned so = 0;
#pragma unroll 1
    for (int t = 0; t < 7; ++t) {
#pragma unroll
      for (int m = 0; m < 16; ++m) { MM0(0, m); SB; if (m < 11) RD0(1, m, so, 1); SB; }
      WAIT_BAR(6);
      const unsigned sn = (so == 2 * SLOT) ? 0 : so + SLOT;
      if (t == 4) { rx.z = 0; rA.z = 0; rB.z = 0; }
#pragma unroll
      for (int m = 0; m < 16; ++m) { MM0(1, m); SB; if (m < 11) RD0(0, m, sn, 0); if (m >= 10) issue1(m - 10, t + 3, so); SB; }
      so = sn;
    }
    TS(1);
    if (g == 0) { *(LDS float*)(sb + L1_CS + (rg * 16 + i) * 4) = cs0[0]; *(LDS float*)(sb + L1_CS + (c1 * 16 + i) * 4) = cs1[0]; }
    WAIT_BAR(0);
    char* hb = ws + WS_H;
    const int kap0 = (7 * j + odd) >> 1, kaps = (7 * j + (odd ? 0 : 6)) >> 1;
    f32x4 cs[7];
#pragma unroll
    for (int bb = 0; bb < 7; ++bb) { int cb = bb < 6 ? bb + odd : (odd ? 0 : 6); cs[bb] = *(LDS const f32x4*)(sb + L1_CS + (cb * 16 + 4 * g) * 4); }
#pragma unroll
    for (int rb = 0; rb < 2; ++rb) {
      const f32x2 st = *(LDS const f32x2*)(sb + L1_ST + (rg * 32 + rb * 16 + i) * 8);
      unsigned pk[14];
#pragma unroll
      for (int bb = 0; bb < 7; ++bb) {
        float bv[4] = {bflo(bb1[bb].x), bfhi(bb1[bb].x), bflo(bb1[bb].y), bfhi(bb1[bb].y)};
        float hv[4];
#pragma unroll
        for (int e = 0; e < 4; ++e) {
          float t = fmaf(acc[rb][bb][e], st.x, fmaf(st.y, cs[bb][e], bv[e]));
          t = fmaxf(t, 0.f); hv[e] = t * t;
        }
        pk[2 * bb] = pk2(hv[0], hv[1]); pk[2 * bb + 1] = pk2(hv[2], hv[3]);
      }
      char* row = hb + (size_t)(R0 + rb) * 56 * 1024;
#pragma unroll
      for (int pp = 0; pp < 3; ++pp)
        *(u32x4*)(row + (kap0 + pp) * 1024 + lane * 16) = (u32x4){pk[4 * pp], pk[4 * pp + 1], pk[4 * pp + 2], pk[4 * pp + 3]};
      *(u32x2*)(row + kaps * 1024 + odd * 512 + lane * 8) = (u32x2){pk[12], pk[13]};
    }
    TS(2);
  } else {
    const unsigned wr = sb + OFF_WQ + (4 * g + q) * 192 + p * 8;
    const unsigned qa0 = wr + (g & 1) * 32, qa1 = wr + ((g & 1) ^ 1) * 32;
    const int c1 = rg + 4 < 6 ? rg + 4 : 5;
    const unsigned ca0 = wr + ((rg ^ (g & 1)) * 32), ca1 = wr + ((c1 ^ (g & 1)) * 32);
    const bool padq = i >= 12;
    f32x4 acc[2][6], cs0 = z4, cs1 = z4, sq0 = z4, sq1 = z4;
#pragma unroll
    for (int a = 0; a < 2; ++a)
#pragma unroll
      for (int b = 0; b < 6; ++b) acc[a][b] = z4;
    bf16x8 X[2][2], W[2][8];
#define RD1(buf, n, so_, hf) do { \
      if ((n) < 2) X[buf][(n) & 1] = ld_frag(((hf) ? xa1 : xa0) + (so_) + ((n) & 1) * 2048); \
      else { const unsigned a_ = ((n) < 8 ? (((n) & 1) ? qa1 : qa0) + (((n) - 2) >> 1) * 64 : (n) == 8 ? ca0 : ca1) + (so_) + (hf) * 6144; \
             W[buf][(n) < 2 ? 0 : (n) - 2] = tr_frag(a_, a_ + 3072); } } while (0)
#define MM1(buf, m) do { \
      if ((m) == 0) sq0 = mfma(X[buf][0], X[buf][0], sq0); else if ((m) == 1) sq1 = mfma(X[buf][1], X[buf][1], sq1); \
      else if ((m) < 6) { if ((m) == 4) W[buf][1] = padq ? ones : W[buf][1]; \
                          acc[(m) & 1][((m) - 2) >> 1] = mfma(W[buf][((m) - 2) >> 1], X[buf][(m) & 1], acc[(m) & 1][((m) - 2) >> 1]); } \
      else if ((m) < 14) acc[(m) & 1][((m) - 2) >> 1] = mfma(X[buf][(m) & 1], W[buf][((m) - 2) >> 1], acc[(m) & 1][((m) - 2) >> 1]); \
      else if ((m) == 14) cs0 = mfma(ones, W[buf][6], cs0); else cs1 = mfma(ones, W[buf][7], cs1); } while (0)
    WAIT_BAR(12);
#pragma unroll
    for (int n = 0; n < 10; ++n) RD1(0, n, 0u, 0);
    unsigned so = 0;
#pragma unroll 1
    for (int t = 0; t < 7; ++t) {
#pragma unroll
      for (int m = 0; m < 16; ++m) { MM1(0, m); SB; if (m < 10) RD1(1, m, so, 1); SB; }
      WAIT_BAR(6);
      const unsigned sn = (so == 2 * SLOT) ? 0 : so + SLOT;
      if (t == 4) { rx.z = 0; rA.z = 0; rB.z = 0; }
#pragma unroll
      for (int m = 0; m < 16; ++m) { MM1(1, m); SB; if (m < 10) RD1(0, m, sn, 0); if (m >= 10) issue1(m - 10, t + 3, so); SB; }
      so = sn;
    }
    TSW(4, 4);
    if (g == 0) { *(LDS float*)(sb + L1_CS + (112 + rg * 16 + i) * 4) = cs0[0]; *(LDS float*)(sb + L1_CS + (112 + c1 * 16 + i) * 4) = cs1[0]; }
#pragma unroll
    for (int rb = 0; rb < 2; ++rb) {
      const f32x4 sq = rb ? sq1 : sq0;
      float s01 = (p & 1) ? sq[1] : sq[0], s23 = (p & 1) ? sq[3] : sq[2];
      float ss = (p & 2) ? s23 : s01;
      float sm = __shfl(acc[rb][1][0], 48 + i);
      float m = sm * (1.f / D);
      float var = fmaxf(ss * (1.f / D) - m * m, 0.f);
      float den = rbf(__builtin_amdgcn_sqrtf(rbf(rbf(var) + 1e-5f)));
      float rs = __builtin_amdgcn_rcpf(den);
      if (g == q) *(LDS f32x2*)(sb + L1_ST + (rg * 32 + rb * 16 + i) * 8) = (f32x2){rs, -rbf(m) * rs};
    }
    WAIT_BAR(0);
    TSW(5, 4);
    {
      const f32x4 cq0 = *(LDS const f32x4*)(sb + L1_CS + (112 + 4 * g) * 4), cq1 = *(LDS const f32x4*)(sb + L1_CS + (128 + 4 * g) * 4);
#pragma unroll
      for (int rb = 0; rb < 2; ++rb) {
        const f32x2 st = *(LDS const f32x2*)(sb + L1_ST + (rg * 32 + rb * 16 + i) * 8);
        float v0[4], v1[4];
#pragma unroll
        for (int e = 0; e < 4; ++e) {
          v0[e] = phi(fmaf(acc[rb][0][e], st.x, st.y * cq0[e]));
          v1[e] = phi(fmaf(acc[rb][1][e], st.x, st.y * cq1[e]));
        }
        u32x4 o = {pk2(v0[0], v0[1]), pk2(v0[2], v0[3]), pk2(v1[0], v1[1]), pk2(v1[2], v1[3])};
        if (g == 3) { o.z = 0; o.w = 0; }
        *(u32x4*)(ws + WS_PQ + ((size_t)(R0 + rb) * 16 + j) * 1024 + lane * 16) = o;
      }
    }
    {
      float ck[4];
#pragma unroll
      for (int b = 0; b < 4; ++b) ck[b] = *(LDS const float*)(sb + L1_CS + (144 + b * 16 + i) * 4);
      u32x4 fr[4];
#pragma unroll
      for (int rb = 0; rb < 2; ++rb) {
        const f32x4 s01 = *(LDS const f32x4*)(sb + L1_ST + (rg * 32 + rb * 16 + 4 * g) * 8);
        const f32x4 s23 = *(LDS const f32x4*)(sb + L1_ST + (rg * 32 + rb * 16 + 4 * g) * 8 + 16);
        const float rs[4] = {s01[0], s01[2], s23[0], s23[2]}, mr[4] = {s01[1], s01[3], s23[1], s23[3]};
#pragma unroll
        for (int b = 0; b < 4; ++b) {
          float v[4];
#pragma unroll
          for (int e = 0; e < 4; ++e) {
            float t = fmaf(acc[rb][2 + b][e], rs[e], mr[e] * ck[b]);
            v[e] = b < 2 ? phi(t) : t;
          }
          fr[b][2 * rb] = pk2(v[0], v[1]); fr[b][2 * rb + 1] = pk2(v[2], v[3]);
        }
      }
      if (i == 12) fr[3] = (u32x4){0x3f803f80u, 0x3f803f80u, 0x3f803f80u, 0x3f803f80u};
#pragma unroll
      for (int be = 0; be < 2; ++be)
#pragma unroll
        for (int bd = 0; bd < 2; ++bd) {
          f32x4 kv = mfma(__builtin_bit_cast(bf16x8, fr[bd]), __builtin_bit_cast(bf16x8, fr[2 + be]), z4);
          *(LDS f32x4*)(sb + rg * 4096 + be * 2048 + lane * 32 + bd * 16) = kv;
        }
      TSW(6, 4);
      WAIT_BAR(63);
      const int t = tid - 256;
      f32x4 s = *(LDS const f32x4*)(sb + t * 16);
#pragma unroll
      for (int a = 1; a < 4; ++a) s += *(LDS const f32x4*)(sb + a * 4096 + t * 16);
      *(f32x4*)(ws + WS_KVP + ((size_t)(j * 2 + (t >> 7)) * 16 + r) * 2048 + (t & 127) * 16) = s;
    }
    TSW(3, 4);
  }
}

constexpr unsigned L2_KV = 131072;
#define LD4(dst, voff, rsrc, soff, imm) asm volatile("buffer_load_dwordx4 %0, %1, %2, %3 offen offset:" #imm : "=v"(dst) : "v"(voff), "s"(rsrc), "s"(soff) : "memory")
#define LD2(dst, voff, rsrc, soff, imm) asm volatile("buffer_load_dwordx2 %0, %1, %2, %3 offen offset:" #imm : "=v"(dst) : "v"(voff), "s"(rsrc), "s"(soff) : "memory")

__global__ __launch_bounds__(512, 2) void k2(const u16* __restrict__ x, const u16* __restrict__ wo, const u16* __restrict__ w2,
                                             const u16* __restrict__ b2, u16* __restrict__ out, char* __restrict__ ws) {
  extern __shared__ __attribute__((aligned(16))) char smem[];
  const unsigned sb = (unsigned)(size_t)(LDS char*)smem;
  const int bid = blockIdx.x, tid = threadIdx.x, lane = tid & 63;
  const int w = __builtin_amdgcn_readfirstlane(tid >> 6);
  const int xc = bid & 7, ix = bid >> 3;
  TS(8);
  if (ix < 4) {
    const int id = xc * 4 + ix, t = tid & 127, gq = tid >> 7;
    const char* src = ws + WS_KVP + ((size_t)id * 16 + gq * 4) * 2048 + t * 16;
    f32x4 s = *(const f32x4*)src;
#pragma unroll
    for (int a = 1; a < 4; ++a) s += *(const f32x4*)(src + a * 2048);
    *(LDS f32x4*)(sb + gq * 2048 + t * 16) = s;
    __syncthreads();
    if (tid < 64) {
      f32x4 a = {0.f, 0.f, 0.f, 0.f}, b = a;
#pragma unroll
      for (int e = 0; e < 4; ++e) { a += *(LDS const f32x4*)(sb + e * 2048 + tid * 32); b += *(LDS const f32x4*)(sb + e * 2048 + tid * 32 + 16); }
      u32x4 o = {pk2(a[0], a[1]), pk2(a[2], a[3]), pk2(b[0], b[1]), pk2(b[2], b[3])};
      if (tid >= 48) { o.z = 0; o.w = 0; }
      const u32x4 rk = mk_rsrc(ws + WS_KV, 32768);
      asm volatile("buffer_store_dwordx4 %0, %1, %2, 0 offen sc1\n\ts_waitcnt vmcnt(0)" :: "v"(o), "v"(id * 1024 + tid * 16), "s"(rk) : "memory");
      if (tid == 0) __hip_atomic_store((unsigned*)(ws + WS_FLAG + id * 128), 1u, __ATOMIC_RELAXED, __HIP_MEMORY_SCOPE_AGENT);
    }
    return;
  }
  const int rt = xc * 4 + (ix - 4) / 7, ct = (ix - 4) % 7;
  const int i = lane & 15, g = lane >> 4, q = i >> 2, p = i & 3;
  const int rb = w & 3, kh = w >> 2;
  const int R = rt * 4 + rb;
  const u32x4 rw2 = mk_rsrc(w2, F * D * 2), rwo = mk_rsrc(wo, D * D * 2), rws = mk_rsrc(ws, (unsigned)WS_END);
  const unsigned swz = (unsigned)(((lane & 7) ^ (2 * ((lane >> 4) & 3))) << 4);
  const unsigned vw2 = (unsigned)(kh * 896 + rb * 8 + (lane >> 3)) * 896u + ct * 128 + swz;
  const unsigned vwo = (unsigned)(kh * 224 + rb * 8 + (lane >> 3)) * 896u + ct * 128 + swz;
  const unsigned v16 = lane * 16, v8 = lane * 8;
  const unsigned hs = (unsigned)WS_H + (unsigned)(R * 56 + kh * 28) * 1024u;
  u32x4 hf[28], pq[8], kvr[4];
  u32x2 hl[4], hh[4];
  unsigned flag;
#define HL(u) do { if ((u) % 7 == 3) { LD2(hl[(u) / 7], v8, rws, hs + (u) * 1024, 0); LD2(hh[(u) / 7], v8, rws, hs + (u) * 1024, 512); } \
                   else LD4(hf[u], v16, rws, hs + (u) * 1024, 0); } while (0)
#define GRP(bt, sl) do { _Pragma("unroll") for (int v = 0; v < 4; ++v) { \
      dma16(sb + (sl) * 32768 + v * 8192 + w * 1024, vw2, rw2, (unsigned)((bt) * 4 + v) * 28672u); HL((bt) * 4 + v); } } while (0)
#define GRPO(z, sl) do { _Pragma("unroll") for (int v = 0; v < 4; ++v) \
      dma16(sb + (sl) * 32768 + kh * 16384 + (v * 4 + rb) * 1024, vwo, rwo, (unsigned)((z) * 112 + v * 32) * 896u); } while (0)
  GRP(0, 0); GRP(1, 1); GRP(2, 2); GRP(3, 3);
  unsigned ra[2][4];
  {
    const int sw = ((4 * g + q) >> 1) & 3;
#pragma unroll
    for (int cb = 0; cb < 4; ++cb) {
      ra[0][cb] = sb + kh * 4096 + (4 * g + q) * 128 + ((cb ^ sw) << 5) + p * 8; ra[1][cb] = ra[0][cb] + 65536;
      asm volatile("" : "+v"(ra[0][cb]), "+v"(ra[1][cb]));
    }
  }
  const f32x4 z4 = {0.f, 0.f, 0.f, 0.f};
  f32x4 af[4] = {z4, z4, z4, z4}, as[4] = {z4, z4, z4, z4};
  bf16x8 W[2][4];
#define PIN4(a) asm volatile("" : "+v"(a))
#define PINH(u) do { if ((u) % 7 == 3) { PIN4(hl[(u) / 7]); PIN4(hh[(u) / 7]); hf[u] = (u32x4){hl[(u) / 7].x, hl[(u) / 7].y, hh[(u) / 7].x, hh[(u) / 7].y}; } else PIN4(hf[u]); } while (0)
#define RDW(buf, cb, sl, v) do { const unsigned a_ = ra[(sl) >> 1][cb] + ((sl) & 1) * 32768 + (v) * 8192; W[buf][cb] = tr_frag(a_, a_ + 2048); } while (0)
#define BATCH(bt, sl) do { PINH((bt) * 4); PINH((bt) * 4 + 1); PINH((bt) * 4 + 2); PINH((bt) * 4 + 3); \
      _Pragma("unroll") for (int cb = 0; cb < 4; ++cb) RDW(0, cb, sl, 0); SB; \
      _Pragma("unroll") for (int v = 0; v < 4; ++v) _Pragma("unroll") for (int cb = 0; cb < 4; ++cb) { \
        if (v < 3) RDW((v + 1) & 1, cb, sl, v + 1); SB; \
        af[cb] = mfma(W[v & 1][cb], __builtin_bit_cast(bf16x8, hf[(bt) * 4 + v]), af[cb]); SB; } } while (0)
  TS(14); WAIT_BAR(25); TS(15); BATCH(0, 0);
  WAIT_BAR(17); GRP(4, 0);
  asm volatile("buffer_load_dword %0, %1, %2, %3 offen sc1" : "=v"(flag) : "v"((lane & 3) * 128), "s"(rws), "s"((unsigned)WS_FLAG + w * 512) : "memory");
  {
    const unsigned ps = (unsigned)WS_PQ + (unsigned)(R * 16 + kh * 8) * 1024u, ps2 = ps + 4096;
    LD4(pq[0], v16, rws, ps, 0); LD4(pq[1], v16, rws, ps, 1024); LD4(pq[2], v16, rws, ps, 2048); LD4(pq[3], v16, rws, ps, 3072);
    LD4(pq[4], v16, rws, ps2, 0); LD4(pq[5], v16, rws, ps2, 1024); LD4(pq[6], v16, rws, ps2, 2048); LD4(pq[7], v16, rws, ps2, 3072);
  }
  BATCH(1, 1);
  WAIT_BAR(26); GRPO(0, 1);
  BATCH(2, 2);
  WAIT_BAR(22); GRPO(1, 2);
  BATCH(3, 3);
  WAIT_BAR(16);
  PIN4(flag);
  if (!__all(flag == 1u)) {
    unsigned spins = 0;
    do {
      __builtin_amdgcn_s_sleep(2);
      flag = __hip_atomic_load((unsigned*)(ws + WS_FLAG + w * 512 + (lane & 3) * 128), __ATOMIC_RELAXED, __HIP_MEMORY_SCOPE_AGENT);
    } while (!__all(flag == 1u) && ++spins < (1u << 20));
  }
  {
    const unsigned ks = (unsigned)WS_KV + w * 4096;
    asm volatile("buffer_load_dwordx4 %0, %4, %5, %6 offen sc1\n\tbuffer_load_dwordx4 %1, %4, %5, %6 offen offset:1024 sc1\n\t"
                 "buffer_load_dwordx4 %2, %4, %5, %6 offen offset:2048 sc1\n\tbuffer_load_dwordx4 %3, %4, %5, %6 offen offset:3072 sc1"
                 : "=&v"(kvr[0]), "=&v"(kvr[1]), "=&v"(kvr[2]), "=&v"(kvr[3]) : "v"(v16), "s"(rws), "s"(ks) : "memory");
  }
  GRP(5, 3);
  BATCH(4, 0);
  asm volatile("s_waitcnt vmcnt(8)" ::: "memory");
#pragma unroll
  for (int a = 0; a < 4; ++a) { PIN4(kvr[a]); *(LDS u32x4*)(sb + L2_KV + w * 4096 + a * 1024 + lane * 16) = kvr[a]; }
  WAIT_BAR(8);
  GRP(6, 0);
  u32x2 xr[2], br[2];
  {
    const u32x4 rx = mk_rsrc(x, S * D * 2), rb2 = mk_rsrc(b2, D * 2);
    const unsigned co = ct * 128 + kh * 64 + g * 8;
    const unsigned xo = (unsigned)(R * 16 + i) * 896u + co;
    LD2(xr[0], xo, rx, 0u, 0); LD2(xr[1], xo, rx, 0u, 32); LD2(br[0], co, rb2, 0u, 0); LD2(br[1], co, rb2, 0u, 32);
  }
  TS(12);
  {
#pragma unroll
    for (int a = 0; a < 8; ++a) PIN4(pq[a]);
    const unsigned ka = sb + L2_KV + kh * 16384 + lane * 16;
    f32x4 n0[8], n1[8];
#pragma unroll
    for (int a = 0; a < 8; ++a) {
      const bf16x8 pf = __builtin_bit_cast(bf16x8, pq[a]);
      n1[a] = mfma(ld_frag(ka + a * 2048 + 1024), pf, z4); n0[a] = mfma(ld_frag(ka + a * 2048), pf, z4);
    }
    SB;
    float rd[8];
#pragma unroll
    for (int a = 0; a < 8; ++a) rd[a] = __shfl(n1[a][0], 48 + i);
    SB;
    const unsigned km = g == 3 ? 0u : ~0u;
#pragma unroll
    for (int a = 0; a < 8; ++a) {
      const float r_ = __builtin_amdgcn_rcpf(rd[a] + 1e-6f);
      pq[a] = (u32x4){pk2(n0[a][0] * r_, n0[a][1] * r_), pk2(n0[a][2] * r_, n0[a][3] * r_), pk2(n1[a][0] * r_, n1[a][1] * r_) & km, pk2(n1[a][2] * r_, n1[a][3] * r_) & km};
    }
    unsigned rq[4];
#pragma unroll
    for (int cb = 0; cb < 4; ++cb) { rq[cb] = ra[0][cb] + kh * 12288 + 32768; PIN4(rq[cb]); }
#define RDO(buf, cb, a) do { const unsigned a_ = rq[((a) & 1) ? (cb) ^ 2 : (cb)] + ((a) >> 2) * 32768 + ((a) & 3) * 3584; W[buf][cb] = tr_frag(a_, a_ + 2048); } while (0)
#pragma unroll
    for (int cb = 0; cb < 4; ++cb) RDO(0, cb, 0);
    SB;
#pragma unroll
    for (int a = 0; a < 8; ++a)
#pragma unroll
      for (int cb = 0; cb < 4; ++cb) {
        if (a < 7) RDO((a + 1) & 1, cb, a + 1);
        SB;
        as[cb] = mfma(W[a & 1][cb], __builtin_bit_cast(bf16x8, pq[a]), as[cb]); SB;
      }
  }
  TS(13);
  WAIT_BAR(13);
  BATCH(5, 3);
  WAIT_BAR(0);
  TS(10);
  BATCH(6, 0);
  {
    const unsigned ex = sb + 32768 + w * 4096 + lane * 16;
    *(LDS f32x4*)(ex) = kh ? af[0] : af[2]; *(LDS f32x4*)(ex + 1024) = kh ? af[1] : af[3];
    *(LDS f32x4*)(ex + 2048) = kh ? as[0] : as[2]; *(LDS f32x4*)(ex + 3072) = kh ? as[1] : as[3];
    WAIT_BAR(0);
    const unsigned ey = sb + 32768 + (w ^ 4) * 4096 + lane * 16;
    PIN4(xr[0]); PIN4(xr[1]); PIN4(br[0]); PIN4(br[1]);
#pragma unroll
    for (int c = 0; c < 2; ++c) {
      f32x4 f = (kh ? af[2 + c] : af[c]) + *(LDS const f32x4*)(ey + c * 1024);
      f32x4 s = (kh ? as[2 + c] : as[c]) + *(LDS const f32x4*)(ey + 2048 + c * 1024);
      const float xv[4] = {bflo(xr[c].x), bfhi(xr[c].x), bflo(xr[c].y), bfhi(xr[c].y)};
      const float bv[4] = {bflo(br[c].x), bfhi(br[c].x), bflo(br[c].y), bfhi(br[c].y)};
      float o[4];
#pragma unroll
      for (int e = 0; e < 4; ++e) {
        float t = f[e] + bv[e];
        float gt = t * __builtin_amdgcn_rcpf(1.f + __expf(-t));
        o[e] = fmaf(s[e], gt, xv[e]);
      }
      *(u32x2*)((char*)out + (size_t)(R * 16 + i) * 896 + ct * 128 + kh * 64 + c * 32 + g * 8) = (u32x2){pk2(o[0], o[1]), pk2(o[2], o[3])};
    }
  }
  TS(11);
}

__global__ void k_empty(char* ws) {}
__global__ void k_spin(char* ws, int ticks) { unsigned long long t0 = __builtin_amdgcn_s_memrealtime(); while (__builtin_amdgcn_s_memrealtime() - t0 < (unsigned long long)ticks) __builtin_amdgcn_s_sleep(4); }
__global__ void k_touch(const char* a, int na, const char* b, int nb, const char* c, int nc, const char* d, int nd, const char* e, int ne, char* ws) {
  int t = blockIdx.x * blockDim.x + threadIdx.x; unsigned acc = 0;
  if (t * 128 < na) acc += *(const unsigned*)(a + t * 128);
  if (t * 128 < nb) acc += *(const unsigned*)(b + t * 128);
  if (t * 128 < nc) acc += *(const unsigned*)(c + t * 128);
  if (t * 128 < nd) acc += *(const unsigned*)(d + t * 128);
  if (t * 128 < ne) acc += *(const unsigned*)(e + t * 128);
  if (acc == 0x12345678u) *(unsigned*)(ws + (100u << 20)) = acc;
}
extern "C" void kernel_launch(void* const* d_in, const int* in_sizes, int n_in, void* d_out, int out_size, void* d_ws,
                              size_t ws_size, hipStream_t stream) {
  const u16* x = (const u16*)d_in[0]; const u16* wqkv = (const u16*)d_in[1]; const u16* wo = (const u16*)d_in[2];
  const u16* w1 = (const u16*)d_in[4]; const u16* b1 = (const u16*)d_in[5];
  const u16* w2 = (const u16*)d_in[6]; const u16* b2 = (const u16*)d_in[7];
  (void)hipFuncSetAttribute((const void*)k1, hipFuncAttributeMaxDynamicSharedMemorySize, L1_END);
  (void)hipFuncSetAttribute((const void*)k2, hipFuncAttributeMaxDynamicSharedMemorySize, 163840);
  k_touch<<<256,64,0,stream>>>((const char*)x,S*D*2,(const char*)wqkv,D*3*D*2,(const char*)wo,D*D*2,(const char*)w1,D*F*2,(const char*)w2,D*F*2,(char*)d_ws);
  k1<<<256, 512, L1_END, stream>>>(x, wqkv, w1, b1, (char*)d_ws);
  k2<<<256, 512, 163840, stream>>>(x, wo, w2, b2, (u16*)d_out, (char*)d_ws);
}
```

```cpp
#include <hip/hip_runtime.h>
#include <hip/hip_bf16.h>
#include <stdint.h>
#pragma clang diagnostic ignored "-Wint-to-pointer-cast"

typedef unsigned short u16;
typedef __bf16 bf16x8 __attribute__((ext_vector_type(8)));
typedef __bf16 bf16x2 __attribute__((ext_vector_type(2)));
typedef short s16x4 __attribute__((ext_vector_type(4)));
typedef float f32x4 __attribute__((ext_vector_type(4)));
typedef float f32x2 __attribute__((ext_vector_type(2)));
typedef unsigned u32x4 __attribute__((ext_vector_type(4)));
typedef unsigned u32x2 __attribute__((ext_vector_type(2)));
#define LDS __attribute__((address_space(3)))
#define DI __device__ __forceinline__

constexpr int S = 2048, D = 448, F = 1792;
constexpr size_t WS_H = 0;
constexpr size_t WS_PQ = WS_H + 128 * 56 * 1024;
constexpr size_t WS_KVP = WS_PQ + 128 * 16 * 1024;
constexpr size_t WS_KV = WS_KVP + 32 * 16 * 2048;
constexpr size_t WS_FLAG = WS_KV + 32 * 1024;
constexpr size_t WS_END = WS_FLAG + 32 * 128;

#define TS(n)
#define TSW(n, wv)

DI u32x4 mk_rsrc(const void* p, unsigned bytes) {
  uint64_t a = (uint64_t)p;
  u32x4 r; r.x = (unsigned)a; r.y = (unsigned)(a >> 32) & 0xffffu; r.z = bytes; r.w = 0x00020000u;
  return r;
}
DI void dma16(unsigned lds_addr, unsigned voff, u32x4 rsrc, unsigned soff) {
  asm volatile("s_mov_b32 m0, %0\n\ts_nop 0\n\tbuffer_load_dwordx4 %1, %2, %3 offen lds"
               :: "s"(lds_addr), "v"(voff), "s"(rsrc), "s"(soff) : "memory");
}
#define SB __builtin_amdgcn_sched_barrier(0)
#define WAIT_BAR(n) asm volatile("s_waitcnt vmcnt(" #n ") lgkmcnt(0)\n\ts_barrier" ::: "memory")
DI f32x4 mfma(bf16x8 a, bf16x8 b, f32x4 c) { return __builtin_amdgcn_mfma_f32_16x16x32_bf16(a, b, c, 0, 0, 0); }
DI bf16x8 ld_frag(unsigned a) { return *(LDS const bf16x8*)a; }
DI bf16x8 tr_frag(unsigned a0, unsigned a1) {
  s16x4 lo = __builtin_amdgcn_ds_read_tr16_b64_v4i16((LDS s16x4*)a0);
  s16x4 hi = __builtin_amdgcn_ds_read_tr16_b64_v4i16((LDS s16x4*)a1);
  return __builtin_bit_cast(bf16x8, __builtin_shufflevector(lo, hi, 0, 1, 2, 3, 4, 5, 6, 7));
}
DI unsigned pk2(float a, float b) { f32x2 v = {a, b}; return __builtin_bit_cast(unsigned, __builtin_convertvector(v, bf16x2)); }
DI float bflo(unsigned u) { return __uint_as_float(u << 16); }
DI float bfhi(unsigned u) { return __uint_as_float(u & 0xffff0000u); }
DI float rbf(float f) { return bflo(pk2(f, 0.f)); }
DI float phi(float t) { return (t > 0.f ? t + 1.f : __expf(t)) + 1e-6f; }

constexpr unsigned SLOT = 43008, OFF_W1 = 16384, OFF_WQ = 30720;
constexpr unsigned L1_DUMMY = 3 * SLOT, L1_ST = L1_DUMMY + 1024, L1_CS = L1_ST + 1024, L1_END = L1_CS + 1024;

__global__ __launch_bounds__(512, 2) void k1(const u16* __restrict__ x, const u16* __restrict__ wqkv,
                                             const u16* __restrict__ w1, const u16* __restrict__ b1, char* __restrict__ ws) {
  extern __shared__ __attribute__((aligned(16))) char smem[];
  const unsigned sb = (unsigned)(size_t)(LDS char*)smem;
  const int bid = blockIdx.x, tid = threadIdx.x, lane = tid & 63;
  const int w = __builtin_amdgcn_readfirstlane(tid >> 6);
  const int xc = bid & 7, ix = bid >> 3;
  const int r = (xc >> 1) * 4 + (ix >> 3), j = (xc & 1) * 8 + (ix & 7);
  const int i = lane & 15, g = lane >> 4, q = i >> 2, p = i & 3;
  const int rg = w & 3, role = w >> 2, odd = j & 1;
  TS(0);
  u32x4 rx = mk_rsrc(x, S * D * 2);
  const unsigned vx = (unsigned)(r * 128 + 16 * w + (lane >> 3)) * 896u + (unsigned)(((lane & 7) ^ (lane >> 3)) << 4);
  unsigned vA, vB;
  {
    int n = w * 64 + lane, rho = n / 14, c = n - rho * 14;
    int k = 8 * ((rho >> 2) & 3) + 4 * (rho >> 4) + (rho & 3);
    unsigned a1 = k * 3584 + j * 224 + c * 16;
    int pq_ = (w < 5) ? w : 5;
    n = pq_ * 64 + lane; rho = n / 12; c = n - rho * 12;
    k = 8 * ((rho >> 2) & 3) + 4 * (rho >> 4) + (rho & 3);
    int u = (c >> 1) ^ ((rho >> 2) & 1);
    unsigned a2 = k * 2688 + (u >> 1) * 896 + j * 56 + (u & 1) * 32 + (c & 1) * 16;
    vA = (w < 7) ? a1 : a2; vB = a2;
  }
  u32x4 rA = (w < 7) ? mk_rsrc(w1, D * F * 2) : mk_rsrc(wqkv, D * 3 * D * 2);
  u32x4 rB = mk_rsrc(wqkv, (w < 5) ? D * 3 * D * 2 : 0);
  const unsigned sA = (w < 7) ? 114688u : 86016u;
  const unsigned lA = (w < 7) ? OFF_W1 + w * 1024 : OFF_WQ + 5 * 1024, lAh = (w < 7) ? 7168u : 6144u;
  const unsigned lB = (w < 5) ? OFF_WQ + w * 1024 : L1_DUMMY, lBh = (w < 5) ? 6144u : 0u, lBm = (w < 5) ? ~0u : 0u;
  auto issue1 = [&](int n, int t, unsigned slot) {
    const unsigned hf = n >= 3;
    if (n % 3 == 0) dma16(sb + slot + w * 2048 + hf * 1024, vx, rx, t * 128 + hf * 7168);
    else if (n % 3 == 1) dma16(sb + slot + lA + hf * lAh, vA, rA, (2 * t + hf) * sA);
    else dma16(sb + (slot & lBm) + lB + hf * lBh, vB, rB, (2 * t + hf) * 86016u);
  };
  auto issue = [&](int t, unsigned slot) {
#pragma unroll
    for (int n = 0; n < 6; ++n) issue1(n, t, slot);
  };
  u32x2 bb1[7];
#pragma unroll
  for (int bb = 0; bb < 7; ++bb) { int cb = bb < 6 ? bb + odd : (odd ? 0 : 6); bb1[bb] = *(const u32x2*)(b1 + j * 112 + cb * 16 + 4 * g); }
  if (bid < 32 && tid == 0) *(unsigned*)(ws + WS_FLAG + bid * 128) = 0u;
  TS(7);
  issue(0, 0); issue(1, SLOT); issue(2, 2 * SLOT);
  TS(9);

  const unsigned xa0 = sb + (rg * 4 + (i >> 3)) * 1024 + (i & 7) * 128 + ((g ^ (i & 7)) << 4), xa1 = xa0 ^ 64u;
  bf16x8 ones = __builtin_bit_cast(bf16x8, (u32x4){0x3f803f80u, 0x3f803f80u, 0x3f803f80u, 0x3f803f80u});
  asm volatile("" : "+v"(ones));
  const f32x4 z4 = {0.f, 0.f, 0.f, 0.f};
  const int R0 = r * 8 + rg * 2;

  if (role == 0) {
    const unsigned wr = sb + OFF_W1 + (4 * g + q) * 224 + p * 8;
    const unsigned wa = wr + odd * 32, wa6 = wr + (odd ? 0 : 192);
    const int c1 = rg + 4 < 7 ? rg + 4 : 6;
    const unsigned ca0 = wr + rg * 32, ca1 = wr + c1 * 32;
    f32x4 acc[2][7], cs0 = z4, cs1 = z4;
#pragma unroll
    for (int a = 0; a < 2; ++a)
#pragma unroll
      for (int b = 0; b < 7; ++b) acc[a][b] = z4;
    bf16x8 X[2][2], W[2][9];
#define RD0(buf, n, so_, hf) do { \
      if ((n) < 2) X[buf][(n) & 1] = ld_frag(((hf) ? xa1 : xa0) + (so_) + ((n) & 1) * 2048); \
      else { const unsigned a_ = ((n) < 8 ? wa + ((n) - 2) * 32 : (n) == 8 ? wa6 : (n) == 9 ? ca0 : ca1) + (so_) + (hf) * 7168; \
             W[buf][(n) < 2 ? 0 : (n) - 2] = tr_frag(a_, a_ + 3584); } } while (0)
#define MM0(buf, m) do { \
      if ((m) < 14) acc[(m) & 1][(m) >> 1] = mfma(W[buf][(m) >> 1], X[buf][(m) & 1], acc[(m) & 1][(m) >> 1]); \
      else if ((m) == 14) cs0 = mfma(ones, W[buf][7], cs0); else cs1 = mfma(ones, W[buf][8], cs1); } while (0)
    WAIT_BAR(12);
#pragma unroll
    for (int n = 0; n < 11; ++n) RD0(0, n, 0u, 0);
    unsigned so = 0;
#pragma unroll 1
    for (int t = 0; t < 7; ++t) {
#pragma unroll
      for (int m = 0; m < 16; ++m) { MM0(0, m); SB; if (m < 11) RD0(1, m, so, 1); SB; }
      WAIT_BAR(6);
      const unsigned sn = (so == 2 * SLOT) ? 0 : so + SLOT;
      if (t == 4) { rx.z = 0; rA.z = 0; rB.z = 0; }
#pragma unroll
      for (int m = 0; m < 16; ++m) { MM0(1, m); SB; if (m < 11) RD0(0, m, sn, 0); if (m >= 10) issue1(m - 10, t + 3, so); SB; }
      so = sn;
    }
    TS(1);
    if (g == 0) { *(LDS float*)(sb + L1_CS + (rg * 16 + i) * 4) = cs0[0]; *(LDS float*)(sb + L1_CS + (c1 * 16 + i) * 4) = cs1[0]; }
    WAIT_BAR(0);
    char* hb = ws + WS_H;
    const int kap0 = (7 * j + odd) >> 1, kaps = (7 * j + (odd ? 0 : 6)) >> 1;
    f32x4 cs[7];
#pragma unroll
    for (int bb = 0; bb < 7; ++bb) { int cb = bb < 6 ? bb + odd : (odd ? 0 : 6); cs[bb] = *(LDS const f32x4*)(sb + L1_CS + (cb * 16 + 4 * g) * 4); }
#pragma unroll
    for (int rb = 0; rb < 2; ++rb) {
      const f32x2 st = *(LDS const f32x2*)(sb + L1_ST + (rg * 32 + rb * 16 + i) * 8);
      unsigned pk[14];
#pragma unroll
      for (int bb = 0; bb < 7; ++bb) {
        float bv[4] = {bflo(bb1[bb].x), bfhi(bb1[bb].x), bflo(bb1[bb].y), bfhi(bb1[bb].y)};
        float hv[4];
#pragma unroll
        for (int e = 0; e < 4; ++e) {
          float t = fmaf(acc[rb][bb][e], st.x, fmaf(st.y, cs[bb][e], bv[e]));
          t = fmaxf(t, 0.f); hv[e] = t * t;
        }
        pk[2 * bb] = pk2(hv[0], hv[1]); pk[2 * bb + 1] = pk2(hv[2], hv[3]);
      }
      char* row = hb + (size_t)(R0 + rb) * 56 * 1024;
#pragma unroll
      for (int pp = 0; pp < 3; ++pp)
        *(u32x4*)(row + (kap0 + pp) * 1024 + lane * 16) = (u32x4){pk[4 * pp], pk[4 * pp + 1], pk[4 * pp + 2], pk[4 * pp + 3]};
      *(u32x2*)(row + kaps * 1024 + odd * 512 + lane * 8) = (u32x2){pk[12], pk[13]};
    }
    TS(2);
  } else {
    const unsigned wr = sb + OFF_WQ + (4 * g + q) * 192 + p * 8;
    const unsigned qa0 = wr + (g & 1) * 32, qa1 = wr + ((g & 1) ^ 1) * 32;
    const int c1 = rg + 4 < 6 ? rg + 4 : 5;
    const unsigned ca0 = wr + ((rg ^ (g & 1)) * 32), ca1 = wr + ((c1 ^ (g & 1)) * 32);
    const bool padq = i >= 12;
    f32x4 acc[2][6], cs0 = z4, cs1 = z4, sq0 = z4, sq1 = z4;
#pragma unroll
    for (int a = 0; a < 2; ++a)
#pragma unroll
      for (int b = 0; b < 6; ++b) acc[a][b] = z4;
    bf16x8 X[2][2], W[2][8];
#define RD1(buf, n, so_, hf) do { \
      if ((n) < 2) X[buf][(n) & 1] = ld_frag(((hf) ? xa1 : xa0) + (so_) + ((n) & 1) * 2048); \
      else { const unsigned a_ = ((n) < 8 ? (((n) & 1) ? qa1 : qa0) + (((n) - 2) >> 1) * 64 : (n) == 8 ? ca0 : ca1) + (so_) + (hf) * 6144; \
             W[buf][(n) < 2 ? 0 : (n) - 2] = tr_frag(a_, a_ + 3072); } } while (0)
#define MM1(buf, m) do { \
      if ((m) == 0) sq0 = mfma(X[buf][0], X[buf][0], sq0); else if ((m) == 1) sq1 = mfma(X[buf][1], X[buf][1], sq1); \
      else if ((m) < 6) { if ((m) == 4) W[buf][1] = padq ? ones : W[buf][1]; \
                          acc[(m) & 1][((m) - 2) >> 1] = mfma(W[buf][((m) - 2) >> 1], X[buf][(m) & 1], acc[(m) & 1][((m) - 2) >> 1]); } \
      else if ((m) < 14) acc[(m) & 1][((m) - 2) >> 1] = mfma(X[buf][(m) & 1], W[buf][((m) - 2) >> 1], acc[(m) & 1][((m) - 2) >> 1]); \
      else if ((m) == 14) cs0 = mfma(ones, W[buf][6], cs0); else cs1 = mfma(ones, W[buf][7], cs1); } while (0)
    WAIT_BAR(12);
#pragma unroll
    for (int n = 0; n < 10; ++n) RD1(0, n, 0u, 0);
    unsigned so = 0;
#pragma unroll 1
    for (int t = 0; t < 7; ++t) {
#pragma unroll
      for (int m = 0; m < 16; ++m) { MM1(0, m); SB; if (m < 10) RD1(1, m, so, 1); SB; }
      WAIT_BAR(6);
      const unsigned sn = (so == 2 * SLOT) ? 0 : so + SLOT;
      if (t == 4) { rx.z = 0; rA.z = 0; rB.z = 0; }
#pragma unroll
      for (int m = 0; m < 16; ++m) { MM1(1, m); SB; if (m < 10) RD1(0, m, sn, 0); if (m >= 10) issue1(m - 10, t + 3, so); SB; }
      so = sn;
    }
    TSW(4, 4);
    if (g == 0) { *(LDS float*)(sb + L1_CS + (112 + rg * 16 + i) * 4) = cs0[0]; *(LDS float*)(sb + L1_CS + (112 + c1 * 16 + i) * 4) = cs1[0]; }
#pragma unroll
    for (int rb = 0; rb < 2; ++rb) {
      const f32x4 sq = rb ? sq1 : sq0;
      float s01 = (p & 1) ? sq[1] : sq[0], s23 = (p & 1) ? sq[3] : sq[2];
      float ss = (p & 2) ? s23 : s01;
      float sm = __shfl(acc[rb][1][0], 48 + i);
      float m = sm * (1.f / D);
      float var = fmaxf(ss * (1.f / D) - m * m, 0.f);
      float den = rbf(__builtin_amdgcn_sqrtf(rbf(rbf(var) + 1e-5f)));
      float rs = __builtin_amdgcn_rcpf(den);
      if (g == q) *(LDS f32x2*)(sb + L1_ST + (rg * 32 + rb * 16 + i) * 8) = (f32x2){rs, -rbf(m) * rs};
    }
    WAIT_BAR(0);
    TSW(5, 4);
    {
      const f32x4 cq0 = *(LDS const f32x4*)(sb + L1_CS + (112 + 4 * g) * 4), cq1 = *(LDS const f32x4*)(sb + L1_CS + (128 + 4 * g) * 4);
#pragma unroll
      for (int rb = 0; rb < 2; ++rb) {
        const f32x2 st = *(LDS const f32x2*)(sb + L1_ST + (rg * 32 + rb * 16 + i) * 8);
        float v0[4], v1[4];
#pragma unroll
        for (int e = 0; e < 4; ++e) {
          v0[e] = phi(fmaf(acc[rb][0][e], st.x, st.y * cq0[e]));
          v1[e] = phi(fmaf(acc[rb][1][e], st.x, st.y * cq1[e]));
        }
        u32x4 o = {pk2(v0[0], v0[1]), pk2(v0[2], v0[3]), pk2(v1[0], v1[1]), pk2(v1[2], v1[3])};
        if (g == 3) { o.z = 0; o.w = 0; }
        *(u32x4*)(ws + WS_PQ + ((size_t)(R0 + rb) * 16 + j) * 1024 + lane * 16) = o;
      }
    }
    {
      float ck[4];
#pragma unroll
      for (int b = 0; b < 4; ++b) ck[b] = *(LDS const float*)(sb + L1_CS + (144 + b * 16 + i) * 4);
      u32x4 fr[4];
#pragma unroll
      for (int rb = 0; rb < 2; ++rb) {
        const f32x4 s01 = *(LDS const f32x4*)(sb + L1_ST + (rg * 32 + rb * 16 + 4 * g) * 8);
        const f32x4 s23 = *(LDS const f32x4*)(sb + L1_ST + (rg * 32 + rb * 16 + 4 * g) * 8 + 16);
        const float rs[4] = {s01[0], s01[2], s23[0], s23[2]}, mr[4] = {s01[1], s01[3], s23[1], s23[3]};
#pragma unroll
        for (int b = 0; b < 4; ++b) {
          float v[4];
#pragma unroll
          for (int e = 0; e < 4; ++e) {
            float t = fmaf(acc[rb][2 + b][e], rs[e], mr[e] * ck[b]);
            v[e] = b < 2 ? phi(t) : t;
          }
          fr[b][2 * rb] = pk2(v[0], v[1]); fr[b][2 * rb + 1] = pk2(v[2], v[3]);
        }
      }
      if (i == 12) fr[3] = (u32x4){0x3f803f80u, 0x3f803f80u, 0x3f803f80u, 0x3f803f80u};
#pragma unroll
      for (int be = 0; be < 2; ++be)
#pragma unroll
        for (int bd = 0; bd < 2; ++bd) {
          f32x4 kv = mfma(__builtin_bit_cast(bf16x8, fr[bd]), __builtin_bit_cast(bf16x8, fr[2 + be]), z4);
          *(LDS f32x4*)(sb + rg * 4096 + be * 2048 + lane * 32 + bd * 16) = kv;
        }
      TSW(6, 4);
      WAIT_BAR(63);
      const int t = tid - 256;
      f32x4 s = *(LDS const f32x4*)(sb + t * 16);
#pragma unroll
      for (int a = 1; a < 4; ++a) s += *(LDS const f32x4*)(sb + a * 4096 + t * 16);
      *(f32x4*)(ws + WS_KVP + ((size_t)(j * 2 + (t >> 7)) * 16 + r) * 2048 + (t & 127) * 16) = s;
    }
    TSW(3, 4);
  }
}

constexpr unsigned L2_KV = 131072;
#define LD4(dst, voff, rsrc, soff, imm) asm volatile("buffer_load_dwordx4 %0, %1, %2, %3 offen offset:" #imm : "=v"(dst) : "v"(voff), "s"(rsrc), "s"(soff) : "memory")
#define LD2(dst, voff, rsrc, soff, imm) asm volatile("buffer_load_dwordx2 %0, %1, %2, %3 offen offset:" #imm : "=v"(dst) : "v"(voff), "s"(rsrc), "s"(soff) : "memory")

__global__ __launch_bounds__(512, 2) void k2(const u16* __restrict__ x, const u16* __restrict__ wo, const u16* __restrict__ w2,
                                             const u16* __restrict__ b2, u16* __restrict__ out, char* __restrict__ ws) {
  extern __shared__ __attribute__((aligned(16))) char smem[];
  const unsigned sb = (unsigned)(size_t)(LDS char*)smem;
  const int bid = blockIdx.x, tid = threadIdx.x, lane = tid & 63;
  const int w = __builtin_amdgcn_readfirstlane(tid >> 6);
  const int xc = bid & 7, ix = bid >> 3;
  const unsigned long long t00 = __builtin_amdgcn_s_memrealtime();
  if (ix < 4) {
    const int id = xc * 4 + ix, t = tid & 127, gq = tid >> 7;
    const char* src = ws + WS_KVP + ((size_t)id * 16 + gq * 4) * 2048 + t * 16;
    f32x4 s = *(const f32x4*)src;
#pragma unroll
    for (int a = 1; a < 4; ++a) s += *(const f32x4*)(src + a * 2048);
    *(LDS f32x4*)(sb + gq * 2048 + t * 16) = s;
    __syncthreads();
    if (tid < 64) {
      f32x4 a = {0.f, 0.f, 0.f, 0.f}, b = a;
#pragma unroll
      for (int e = 0; e < 4; ++e) { a += *(LDS const f32x4*)(sb + e * 2048 + tid * 32); b += *(LDS const f32x4*)(sb + e * 2048 + tid * 32 + 16); }
      u32x4 o = {pk2(a[0], a[1]), pk2(a[2], a[3]), pk2(b[0], b[1]), pk2(b[2], b[3])};
      if (tid >= 48) { o.z = 0; o.w = 0; }
      const u32x4 rk = mk_rsrc(ws + WS_KV, 32768);
      asm volatile("buffer_store_dwordx4 %0, %1, %2, 0 offen sc1\n\ts_waitcnt vmcnt(0)" :: "v"(o), "v"(id * 1024 + tid * 16), "s"(rk) : "memory");
      if (tid == 0) __hip_atomic_store((unsigned*)(ws + WS_FLAG + id * 128), 1u, __ATOMIC_RELAXED, __HIP_MEMORY_SCOPE_AGENT);
    }
    return;
  }
  const int rt = xc * 4 + (ix - 4) / 7, ct = (ix - 4) % 7;
  const int i = lane & 15, g = lane >> 4, q = i >> 2, p = i & 3;
  const int rb = w & 3, kh = w >> 2;
  const int R = rt * 4 + rb;
  const u32x4 rw2 = mk_rsrc(w2, F * D * 2), rwo = mk_rsrc(wo, D * D * 2), rws = mk_rsrc(ws, (unsigned)WS_END);
  const unsigned swz = (unsigned)(((lane & 7) ^ (2 * ((lane >> 4) & 3))) << 4);
  const unsigned vw2 = (unsigned)(kh * 896 + rb * 8 + (lane >> 3)) * 896u + ct * 128 + swz;
  const unsigned vwo = (unsigned)(kh * 224 + rb * 8 + (lane >> 3)) * 896u + ct * 128 + swz;
  const unsigned v16 = lane * 16, v8 = lane * 8;
  const unsigned hs = (unsigned)WS_H + (unsigned)(R * 56 + kh * 28) * 1024u;
  u32x4 hf[28], pq[8], kvr[4];
  u32x2 hl[4], hh[4];
  unsigned flag;
#define HL(u) do { if ((u) % 7 == 3) { LD2(hl[(u) / 7], v8, rws, hs + (u) * 1024, 0); LD2(hh[(u) / 7], v8, rws, hs + (u) * 1024, 512); } \
                   else LD4(hf[u], v16, rws, hs + (u) * 1024, 0); } while (0)
#define GRP(bt, sl) do { _Pragma("unroll") for (int v = 0; v < 4; ++v) { \
      dma16(sb + (sl) * 32768 + v * 8192 + w * 1024, vw2, rw2, (unsigned)((bt) * 4 + v) * 28672u); HL((bt) * 4 + v); } } while (0)
#define GRPO(z, sl) do { _Pragma("unroll") for (int v = 0; v < 4; ++v) \
      dma16(sb + (sl) * 32768 + kh * 16384 + (v * 4 + rb) * 1024, vwo, rwo, (unsigned)((z) * 112 + v * 32) * 896u); } while (0)
  GRP(0, 0); GRP(1, 1); GRP(2, 2); GRP(3, 3);
  unsigned ra[2][4];
  {
    const int sw = ((4 * g + q) >> 1) & 3;
#pragma unroll
    for (int cb = 0; cb < 4; ++cb) {
      ra[0][cb] = sb + kh * 4096 + (4 * g + q) * 128 + ((cb ^ sw) << 5) + p * 8; ra[1][cb] = ra[0][cb] + 65536;
      asm volatile("" : "+v"(ra[0][cb]), "+v"(ra[1][cb]));
    }
  }
  const f32x4 z4 = {0.f, 0.f, 0.f, 0.f};
  f32x4 af[4] = {z4, z4, z4, z4}, as[4] = {z4, z4, z4, z4};
  bf16x8 W[2][4];
#define PIN4(a) asm volatile("" : "+v"(a))
#define PINH(u) do { if ((u) % 7 == 3) { PIN4(hl[(u) / 7]); PIN4(hh[(u) / 7]); hf[u] = (u32x4){hl[(u) / 7].x, hl[(u) / 7].y, hh[(u) / 7].x, hh[(u) / 7].y}; } else PIN4(hf[u]); } while (0)
#define RDW(buf, cb, sl, v) do { const unsigned a_ = ra[(sl) >> 1][cb] + ((sl) & 1) * 32768 + (v) * 8192; W[buf][cb] = tr_frag(a_, a_ + 2048); } while (0)
#define BATCH(bt, sl) do { PINH((bt) * 4); PINH((bt) * 4 + 1); PINH((bt) * 4 + 2); PINH((bt) * 4 + 3); \
      _Pragma("unroll") for (int cb = 0; cb < 4; ++cb) RDW(0, cb, sl, 0); SB; \
      _Pragma("unroll") for (int v = 0; v < 4; ++v) _Pragma("unroll") for (int cb = 0; cb < 4; ++cb) { \
        if (v < 3) RDW((v + 1) & 1, cb, sl, v + 1); SB; \
        af[cb] = mfma(W[v & 1][cb], __builtin_bit_cast(bf16x8, hf[(bt) * 4 + v]), af[cb]); SB; } } while (0)
  TS(14); WAIT_BAR(25); TS(15); BATCH(0, 0);
  WAIT_BAR(17); GRP(4, 0);
  asm volatile("buffer_load_dword %0, %1, %2, %3 offen sc1" : "=v"(flag) : "v"((lane & 3) * 128), "s"(rws), "s"((unsigned)WS_FLAG + w * 512) : "memory");
  {
    const unsigned ps = (unsigned)WS_PQ + (unsigned)(R * 16 + kh * 8) * 1024u, ps2 = ps + 4096;
    LD4(pq[0], v16, rws, ps, 0); LD4(pq[1], v16, rws, ps, 1024); LD4(pq[2], v16, rws, ps, 2048); LD4(pq[3], v16, rws, ps, 3072);
    LD4(pq[4], v16, rws, ps2, 0); LD4(pq[5], v16, rws, ps2, 1024); LD4(pq[6], v16, rws, ps2, 2048); LD4(pq[7], v16, rws, ps2, 3072);
  }
  BATCH(1, 1);
  WAIT_BAR(26); GRPO(0, 1);
  BATCH(2, 2);
  WAIT_BAR(22); GRPO(1, 2);
  BATCH(3, 3);
  WAIT_BAR(16);
  PIN4(flag);
  if (!__all(flag == 1u)) {
    unsigned spins = 0;
    do {
      __builtin_amdgcn_s_sleep(2);
      flag = __hip_atomic_load((unsigned*)(ws + WS_FLAG + w * 512 + (lane & 3) * 128), __ATOMIC_RELAXED, __HIP_MEMORY_SCOPE_AGENT);
    } while (!__all(flag == 1u) && ++spins < (1u << 20));
  }
  {
    const unsigned ks = (unsigned)WS_KV + w * 4096;
    asm volatile("buffer_load_dwordx4 %0, %4, %5, %6 offen sc1\n\tbuffer_load_dwordx4 %1, %4, %5, %6 offen offset:1024 sc1\n\t"
                 "buffer_load_dwordx4 %2, %4, %5, %6 offen offset:2048 sc1\n\tbuffer_load_dwordx4 %3, %4, %5, %6 offen offset:3072 sc1"
                 : "=&v"(kvr[0]), "=&v"(kvr[1]), "=&v"(kvr[2]), "=&v"(kvr[3]) : "v"(v16), "s"(rws), "s"(ks) : "memory");
  }
  GRP(5, 3);
  BATCH(4, 0);
  asm volatile("s_waitcnt vmcnt(8)" ::: "memory");
#pragma unroll
  for (int a = 0; a < 4; ++a) { PIN4(kvr[a]); *(LDS u32x4*)(sb + L2_KV + w * 4096 + a * 1024 + lane * 16) = kvr[a]; }
  WAIT_BAR(8);
  GRP(6, 0);
  u32x2 xr[2], br[2];
  {
    const u32x4 rx = mk_rsrc(x, S * D * 2), rb2 = mk_rsrc(b2, D * 2);
    const unsigned co = ct * 128 + kh * 64 + g * 8;
    const unsigned xo = (unsigned)(R * 16 + i) * 896u + co;
    LD2(xr[0], xo, rx, 0u, 0); LD2(xr[1], xo, rx, 0u, 32); LD2(br[0], co, rb2, 0u, 0); LD2(br[1], co, rb2, 0u, 32);
  }
  TS(12);
  {
#pragma unroll
    for (int a = 0; a < 8; ++a) PIN4(pq[a]);
    const unsigned ka = sb + L2_KV + kh * 16384 + lane * 16;
    f32x4 n0[8], n1[8];
#pragma unroll
    for (int a = 0; a < 8; ++a) {
      const bf16x8 pf = __builtin_bit_cast(bf16x8, pq[a]);
      n1[a] = mfma(ld_frag(ka + a * 2048 + 1024), pf, z4); n0[a] = mfma(ld_frag(ka + a * 2048), pf, z4);
    }
    SB;
    float rd[8];
#pragma unroll
    for (int a = 0; a < 8; ++a) rd[a] = __shfl(n1[a][0], 48 + i);
    SB;
    const unsigned km = g == 3 ? 0u : ~0u;
#pragma unroll
    for (int a = 0; a < 8; ++a) {
      const float r_ = __builtin_amdgcn_rcpf(rd[a] + 1e-6f);
      pq[a] = (u32x4){pk2(n0[a][0] * r_, n0[a][1] * r_), pk2(n0[a][2] * r_, n0[a][3] * r_), pk2(n1[a][0] * r_, n1[a][1] * r_) & km, pk2(n1[a][2] * r_, n1[a][3] * r_) & km};
    }
    unsigned rq[4];
#pragma unroll
    for (int cb = 0; cb < 4; ++cb) { rq[cb] = ra[0][cb] + kh * 12288 + 32768; PIN4(rq[cb]); }
#define RDO(buf, cb, a) do { const unsigned a_ = rq[((a) & 1) ? (cb) ^ 2 : (cb)] + ((a) >> 2) * 32768 + ((a) & 3) * 3584; W[buf][cb] = tr_frag(a_, a_ + 2048); } while (0)
#pragma unroll
    for (int cb = 0; cb < 4; ++cb) RDO(0, cb, 0);
    SB;
#pragma unroll
    for (int a = 0; a < 8; ++a)
#pragma unroll
      for (int cb = 0; cb < 4; ++cb) {
        if (a < 7) RDO((a + 1) & 1, cb, a + 1);
        SB;
        as[cb] = mfma(W[a & 1][cb], __builtin_bit_cast(bf16x8, pq[a]), as[cb]); SB;
      }
  }
  TS(13);
  WAIT_BAR(13);
  BATCH(5, 3);
  WAIT_BAR(0);
  TS(10);
  BATCH(6, 0);
  {
    const unsigned ex = sb + 32768 + w * 4096 + lane * 16;
    *(LDS f32x4*)(ex) = kh ? af[0] : af[2]; *(LDS f32x4*)(ex + 1024) = kh ? af[1] : af[3];
    *(LDS f32x4*)(ex + 2048) = kh ? as[0] : as[2]; *(LDS f32x4*)(ex + 3072) = kh ? as[1] : as[3];
    WAIT_BAR(0);
    const unsigned ey = sb + 32768 + (w ^ 4) * 4096 + lane * 16;
    PIN4(xr[0]); PIN4(xr[1]); PIN4(br[0]); PIN4(br[1]);
#pragma unroll
    for (int c = 0; c < 2; ++c) {
      f32x4 f = (kh ? af[2 + c] : af[c]) + *(LDS const f32x4*)(ey + c * 1024);
      f32x4 s = (kh ? as[2 + c] : as[c]) + *(LDS const f32x4*)(ey + 2048 + c * 1024);
      const float xv[4] = {bflo(xr[c].x), bfhi(xr[c].x), bflo(xr[c].y), bfhi(xr[c].y)};
      const float bv[4] = {bflo(br[c].x), bfhi(br[c].x), bflo(br[c].y), bfhi(br[c].y)};
      float o[4];
#pragma unroll
      for (int e = 0; e < 4; ++e) {
        float t = f[e] + bv[e];
        float gt = t * __builtin_amdgcn_rcpf(1.f + __expf(-t));
        o[e] = fmaf(s[e], gt, xv[e]);
      }
      *(u32x2*)((char*)out + (size_t)(R * 16 + i) * 896 + ct * 128 + kh * 64 + c * 32 + g * 8) = (u32x2){pk2(o[0], o[1]), pk2(o[2], o[3])};
    }
  }
  while (__builtin_amdgcn_s_memrealtime() - t00 < 3000ull) __builtin_amdgcn_s_sleep(1);
}

extern "C" void kernel_launch(void* const* d_in, const int* in_sizes, int n_in, void* d_out, int out_size, void* d_ws,
                              size_t ws_size, hipStream_t stream) {
  const u16* x = (const u16*)d_in[0]; const u16* wqkv = (const u16*)d_in[1]; const u16* wo = (const u16*)d_in[2];
  const u16* w1 = (const u16*)d_in[4]; const u16* b1 = (const u16*)d_in[5];
  const u16* w2 = (const u16*)d_in[6]; const u16* b2 = (const u16*)d_in[7];
  (void)hipFuncSetAttribute((const void*)k1, hipFuncAttributeMaxDynamicSharedMemorySize, L1_END);
  (void)hipFuncSetAttribute((const void*)k2, hipFuncAttributeMaxDynamicSharedMemorySize, 163840);
  k1<<<256, 512, L1_END, stream>>>(x, wqkv, w1, b1, (char*)d_ws);
  k2<<<256, 512, 163840, stream>>>(x, wo, w2, b2, (u16*)d_out, (char*)d_ws);
}
```

```cpp
#include <hip/hip_runtime.h>
#include <hip/hip_bf16.h>
#include <stdint.h>
#pragma clang diagnostic ignored "-Wint-to-pointer-cast"

typedef unsigned short u16;
typedef __bf16 bf16x8 __attribute__((ext_vector_type(8)));
typedef __bf16 bf16x2 __attribute__((ext_vector_type(2)));
typedef short s16x4 __attribute__((ext_vector_type(4)));
typedef float f32x4 __attribute__((ext_vector_type(4)));
typedef float f32x2 __attribute__((ext_vector_type(2)));
typedef unsigned u32x4 __attribute__((ext_vector_type(4)));
typedef unsigned u32x2 __attribute__((ext_vector_type(2)));
#define LDS __attribute__((address_space(3)))
#define DI __device__ __forceinline__

constexpr int S = 2048, D = 448, F = 1792;
constexpr size_t WS_H = 0;
constexpr size_t WS_PQ = WS_H + 128 * 56 * 1024;
constexpr size_t WS_KVP = WS_PQ + 128 * 16 * 1024;
constexpr size_t WS_KV = WS_KVP + 32 * 16 * 2048;
constexpr size_t WS_FLAG = WS_KV + 32 * 1024;
constexpr size_t WS_END = WS_FLAG + 32 * 128;

#define TS(n)
#define TSW(n, wv)

DI u32x4 mk_rsrc(const void* p, unsigned bytes) {
  uint64_t a = (uint64_t)p;
  u32x4 r; r.x = (unsigned)a; r.y = (unsigned)(a >> 32) & 0xffffu; r.z = bytes; r.w = 0x00020000u;
  return r;
}
DI void dma16(unsigned lds_addr, unsigned voff, u32x4 rsrc, unsigned soff) {
  asm volatile("s_mov_b32 m0, %0\n\ts_nop 0\n\tbuffer_load_dwordx4 %1, %2, %3 offen lds"
               :: "s"(lds_addr), "v"(voff), "s"(rsrc), "s"(soff) : "memory");
}
#define SB __builtin_amdgcn_sched_barrier(0)
#define WAIT_BAR(n) asm volatile("s_waitcnt vmcnt(" #n ") lgkmcnt(0)\n\ts_barrier" ::: "memory")
DI f32x4 mfma(bf16x8 a, bf16x8 b, f32x4 c) { return __builtin_amdgcn_mfma_f32_16x16x32_bf16(a, b, c, 0, 0, 0); }
DI bf16x8 ld_frag(unsigned a) { return *(LDS const bf16x8*)a; }
DI bf16x8 tr_frag(unsigned a0, unsigned a1) {
  s16x4 lo = __builtin_amdgcn_ds_read_tr16_b64_v4i16((LDS s16x4*)a0);
  s16x4 hi = __builtin_amdgcn_ds_read_tr16_b64_v4i16((LDS s16x4*)a1);
  return __builtin_bit_cast(bf16x8, __builtin_shufflevector(lo, hi, 0, 1, 2, 3, 4, 5, 6, 7));
}
DI unsigned pk2(float a, float b) { f32x2 v = {a, b}; return __builtin_bit_cast(unsigned, __builtin_convertvector(v, bf16x2)); }
DI float bflo(unsigned u) { return __uint_as_float(u << 16); }
DI float bfhi(unsigned u) { return __uint_as_float(u & 0xffff0000u); }
DI float rbf(float f) { return bflo(pk2(f, 0.f)); }
DI float phi(float t) { return (t > 0.f ? t + 1.f : __expf(t)) + 1e-6f; }

constexpr unsigned SLOT = 43008, OFF_W1 = 16384, OFF_WQ = 30720;
constexpr unsigned L1_DUMMY = 3 * SLOT, L1_ST = L1_DUMMY + 1024, L1_CS = L1_ST + 1024, L1_END = L1_CS + 1024;

DI void k1(const u16* __restrict__ x, const u16* __restrict__ wqkv,
                                             const u16* __restrict__ w1, const u16* __restrict__ b1, char* ws) {
  extern __shared__ __attribute__((aligned(16))) char smem[];
  const unsigned sb = (unsigned)(size_t)(LDS char*)smem;
  const int bid = blockIdx.x, tid = threadIdx.x, lane = tid & 63;
  const int w = __builtin_amdgcn_readfirstlane(tid >> 6);
  const int xc = bid & 7, ix = bid >> 3;
  const int r = (xc >> 1) * 4 + (ix >> 3), j = (xc & 1) * 8 + (ix & 7);
  const int i = lane & 15, g = lane >> 4, q = i >> 2, p = i & 3;
  const int rg = w & 3, role = w >> 2, odd = j & 1;
  TS(0);
  u32x4 rx = mk_rsrc(x, S * D * 2);
  const unsigned vx = (unsigned)(r * 128 + 16 * w + (lane >> 3)) * 896u + (unsigned)(((lane & 7) ^ (lane >> 3)) << 4);
  unsigned vA, vB;
  {
    int n = w * 64 + lane, rho = n / 14, c = n - rho * 14;
    int k = 8 * ((rho >> 2) & 3) + 4 * (rho >> 4) + (rho & 3);
    unsigned a1 = k * 3584 + j * 224 + c * 16;
    int pq_ = (w < 5) ? w : 5;
    n = pq_ * 64 + lane; rho = n / 12; c = n - rho * 12;
    k = 8 * ((rho >> 2) & 3) + 4 * (rho >> 4) + (rho & 3);
    int u = (c >> 1) ^ ((rho >> 2) & 1);
    unsigned a2 = k * 2688 + (u >> 1) * 896 + j * 56 + (u & 1) * 32 + (c & 1) * 16;
    vA = (w < 7) ? a1 : a2; vB = a2;
  }
  u32x4 rA = (w < 7) ? mk_rsrc(w1, D * F * 2) : mk_rsrc(wqkv, D * 3 * D * 2);
  u32x4 rB = mk_rsrc(wqkv, (w < 5) ? D * 3 * D * 2 : 0);
  const unsigned sA = (w < 7) ? 114688u : 86016u;
  const unsigned lA = (w < 7) ? OFF_W1 + w * 1024 : OFF_WQ + 5 * 1024, lAh = (w < 7) ? 7168u : 6144u;
  const unsigned lB = (w < 5) ? OFF_WQ + w * 1024 : L1_DUMMY, lBh = (w < 5) ? 6144u : 0u, lBm = (w < 5) ? ~0u : 0u;
  auto issue1 = [&](int n, int t, unsigned slot) {
    const unsigned hf = n >= 3;
    if (n % 3 == 0) dma16(sb + slot + w * 2048 + hf * 1024, vx, rx, t * 128 + hf * 7168);
    else if (n % 3 == 1) dma16(sb + slot + lA + hf * lAh, vA, rA, (2 * t + hf) * sA);
    else dma16(sb + (slot & lBm) + lB + hf * lBh, vB, rB, (2 * t + hf) * 86016u);
  };
  auto issue = [&](int t, unsigned slot) {
#pragma unroll
    for (int n = 0; n < 6; ++n) issue1(n, t, slot);
  };
  u32x2 bb1[7];
#pragma unroll
  for (int bb = 0; bb < 7; ++bb) { int cb = bb < 6 ? bb + odd : (odd ? 0 : 6); bb1[bb] = *(const u32x2*)(b1 + j * 112 + cb * 16 + 4 * g); }
  if (bid < 32 && tid == 0) *(unsigned*)(ws + WS_FLAG + bid * 128) = 0u;
  TS(7);
  issue(0, 0); issue(1, SLOT); issue(2, 2 * SLOT);
  TS(9);

  const unsigned xa0 = sb + (rg * 4 + (i >> 3)) * 1024 + (i & 7) * 128 + ((g ^ (i & 7)) << 4), xa1 = xa0 ^ 64u;
  bf16x8 ones = __builtin_bit_cast(bf16x8, (u32x4){0x3f803f80u, 0x3f803f80u, 0x3f803f80u, 0x3f803f80u});
  asm volatile("" : "+v"(ones));
  const f32x4 z4 = {0.f, 0.f, 0.f, 0.f};
  const int R0 = r * 8 + rg * 2;

  if (role == 0) {
    const unsigned wr = sb + OFF_W1 + (4 * g + q) * 224 + p * 8;
    const unsigned wa = wr + odd * 32, wa6 = wr + (odd ? 0 : 192);
    const int c1 = rg + 4 < 7 ? rg + 4 : 6;
    const unsigned ca0 = wr + rg * 32, ca1 = wr + c1 * 32;
    f32x4 acc[2][7], cs0 = z4, cs1 = z4;
#pragma unroll
    for (int a = 0; a < 2; ++a)
#pragma unroll
      for (int b = 0; b < 7; ++b) acc[a][b] = z4;
    bf16x8 X[2][2], W[2][9];
#define RD0(buf, n, so_, hf) do { \
      if ((n) < 2) X[buf][(n) & 1] = ld_frag(((hf) ? xa1 : xa0) + (so_) + ((n) & 1) * 2048); \
      else { const unsigned a_ = ((n) < 8 ? wa + ((n) - 2) * 32 : (n) == 8 ? wa6 : (n) == 9 ? ca0 : ca1) + (so_) + (hf) * 7168; \
             W[buf][(n) < 2 ? 0 : (n) - 2] = tr_frag(a_, a_ + 3584); } } while (0)
#define MM0(buf, m) do { \
      if ((m) < 14) acc[(m) & 1][(m) >> 1] = mfma(W[buf][(m) >> 1], X[buf][(m) & 1], acc[(m) & 1][(m) >> 1]); \
      else if ((m) == 14) cs0 = mfma(ones, W[buf][7], cs0); else cs1 = mfma(ones, W[buf][8], cs1); } while (0)
    WAIT_BAR(12);
#pragma unroll
    for (int n = 0; n < 11; ++n) RD0(0, n, 0u, 0);
    unsigned so = 0;
#pragma unroll 1
    for (int t = 0; t < 7; ++t) {
#pragma unroll
      for (int m = 0; m < 16; ++m) { MM0(0, m); SB; if (m < 11) RD0(1, m, so, 1); SB; }
      WAIT_BAR(6);
      const unsigned sn = (so == 2 * SLOT) ? 0 : so + SLOT;
      if (t == 4) { rx.z = 0; rA.z = 0; rB.z = 0; }
#pragma unroll
      for (int m = 0; m < 16; ++m) { MM0(1, m); SB; if (m < 11) RD0(0, m, sn, 0); if (m >= 10) issue1(m - 10, t + 3, so); SB; }
      so = sn;
    }
    TS(1);
    if (g == 0) { *(LDS float*)(sb + L1_CS + (rg * 16 + i) * 4) = cs0[0]; *(LDS float*)(sb + L1_CS + (c1 * 16 + i) * 4) = cs1[0]; }
    WAIT_BAR(0);
    char* hb = ws + WS_H;
    const int kap0 = (7 * j + odd) >> 1, kaps = (7 * j + (odd ? 0 : 6)) >> 1;
    f32x4 cs[7];
#pragma unroll
    for (int bb = 0; bb < 7; ++bb) { int cb = bb < 6 ? bb + odd : (odd ? 0 : 6); cs[bb] = *(LDS const f32x4*)(sb + L1_CS + (cb * 16 + 4 * g) * 4); }
#pragma unroll
    for (int rb = 0; rb < 2; ++rb) {
      const f32x2 st = *(LDS const f32x2*)(sb + L1_ST + (rg * 32 + rb * 16 + i) * 8);
      unsigned pk[14];
#pragma unroll
      for (int bb = 0; bb < 7; ++bb) {
        float bv[4] = {bflo(bb1[bb].x), bfhi(bb1[bb].x), bflo(bb1[bb].y), bfhi(bb1[bb].y)};
        float hv[4];
#pragma unroll
        for (int e = 0; e < 4; ++e) {
          float t = fmaf(acc[rb][bb][e], st.x, fmaf(st.y, cs[bb][e], bv[e]));
          t = fmaxf(t, 0.f); hv[e] = t * t;
        }
        pk[2 * bb] = pk2(hv[0], hv[1]); pk[2 * bb + 1] = pk2(hv[2], hv[3]);
      }
      char* row = hb + (size_t)(R0 + rb) * 56 * 1024;
#pragma unroll
      for (int pp = 0; pp < 3; ++pp)
        *(u32x4*)(row + (kap0 + pp) * 1024 + lane * 16) = (u32x4){pk[4 * pp], pk[4 * pp + 1], pk[4 * pp + 2], pk[4 * pp + 3]};
      *(u32x2*)(row + kaps * 1024 + odd * 512 + lane * 8) = (u32x2){pk[12], pk[13]};
    }
    WAIT_BAR(63);
  } else {
    const unsigned wr = sb + OFF_WQ + (4 * g + q) * 192 + p * 8;
    const unsigned qa0 = wr + (g & 1) * 32, qa1 = wr + ((g & 1) ^ 1) * 32;
    const int c1 = rg + 4 < 6 ? rg + 4 : 5;
    const unsigned ca0 = wr + ((rg ^ (g & 1)) * 32), ca1 = wr + ((c1 ^ (g & 1)) * 32);
    const bool padq = i >= 12;
    f32x4 acc[2][6], cs0 = z4, cs1 = z4, sq0 = z4, sq1 = z4;
#pragma unroll
    for (int a = 0; a < 2; ++a)
#pragma unroll
      for (int b = 0; b < 6; ++b) acc[a][b] = z4;
    bf16x8 X[2][2], W[2][8];
#define RD1(buf, n, so_, hf) do { \
      if ((n) < 2) X[buf][(n) & 1] = ld_frag(((hf) ? xa1 : xa0) + (so_) + ((n) & 1) * 2048); \
      else { const unsigned a_ = ((n) < 8 ? (((n) & 1) ? qa1 : qa0) + (((n) - 2) >> 1) * 64 : (n) == 8 ? ca0 : ca1) + (so_) + (hf) * 6144; \
             W[buf][(n) < 2 ? 0 : (n) - 2] = tr_frag(a_, a_ + 3072); } } while (0)
#define MM1(buf, m) do { \
      if ((m) == 0) sq0 = mfma(X[buf][0], X[buf][0], sq0); else if ((m) == 1) sq1 = mfma(X[buf][1], X[buf][1], sq1); \
      else if ((m) < 6) { if ((m) == 4) W[buf][1] = padq ? ones : W[buf][1]; \
                          acc[(m) & 1][((m) - 2) >> 1] = mfma(W[buf][((m) - 2) >> 1], X[buf][(m) & 1], acc[(m) & 1][((m) - 2) >> 1]); } \
      else if ((m) < 14) acc[(m) & 1][((m) - 2) >> 1] = mfma(X[buf][(m) & 1], W[buf][((m) - 2) >> 1], acc[(m) & 1][((m) - 2) >> 1]); \
      else if ((m) == 14) cs0 = mfma(ones, W[buf][6], cs0); else cs1 = mfma(ones, W[buf][7], cs1); } while (0)
    WAIT_BAR(12);
#pragma unroll
    for (int n = 0; n < 10; ++n) RD1(0, n, 0u, 0);
    unsigned so = 0;
#pragma unroll 1
    for (int t = 0; t < 7; ++t) {
#pragma unroll
      for (int m = 0; m < 16; ++m) { MM1(0, m); SB; if (m < 10) RD1(1, m, so, 1); SB; }
      WAIT_BAR(6);
      const unsigned sn = (so == 2 * SLOT) ? 0 : so + SLOT;
      if (t == 4) { rx.z = 0; rA.z = 0; rB.z = 0; }
#pragma unroll
      for (int m = 0; m < 16; ++m) { MM1(1, m); SB; if (m < 10) RD1(0, m, sn, 0); if (m >= 10) issue1(m - 10, t + 3, so); SB; }
      so = sn;
    }
    TSW(4, 4);
    if (g == 0) { *(LDS float*)(sb + L1_CS + (112 + rg * 16 + i) * 4) = cs0[0]; *(LDS float*)(sb + L1_CS + (112 + c1 * 16 + i) * 4) = cs1[0]; }
#pragma unroll
    for (int rb = 0; rb < 2; ++rb) {
      const f32x4 sq = rb ? sq1 : sq0;
      float s01 = (p & 1) ? sq[1] : sq[0], s23 = (p & 1) ? sq[3] : sq[2];
      float ss = (p & 2) ? s23 : s01;
      float sm = __shfl(acc[rb][1][0], 48 + i);
      float m = sm * (1.f / D);
      float var = fmaxf(ss * (1.f / D) - m * m, 0.f);
      float den = rbf(__builtin_amdgcn_sqrtf(rbf(rbf(var) + 1e-5f)));
      float rs = __builtin_amdgcn_rcpf(den);
      if (g == q) *(LDS f32x2*)(sb + L1_ST + (rg * 32 + rb * 16 + i) * 8) = (f32x2){rs, -rbf(m) * rs};
    }
    WAIT_BAR(0);
    TSW(5, 4);
    {
      const f32x4 cq0 = *(LDS const f32x4*)(sb + L1_CS + (112 + 4 * g) * 4), cq1 = *(LDS const f32x4*)(sb + L1_CS + (128 + 4 * g) * 4);
#pragma unroll
      for (int rb = 0; rb < 2; ++rb) {
        const f32x2 st = *(LDS const f32x2*)(sb + L1_ST + (rg * 32 + rb * 16 + i) * 8);
        float v0[4], v1[4];
#pragma unroll
        for (int e = 0; e < 4; ++e) {
          v0[e] = phi(fmaf(acc[rb][0][e], st.x, st.y * cq0[e]));
          v1[e] = phi(fmaf(acc[rb][1][e], st.x, st.y * cq1[e]));
        }
        u32x4 o = {pk2(v0[0], v0[1]), pk2(v0[2], v0[3]), pk2(v1[0], v1[1]), pk2(v1[2], v1[3])};
        if (g == 3) { o.z = 0; o.w = 0; }
        *(u32x4*)(ws + WS_PQ + ((size_t)(R0 + rb) * 16 + j) * 1024 + lane * 16) = o;
      }
    }
    {
      float ck[4];
#pragma unroll
      for (int b = 0; b < 4; ++b) ck[b] = *(LDS const float*)(sb + L1_CS + (144 + b * 16 + i) * 4);
      u32x4 fr[4];
#pragma unroll
      for (int rb = 0; rb < 2; ++rb) {
        const f32x4 s01 = *(LDS const f32x4*)(sb + L1_ST + (rg * 32 + rb * 16 + 4 * g) * 8);
        const f32x4 s23 = *(LDS const f32x4*)(sb + L1_ST + (rg * 32 + rb * 16 + 4 * g) * 8 + 16);
        const float rs[4] = {s01[0], s01[2], s23[0], s23[2]}, mr[4] = {s01[1], s01[3], s23[1], s23[3]};
#pragma unroll
        for (int b = 0; b < 4; ++b) {
          float v[4];
#pragma unroll
          for (int e = 0; e < 4; ++e) {
            float t = fmaf(acc[rb][2 + b][e], rs[e], mr[e] * ck[b]);
            v[e] = b < 2 ? phi(t) : t;
          }
          fr[b][2 * rb] = pk2(v[0], v[1]); fr[b][2 * rb + 1] = pk2(v[2], v[3]);
        }
      }
      if (i == 12) fr[3] = (u32x4){0x3f803f80u, 0x3f803f80u, 0x3f803f80u, 0x3f803f80u};
#pragma unroll
      for (int be = 0; be < 2; ++be)
#pragma unroll
        for (int bd = 0; bd < 2; ++bd) {
          f32x4 kv = mfma(__builtin_bit_cast(bf16x8, fr[bd]), __builtin_bit_cast(bf16x8, fr[2 + be]), z4);
          *(LDS f32x4*)(sb + rg * 4096 + be * 2048 + lane * 32 + bd * 16) = kv;
        }
      TSW(6, 4);
      WAIT_BAR(63);
      const int t = tid - 256;
      f32x4 s = *(LDS const f32x4*)(sb + t * 16);
#pragma unroll
      for (int a = 1; a < 4; ++a) s += *(LDS const f32x4*)(sb + a * 4096 + t * 16);
      *(f32x4*)(ws + WS_KVP + ((size_t)(j * 2 + (t >> 7)) * 16 + r) * 2048 + (t & 127) * 16) = s;
    }
    TSW(3, 4);
  }
}

constexpr unsigned L2_KV = 131072;
#define LD4(dst, voff, rsrc, soff, imm) asm volatile("buffer_load_dwordx4 %0, %1, %2, %3 offen offset:" #imm : "=v"(dst) : "v"(voff), "s"(rsrc), "s"(soff) : "memory")
#define LD2(dst, voff, rsrc, soff, imm) asm volatile("buffer_load_dwordx2 %0, %1, %2, %3 offen offset:" #imm : "=v"(dst) : "v"(voff), "s"(rsrc), "s"(soff) : "memory")

DI void k2(const u16* __restrict__ x, const u16* __restrict__ wo, const u16* __restrict__ w2,
                                             const u16* __restrict__ b2, u16* __restrict__ out, char* ws) {
  extern __shared__ __attribute__((aligned(16))) char smem[];
  const unsigned sb = (unsigned)(size_t)(LDS char*)smem;
  const int bid = blockIdx.x, tid = threadIdx.x, lane = tid & 63;
  const int w = __builtin_amdgcn_readfirstlane(tid >> 6);
  const int xc = bid & 7, ix = bid >> 3;
  TS(8);
  if (ix < 4) {
    const int id = xc * 4 + ix, t = tid & 127, gq = tid >> 7;
    const char* src = ws + WS_KVP + ((size_t)id * 16 + gq * 4) * 2048 + t * 16;
    f32x4 s = *(const f32x4*)src;
#pragma unroll
    for (int a = 1; a < 4; ++a) s += *(const f32x4*)(src + a * 2048);
    *(LDS f32x4*)(sb + gq * 2048 + t * 16) = s;
    __syncthreads();
    if (tid < 64) {
      f32x4 a = {0.f, 0.f, 0.f, 0.f}, b = a;
#pragma unroll
      for (int e = 0; e < 4; ++e) { a += *(LDS const f32x4*)(sb + e * 2048 + tid * 32); b += *(LDS const f32x4*)(sb + e * 2048 + tid * 32 + 16); }
      u32x4 o = {pk2(a[0], a[1]), pk2(a[2], a[3]), pk2(b[0], b[1]), pk2(b[2], b[3])};
      if (tid >= 48) { o.z = 0; o.w = 0; }
      const u32x4 rk = mk_rsrc(ws + WS_KV, 32768);
      asm volatile("buffer_store_dwordx4 %0, %1, %2, 0 offen sc1\n\ts_waitcnt vmcnt(0)" :: "v"(o), "v"(id * 1024 + tid * 16), "s"(rk) : "memory");
      if (tid == 0) __hip_atomic_store((unsigned*)(ws + WS_FLAG + id * 128), 1u, __ATOMIC_RELAXED, __HIP_MEMORY_SCOPE_AGENT);
    }
    return;
  }
  const int rt = xc * 4 + (ix - 4) / 7, ct = (ix - 4) % 7;
  const int i = lane & 15, g = lane >> 4, q = i >> 2, p = i & 3;
  const int rb = w & 3, kh = w >> 2;
  const int R = rt * 4 + rb;
  const u32x4 rw2 = mk_rsrc(w2, F * D * 2), rwo = mk_rsrc(wo, D * D * 2), rws = mk_rsrc(ws, (unsigned)WS_END);
  const unsigned swz = (unsigned)(((lane & 7) ^ (2 * ((lane >> 4) & 3))) << 4);
  const unsigned vw2 = (unsigned)(kh * 896 + rb * 8 + (lane >> 3)) * 896u + ct * 128 + swz;
  const unsigned vwo = (unsigned)(kh * 224 + rb * 8 + (lane >> 3)) * 896u + ct * 128 + swz;
  const unsigned v16 = lane * 16, v8 = lane * 8;
  const unsigned hs = (unsigned)WS_H + (unsigned)(R * 56 + kh * 28) * 1024u;
  u32x4 hf[28], pq[8], kvr[4];
  u32x2 hl[4], hh[4];
  unsigned flag;
#define HL(u) do { if ((u) % 7 == 3) { LD2(hl[(u) / 7], v8, rws, hs + (u) * 1024, 0); LD2(hh[(u) / 7], v8, rws, hs + (u) * 1024, 512); } \
                   else LD4(hf[u], v16, rws, hs + (u) * 1024, 0); } while (0)
#define GRP(bt, sl) do { _Pragma("unroll") for (int v = 0; v < 4; ++v) { \
      dma16(sb + (sl) * 32768 + v * 8192 + w * 1024, vw2, rw2, (unsigned)((bt) * 4 + v) * 28672u); HL((bt) * 4 + v); } } while (0)
#define GRPO(z, sl) do { _Pragma("unroll") for (int v = 0; v < 4; ++v) \
      dma16(sb + (sl) * 32768 + kh * 16384 + (v * 4 + rb) * 1024, vwo, rwo, (unsigned)((z) * 112 + v * 32) * 896u); } while (0)
  GRP(0, 0); GRP(1, 1); GRP(2, 2); GRP(3, 3);
  unsigned ra[2][4];
  {
    const int sw = ((4 * g + q) >> 1) & 3;
#pragma unroll
    for (int cb = 0; cb < 4; ++cb) {
      ra[0][cb] = sb + kh * 4096 + (4 * g + q) * 128 + ((cb ^ sw) << 5) + p * 8; ra[1][cb] = ra[0][cb] + 65536;
      asm volatile("" : "+v"(ra[0][cb]), "+v"(ra[1][cb]));
    }
  }
  const f32x4 z4 = {0.f, 0.f, 0.f, 0.f};
  f32x4 af[4] = {z4, z4, z4, z4}, as[4] = {z4, z4, z4, z4};
  bf16x8 W[2][4];
#define PIN4(a) asm volatile("" : "+v"(a))
#define PINH(u) do { if ((u) % 7 == 3) { PIN4(hl[(u) / 7]); PIN4(hh[(u) / 7]); hf[u] = (u32x4){hl[(u) / 7].x, hl[(u) / 7].y, hh[(u) / 7].x, hh[(u) / 7].y}; } else PIN4(hf[u]); } while (0)
#define RDW(buf, cb, sl, v) do { const unsigned a_ = ra[(sl) >> 1][cb] + ((sl) & 1) * 32768 + (v) * 8192; W[buf][cb] = tr_frag(a_, a_ + 2048); } while (0)
#define BATCH(bt, sl) do { PINH((bt) * 4); PINH((bt) * 4 + 1); PINH((bt) * 4 + 2); PINH((bt) * 4 + 3); \
      _Pragma("unroll") for (int cb = 0; cb < 4; ++cb) RDW(0, cb, sl, 0); SB; \
      _Pragma("unroll") for (int v = 0; v < 4; ++v) _Pragma("unroll") for (int cb = 0; cb < 4; ++cb) { \
        if (v < 3) RDW((v + 1) & 1, cb, sl, v + 1); SB; \
        af[cb] = mfma(W[v & 1][cb], __builtin_bit_cast(bf16x8, hf[(bt) * 4 + v]), af[cb]); SB; } } while (0)
  TS(14); WAIT_BAR(25); TS(15); BATCH(0, 0);
  WAIT_BAR(17); GRP(4, 0);
  asm volatile("buffer_load_dword %0, %1, %2, %3 offen sc1" : "=v"(flag) : "v"((lane & 3) * 128), "s"(rws), "s"((unsigned)WS_FLAG + w * 512) : "memory");
  {
    const unsigned ps = (unsigned)WS_PQ + (unsigned)(R * 16 + kh * 8) * 1024u, ps2 = ps + 4096;
    LD4(pq[0], v16, rws, ps, 0); LD4(pq[1], v16, rws, ps, 1024); LD4(pq[2], v16, rws, ps, 2048); LD4(pq[3], v16, rws, ps, 3072);
    LD4(pq[4], v16, rws, ps2, 0); LD4(pq[5], v16, rws, ps2, 1024); LD4(pq[6], v16, rws, ps2, 2048); LD4(pq[7], v16, rws, ps2, 3072);
  }
  BATCH(1, 1);
  WAIT_BAR(26); GRPO(0, 1);
  BATCH(2, 2);
  WAIT_BAR(22); GRPO(1, 2);
  BATCH(3, 3);
  WAIT_BAR(16);
  PIN4(flag);
  if (!__all(flag == 1u)) {
    unsigned spins = 0;
    do {
      __builtin_amdgcn_s_sleep(2);
      flag = __hip_atomic_load((unsigned*)(ws + WS_FLAG + w * 512 + (lane & 3) * 128), __ATOMIC_RELAXED, __HIP_MEMORY_SCOPE_AGENT);
    } while (!__all(flag == 1u) && ++spins < (1u << 20));
  }
  {
    const unsigned ks = (unsigned)WS_KV + w * 4096;
    asm volatile("buffer_load_dwordx4 %0, %4, %5, %6 offen sc1\n\tbuffer_load_dwordx4 %1, %4, %5, %6 offen offset:1024 sc1\n\t"
                 "buffer_load_dwordx4 %2, %4, %5, %6 offen offset:2048 sc1\n\tbuffer_load_dwordx4 %3, %4, %5, %6 offen offset:3072 sc1"
                 : "=&v"(kvr[0]), "=&v"(kvr[1]), "=&v"(kvr[2]), "=&v"(kvr[3]) : "v"(v16), "s"(rws), "s"(ks) : "memory");
  }
  GRP(5, 3);
  BATCH(4, 0);
  asm volatile("s_waitcnt vmcnt(8)" ::: "memory");
#pragma unroll
  for (int a = 0; a < 4; ++a) { PIN4(kvr[a]); *(LDS u32x4*)(sb + L2_KV + w * 4096 + a * 1024 + lane * 16) = kvr[a]; }
  WAIT_BAR(8);
  GRP(6, 0);
  u32x2 xr[2], br[2];
  {
    const u32x4 rx = mk_rsrc(x, S * D * 2), rb2 = mk_rsrc(b2, D * 2);
    const unsigned co = ct * 128 + kh * 64 + g * 8;
    const unsigned xo = (unsigned)(R * 16 + i) * 896u + co;
    LD2(xr[0], xo, rx, 0u, 0); LD2(xr[1], xo, rx, 0u, 32); LD2(br[0], co, rb2, 0u, 0); LD2(br[1], co, rb2, 0u, 32);
  }
  TS(12);
  {
#pragma unroll
    for (int a = 0; a < 8; ++a) PIN4(pq[a]);
    const unsigned ka = sb + L2_KV + kh * 16384 + lane * 16;
    f32x4 n0[8], n1[8];
#pragma unroll
    for (int a = 0; a < 8; ++a) {
      const bf16x8 pf = __builtin_bit_cast(bf16x8, pq[a]);
      n1[a] = mfma(ld_frag(ka + a * 2048 + 1024), pf, z4); n0[a] = mfma(ld_frag(ka + a * 2048), pf, z4);
    }
    SB;
    float rd[8];
#pragma unroll
    for (int a = 0; a < 8; ++a) rd[a] = __shfl(n1[a][0], 48 + i);
    SB;
    const unsigned km = g == 3 ? 0u : ~0u;
#pragma unroll
    for (int a = 0; a < 8; ++a) {
      const float r_ = __builtin_amdgcn_rcpf(rd[a] + 1e-6f);
      pq[a] = (u32x4){pk2(n0[a][0] * r_, n0[a][1] * r_), pk2(n0[a][2] * r_, n0[a][3] * r_), pk2(n1[a][0] * r_, n1[a][1] * r_) & km, pk2(n1[a][2] * r_, n1[a][3] * r_) & km};
    }
    unsigned rq[4];
#pragma unroll
    for (int cb = 0; cb < 4; ++cb) { rq[cb] = ra[0][cb] + kh * 12288 + 32768; PIN4(rq[cb]); }
#define RDO(buf, cb, a) do { const unsigned a_ = rq[((a) & 1) ? (cb) ^ 2 : (cb)] + ((a) >> 2) * 32768 + ((a) & 3) * 3584; W[buf][cb] = tr_frag(a_, a_ + 2048); } while (0)
#pragma unroll
    for (int cb = 0; cb < 4; ++cb) RDO(0, cb, 0);
    SB;
#pragma unroll
    for (int a = 0; a < 8; ++a)
#pragma unroll
      for (int cb = 0; cb < 4; ++cb) {
        if (a < 7) RDO((a + 1) & 1, cb, a + 1);
        SB;
        as[cb] = mfma(W[a & 1][cb], __builtin_bit_cast(bf16x8, pq[a]), as[cb]); SB;
      }
  }
  TS(13);
  WAIT_BAR(13);
  BATCH(5, 3);
  WAIT_BAR(0);
  TS(10);
  BATCH(6, 0);
  {
    const unsigned ex = sb + 32768 + w * 4096 + lane * 16;
    *(LDS f32x4*)(ex) = kh ? af[0] : af[2]; *(LDS f32x4*)(ex + 1024) = kh ? af[1] : af[3];
    *(LDS f32x4*)(ex + 2048) = kh ? as[0] : as[2]; *(LDS f32x4*)(ex + 3072) = kh ? as[1] : as[3];
    WAIT_BAR(0);
    const unsigned ey = sb + 32768 + (w ^ 4) * 4096 + lane * 16;
    PIN4(xr[0]); PIN4(xr[1]); PIN4(br[0]); PIN4(br[1]);
#pragma unroll
    for (int c = 0; c < 2; ++c) {
      f32x4 f = (kh ? af[2 + c] : af[c]) + *(LDS const f32x4*)(ey + c * 1024);
      f32x4 s = (kh ? as[2 + c] : as[c]) + *(LDS const f32x4*)(ey + 2048 + c * 1024);
      const float xv[4] = {bflo(xr[c].x), bfhi(xr[c].x), bflo(xr[c].y), bfhi(xr[c].y)};
      const float bv[4] = {bflo(br[c].x), bfhi(br[c].x), bflo(br[c].y), bfhi(br[c].y)};
      float o[4];
#pragma unroll
      for (int e = 0; e < 4; ++e) {
        float t = f[e] + bv[e];
        float gt = t * __builtin_amdgcn_rcpf(1.f + __expf(-t));
        o[e] = fmaf(s[e], gt, xv[e]);
      }
      *(u32x2*)((char*)out + (size_t)(R * 16 + i) * 896 + ct * 128 + kh * 64 + c * 32 + g * 8) = (u32x2){pk2(o[0], o[1]), pk2(o[2], o[3])};
    }
  }
  TS(11);
}

__global__ __launch_bounds__(512, 2) void kall(const u16* __restrict__ x, const u16* __restrict__ wqkv, const u16* __restrict__ wo, const u16* __restrict__ w1, const u16* __restrict__ b1,
   const u16* __restrict__ w2, const u16* __restrict__ b2, u16* __restrict__ out, char* ws) {
  const unsigned long long t00 = __builtin_amdgcn_s_memrealtime();
  k1(x, wqkv, w1, b1, ws);
  asm volatile("s_waitcnt vmcnt(0)" ::: "memory");
  __syncthreads();
  if (threadIdx.x == 0) { __builtin_amdgcn_fence(__ATOMIC_RELEASE, "agent"); asm volatile("s_waitcnt vmcnt(0)" ::: "memory"); }
  while (__builtin_amdgcn_s_memrealtime() - t00 < 4000ull) __builtin_amdgcn_s_sleep(1);
  if (threadIdx.x == 0) { __builtin_amdgcn_fence(__ATOMIC_ACQUIRE, "agent"); asm volatile("s_waitcnt vmcnt(0)" ::: "memory"); }
  __syncthreads();
  k2(x, wo, w2, b2, out, ws);
  while (__builtin_amdgcn_s_memrealtime() - t00 < 8000ull) __builtin_amdgcn_s_sleep(1);
}
extern "C" void kernel_launch(void* const* d_in, const int* in_sizes, int n_in, void* d_out, int out_size, void* d_ws,
                              size_t ws_size, hipStream_t stream) {
  const u16* x = (const u16*)d_in[0]; const u16* wqkv = (const u16*)d_in[1]; const u16* wo = (const u16*)d_in[2];
  const u16* w1 = (const u16*)d_in[4]; const u16* b1 = (const u16*)d_in[5];
  const u16* w2 = (const u16*)d_in[6]; const u16* b2 = (const u16*)d_in[7];
  (void)hipFuncSetAttribute((const void*)kall, hipFuncAttributeMaxDynamicSharedMemorySize, 163840);
  kall<<<256, 512, 163840, stream>>>(x, wqkv, wo, w1, b1, w2, b2, (u16*)d_out, (char*)d_ws);
}
```

```cpp
#include <hip/hip_runtime.h>
#include <hip/hip_bf16.h>
#include <stdint.h>
#pragma clang diagnostic ignored "-Wint-to-pointer-cast"

#define HMOD "sc1"
#define PRIO_H 3
#define PRIO_C 2
#define LSLEEP 1
#define STW 99
#define PF0 2
#define PFN 0
#define FILLAT 8
#define THR_N 0
#define THR_V 4
#define NHW 8
#define TSLEEP 8
#define TOUCH 0
#define S0MODE 1
typedef unsigned short u16;
typedef unsigned long long u64;
typedef __bf16 bf16x8 __attribute__((ext_vector_type(8)));
typedef __bf16 bf16x2 __attribute__((ext_vector_type(2)));
typedef short s16x4 __attribute__((ext_vector_type(4)));
typedef float f32x4 __attribute__((ext_vector_type(4)));
typedef float f32x16 __attribute__((ext_vector_type(16)));
typedef float f32x2 __attribute__((ext_vector_type(2)));
typedef unsigned u32x4 __attribute__((ext_vector_type(4)));
typedef unsigned u32x2 __attribute__((ext_vector_type(2)));
#define LDS __attribute__((address_space(3)))
#define DI __device__ __forceinline__

constexpr int S = 2048, D = 448, F = 1792;
constexpr unsigned WS_EP = 0;
constexpr unsigned WS_KF = 256;
constexpr unsigned WS_FL = 1024;
constexpr unsigned WS_ST = WS_FL + 8192;
constexpr unsigned WS_CS = WS_ST + 16384;
constexpr unsigned WS_KV = WS_CS + 16 * 7 * 2048;
constexpr unsigned WS_KVP = WS_KV + 32768;
constexpr unsigned WS_PQ = WS_KVP + 16 * 16 * 4096;
constexpr unsigned WS_H = WS_PQ + 64 * 28 * 1024;
constexpr unsigned WS_END = WS_H + 64 * 112 * 1024;
constexpr unsigned L_CS = 155648, L_ST = L_CS + 2048, L_RS = L_ST + 512, L_SY = L_RS + 512, L_END = L_SY + 256;
constexpr unsigned SY_FULL = L_SY, SY_AUX = L_SY + 16, SY_DONE = L_SY + 32, SY_RDY = L_SY + 48, SY_FULL2 = L_SY + 64, SY_DONE2 = L_SY + 80,
                   SY_GO = L_SY + 112, SY_KOK = L_SY + 96, SY_HOK = L_SY + 100, SY_TAG = L_SY + 104;
constexpr unsigned CODE_BYTES = 31232;
constexpr unsigned SPIN_CAP = 1u << 17;

#define LDS_BYTES L_END
#define TSW(n, wv)
#define TSC(n, wv)
#define TS_INIT
#define TS_DUMP

DI u32x4 mk_rsrc(const void* p, unsigned bytes) {
  uint64_t a = (uint64_t)p;
  u32x4 r; r.x = (unsigned)a; r.y = (unsigned)(a >> 32) & 0xffffu; r.z = bytes; r.w = 0x00020000u; return r;
}
DI void dma16(unsigned lds_addr, unsigned voff, u32x4 rsrc, unsigned soff) {
  asm volatile("s_mov_b32 m0, %0\n\ts_nop 0\n\tbuffer_load_dwordx4 %1, %2, %3 offen lds"
               :: "s"(lds_addr), "v"(voff), "s"(rsrc), "s"(soff) : "memory");
}
template <unsigned OFF> DI void dma16o(unsigned lds_base, unsigned voff, u32x4 rsrc, unsigned soff) {
  asm volatile("s_add_u32 m0, %0, %4\n\ts_nop 0\n\tbuffer_load_dwordx4 %1, %2, %3 offen lds"
               :: "s"(lds_base), "v"(voff), "s"(rsrc), "s"(soff), "n"(OFF) : "memory", "scc");
}
#define SB __builtin_amdgcn_sched_barrier(0)
#define FENCE asm volatile("" ::: "memory")
#define PIN(a) asm volatile("" : "+v"(a))
#define WAIT_VM(n) asm volatile("s_waitcnt vmcnt(%0)" :: "n"(n) : "memory")
#define WAIT_LDS asm volatile("s_waitcnt lgkmcnt(0)" ::: "memory")
#define BARRIER asm volatile("s_waitcnt lgkmcnt(0)\n\ts_barrier" ::: "memory")
#define LD4(dst, voff, rsrc, soff, mod) asm volatile("buffer_load_dwordx4 %0, %1, %2, %3 offen " mod : "=v"(dst) : "v"(voff), "s"(rsrc), "s"(soff) : "memory")
#define LD2(dst, voff, rsrc, soff, mod) asm volatile("buffer_load_dwordx2 %0, %1, %2, %3 offen " mod : "=v"(dst) : "v"(voff), "s"(rsrc), "s"(soff) : "memory")
#define LD1(dst, voff, rsrc, soff, mod) asm volatile("buffer_load_dword %0, %1, %2, %3 offen " mod : "=v"(dst) : "v"(voff), "s"(rsrc), "s"(soff) : "memory")
#define ST4(src, voff, rsrc, soff, mod) asm volatile("buffer_store_dwordx4 %0, %1, %2, %3 offen " mod "\n\ts_nop 1" :: "v"(src), "v"(voff), "s"(rsrc), "s"(soff) : "memory")
#define ST2(src, voff, rsrc, soff, mod) asm volatile("buffer_store_dwordx2 %0, %1, %2, %3 offen " mod :: "v"(src), "v"(voff), "s"(rsrc), "s"(soff) : "memory")
DI f32x4 mfma16(bf16x8 a, bf16x8 b, f32x4 c) { return __builtin_amdgcn_mfma_f32_16x16x32_bf16(a, b, c, 0, 0, 0); }
DI f32x16 mfma(bf16x8 a, bf16x8 b, f32x16 c) { return __builtin_amdgcn_mfma_f32_32x32x16_bf16(a, b, c, 0, 0, 0); }
DI bf16x8 asbf(u32x4 v) { return __builtin_bit_cast(bf16x8, v); }
DI bf16x8 ld_frag(unsigned a) { return *(LDS const bf16x8*)a; }
DI bf16x8 tr_frag(unsigned a0, unsigned a1) {
  s16x4 lo = __builtin_amdgcn_ds_read_tr16_b64_v4i16((LDS s16x4*)a0);
  s16x4 hi = __builtin_amdgcn_ds_read_tr16_b64_v4i16((LDS s16x4*)a1);
  return __builtin_bit_cast(bf16x8, __builtin_shufflevector(lo, hi, 0, 1, 2, 3, 4, 5, 6, 7));
}
DI unsigned pk2(float a, float b) { f32x2 v = {a, b}; return __builtin_bit_cast(unsigned, __builtin_convertvector(v, bf16x2)); }
DI float sqs(float a) { float r; asm("v_mul_f32_e64 %0, %1, |%1|" : "=v"(r) : "v"(a)); return r; }
DI unsigned relu2(unsigned v) {
  typedef short s16x2 __attribute__((ext_vector_type(2)));
  return __builtin_bit_cast(unsigned, __builtin_elementwise_max(__builtin_bit_cast(s16x2, v), (s16x2){0, 0}));
}
DI float bflo(unsigned u) { return __uint_as_float(u << 16); }
DI float bfhi(unsigned u) { return __uint_as_float(u & 0xffff0000u); }
DI float rbf(float f) { return bflo(pk2(f, 0.f)); }
DI unsigned phi2(f32x2 a, f32x2 rs) {
  const f32x2 t = a * rs, u = t * 1.44269504f;
  const f32x2 e = {__builtin_amdgcn_fmed3f(__builtin_amdgcn_exp2f(u.x), 0.f, 1.f), __builtin_amdgcn_fmed3f(__builtin_amdgcn_exp2f(u.y), 0.f, 1.f)};
  const f32x2 m = {fmaxf(t.x, 0.f), fmaxf(t.y, 0.f)};
  const f32x2 o = (m + 1e-6f) + e;
  return pk2(o.x, o.y);
}
DI float sum8(float v) {
  v += __uint_as_float(__builtin_amdgcn_update_dpp(0u, __float_as_uint(v), 0xB1, 0xf, 0xf, true));
  v += __uint_as_float(__builtin_amdgcn_update_dpp(0u, __float_as_uint(v), 0x4E, 0xf, 0xf, true));
  v += __uint_as_float(__builtin_amdgcn_update_dpp(0u, __float_as_uint(v), 0x141, 0xf, 0xf, true));
  return v;
}
DI void lds_put(unsigned a, unsigned v) { FENCE; *(volatile LDS unsigned*)a = v; FENCE; }
DI void lds_wait(unsigned a, int n, unsigned need, int lane) {
  const unsigned ad = a + (lane & (n - 1)) * 4;
#pragma unroll 1
  for (unsigned sp = 0; sp < SPIN_CAP; ++sp) {
    FENCE; const unsigned v = *(volatile LDS unsigned*)ad; FENCE;
    if (__all(v >= need)) break;
    __builtin_amdgcn_s_sleep(LSLEEP);
  }
}
DI u64 tag_wait(unsigned voff, u32x4 rs, unsigned soff, u64 mask, u32x2 tag) {
  u64 bl = 0;
#pragma unroll 1
  for (unsigned sp = 0; sp < SPIN_CAP; ++sp) {
    u32x2 v;
    asm volatile("buffer_load_dwordx2 %0, %1, %2, %3 offen sc1\n\ts_waitcnt vmcnt(0)" : "=&v"(v) : "v"(voff), "s"(rs), "s"(soff) : "memory");
    bl = __ballot(v.x == tag.x && v.y == tag.y);
    if ((bl & mask) == mask) break;
    __builtin_amdgcn_s_sleep(TSLEEP);
  }
  return bl;
}

__global__ __launch_bounds__(512, 2) void fused(const u16* __restrict__ x, const u16* __restrict__ wqkv, const u16* __restrict__ w1, char* ws,
                                                const u16* __restrict__ wo, const u16* __restrict__ b1, const u16* __restrict__ w2,
                                                const u16* __restrict__ b2, u16* __restrict__ out) {
  const int bid = blockIdx.x, tid = threadIdx.x, lane = tid & 63;
  unsigned cpf;
  {
    u64 pc; asm volatile("s_getpc_b64 %0" : "=s"(pc));
    const u32x4 rc = mk_rsrc((const void*)(pc & ~255ull), CODE_BYTES);
    if ((tid >> 3) == 32) LD1(cpf, (unsigned)((bid >> 3) + (tid & 7) * 32) * 128u, rc, 0u, "");
  }
  const int w = __builtin_amdgcn_readfirstlane(tid >> 6);
  const int xc = bid & 7, ix = bid >> 3, sl = ix & 7;
  const int r = (xc >> 1) * 4 + (ix >> 3);
  const int c = (int)(((xc & 1) ? 0xFEB98765u : 0xDCA43210u) >> (4 * sl)) & 15;
  const bool kvt = sl >= 6, pure = sl == 7;
  const int s0 = r >= 14 ? 0 : r >> 1;
  const int rt = xc * 4 + (ix >> 3), ct = sl, rot2 = (xc * 7) >> 1;
  const int l31 = lane & 31, hh = lane >> 5, q = (lane >> 2) & 3, p = lane & 3, blk = (lane >> 4) & 1;
  const unsigned slot_b = kvt ? 49152u : 40960u;
  const u32x4 rws = mk_rsrc(ws, WS_END), rx = mk_rsrc(x, S * D * 2);
  const unsigned v16 = lane * 16, v8 = lane * 8;
  TS_INIT; TSW(0, 0);
  u32x2 tag;
  const unsigned swz = (unsigned)(((lane & 7) ^ ((lane >> 2) & 4)) << 4);
  f32x16 z16;
#pragma unroll
  for (int e = 0; e < 16; ++e) z16[e] = 0.f;

  u32x4 xs[7];
  unsigned tch[2];
  if (w >= 4) {
    if (w == 7) {
      const unsigned va = (unsigned)(r * 128 + c * 8 + (lane >> 3)) * 896u + (lane & 7) * 16;
#pragma unroll
      for (int k = 0; k < 7; ++k) LD4(xs[k], va, rx, k * 128u, "");
    }
    if (TOUCH && w == 6) {
      if (lane < 49) LD1(tch[0], lane * 128u, mk_rsrc(w1, D * F * 2), bid * 6272u, "");
      if (lane < 37) LD1(tch[1], lane * 128u, mk_rsrc(wqkv, D * 3 * D * 2), bid * 4736u, "");
    }
    *(LDS unsigned*)(L_SY + lane * 4) = (lane >> 1) == 1; BARRIER;
  }
  u32x4 xr, br; unsigned sg_;
  const unsigned fxo = (unsigned)(rt * 64 + (tid >> 3)) * 896u + ct * 128 + (tid & 7) * 16;
#define FIN_LD do { LD4(xr, fxo, rx, 0u, ""); LD4(br, (tid & 7) * 16u, mk_rsrc(b2, D * 2), ct * 128u, ""); LD1(sg_, (unsigned)(tid & ~7), rws, WS_ST + rt * 512u, "sc1"); } while (0)
#define PUT_TILES(base, t) do { _Pragma("unroll") for (int n_ = 0; n_ < 8; ++n_) *(LDS f32x4*)((base) + n_ * 1024 + (v16 ^ (n_ * 32))) = \
      (f32x4){t[n_ >> 2][4 * (n_ & 3)], t[n_ >> 2][4 * (n_ & 3) + 1], t[n_ >> 2][4 * (n_ & 3) + 2], t[n_ >> 2][4 * (n_ & 3) + 3]}; } while (0)
  if (__builtin_expect(w < 4, 1)) {
    const int h = w;
    const u32x4 rw2 = mk_rsrc(w2, F * D * 2), rwo = mk_rsrc(wo, D * D * 2);
    const unsigned vwo = (unsigned)(lane >> 3) * 896u + ct * 128 + swz;
    const unsigned vw2 = vwo + (unsigned)((h >> 1) * 896 + (h & 1) * 16) * 896u;
    const int hg = (h + xc) & 3;
    u32x4 R[32];
#define PFLD(n) do { if ((n) < 18) { int k_ = 19 + ((n) >> 1) + rot2; k_ = k_ >= 28 ? k_ - 28 : k_; LD4(R[n], vw2, rw2, k_ * 28672u + ((n) & 1) * 7168u, ""); } \
      else LD4(R[n], vwo, rwo, hg * 100352u + ((n) - 18) * 7168u, ""); } while (0)
    {
      __builtin_amdgcn_s_setprio(PRIO_H);
      const int hp = h >> 1, hs_ = h & 1;
      const unsigned vx0 = (unsigned)(r * 128 + hs_ * 64 + (lane >> 3)) * 896u + (unsigned)(((lane & 7) ^ (lane >> 4)) << 4), vx1 = (vx0 ^ 64u) + 7168u;
      const bool a1 = c < 10, b1_ = c < 9;
      const u32x4 r0 = mk_rsrc(a1 ? w1 : wqkv, a1 ? D * F * 2 : D * 3 * D * 2), r1 = mk_rsrc(b1_ ? w1 : wqkv, b1_ ? D * F * 2 : D * 3 * D * 2);
      const unsigned p0 = a1 ? 3584u : 2688u, p1 = b1_ ? 3584u : 2688u;
      unsigned V0, V1, V2, V3;
      {
        const unsigned kl = 32 * hs_ + (lane >> 3);
        const unsigned c0 = (a1 ? c : c - 10) * 384, c1 = c == 9 ? 512 : c0;
        const unsigned vk = (32 * hs_ + (lane >> 4)) * 2688 + ((224 * c - 1792) & ~63) + ((q ^ (lane >> 4)) << 6) + p * 16;
        V0 = kvt ? vk : kl * p0 + c0 + swz;
        V1 = kvt ? vk + 4 * 2688 : kl * p1 + c1 + swz + 128;
        V2 = kvt ? V0 + 896 : V1 + 128;
        V3 = V1 + 896;
      }
      int sg = (S0MODE ? s0 : 0) + hp; sg = sg == 7 ? 0 : sg;
      unsigned so = hp * slot_b;
#define GROUP(n) do { const unsigned kk = sg * 64 + 8 * (n), sa = kk * p0, sb_ = kk * p1; \
          dma16o<(n) * 2048>(lx, vx0, rx, sg * 128 + (n) * 14336); \
          dma16o<16384 + (n) * 1024>(lw, V0, r0, sa); \
          dma16o<24576 + (n) * 1024>(lw, V1, r1, sb_); \
          dma16o<(n) * 2048 + 1024>(lx, vx1, rx, sg * 128 + (n) * 14336); \
          dma16o<32768 + (n) * 1024>(lw, V2, r1, sb_); \
          if (kvt) dma16o<40960 + (n) * 1024>(lw, V3, r1, sb_); } while (0)
      if (hp) BARRIER;
#pragma unroll 1
      for (int t = hp; t < 7; t += 2) {
        if (t >= 3) lds_wait(SY_DONE, 4, t - 2, lane);
        TSW(32 + t, 2 * hp);
        const unsigned lx = so + hs_ * 8192, lw = so + hs_ * 4096;
        GROUP(0); GROUP(1); GROUP(2); GROUP(3);
        TSW(40 + t, 2 * hp);
        if (t == 0) BARRIER;
        if (t == 6) lds_put(SY_GO, 1);
        WAIT_VM(0); lds_put(SY_FULL + h * 4, t == 5 ? 8 : t + 2);
        TSW(48 + t, 2 * hp);
        if (t == 1) {
          unsigned t2;
          asm volatile("buffer_load_dword %0, %1, %2, %3 offen\n\ts_waitcnt vmcnt(0)" : "=&v"(t2) : "v"(lane < (hs_ ? 13 : 49) ? lane * 128u : 0x80000000u),
                       "s"(mk_rsrc(hs_ ? wo : w2, hs_ ? D * D * 2 : F * D * 2)), "s"(bid * (hs_ ? 1664u : 6272u)) : "memory");
        }
        so = so == 0 ? 2 * slot_b : so - slot_b;
        sg = sg >= 5 ? sg - 5 : sg + 2;
      }
      tag = *(volatile LDS u32x2*)SY_TAG;
      if (hp) {
        u32x4 xf[8];
        unsigned bi;
        const bool chk = !hs_ || kvt || lane < 32;
        const u32x4 rb1 = mk_rsrc(b1, c < 10 ? F * 2 : 0);
#pragma unroll 1
        for (unsigned sp = 0; sp < SPIN_CAP; ++sp) {
#pragma unroll
          for (int k = 0; k < 7; ++k) LD4(xf[k], v16, rws, WS_CS + c * 14336u + hs_ * 1024u + k * 2048u, "sc1");
          LD4(xf[7], v16, rws, WS_ST + r * 1024u, "sc1");
          LD1(bi, lane * 4u, rb1, c * 384u + hs_ * 256u, "");
          WAIT_VM(0);
          PIN(xf[0]); PIN(xf[7]); PIN(bi);
          unsigned bad = (xf[0].y ^ tag.x) | (xf[0].w ^ tag.x);
          float ax_ = __uint_as_float(xf[0].x), ay_ = __uint_as_float(xf[0].z);
#pragma unroll
          for (int k = 1; k < 7; ++k) {
            PIN(xf[k]);
            ax_ += __uint_as_float(xf[k].x); ay_ += __uint_as_float(xf[k].z); PIN(ax_); PIN(ay_);
            bad |= (xf[k].y ^ tag.x) | (xf[k].w ^ tag.x);
          }
          const unsigned hi = pk2(ax_, ay_); ax_ -= bflo(hi); PIN(ax_); ay_ -= bfhi(hi);
          const unsigned mi = pk2(ax_, ay_); ax_ -= bflo(mi); PIN(ax_); ay_ -= bfhi(mi);
          const unsigned lo = pk2(ax_, ay_);
          *(LDS u32x4*)(L_CS + hs_ * 1024 + v16) = (u32x4){(hi & 0xffffu) | (mi << 16), (lo & 0xffffu) | (bi << 16),
                                                           (hi >> 16) | (mi & 0xffff0000u), (lo >> 16) | (bi & 0xffff0000u)};
          if (hs_) *(LDS u32x2*)(L_ST + v8) = (u32x2){xf[7].x, xf[7].z};
          else *(LDS f32x2*)(L_RS + v8) = (f32x2){__builtin_amdgcn_rcpf(bfhi(xf[7].x)), __builtin_amdgcn_rcpf(bfhi(xf[7].z))};
          if (__builtin_expect(__all((bad == 0 || !chk) && xf[7].y == tag.x && xf[7].w == tag.x), 1)) break;
          __builtin_amdgcn_s_sleep(8);
        }
        lds_put(SY_AUX + hs_ * 4, 1);
        TSW(3, 2);
        lds_wait(SY_GO, 1, 1, lane);
      }
      __builtin_amdgcn_s_setprio(0);
    }
    if (pure) return;
#pragma unroll
    for (int n = 0; n < 32; ++n) PFLD(n);
    TSW(23, 0);
    {
      lds_wait(SY_DONE, 4, FILLAT, lane); WAIT_VM(20);
#pragma unroll 1
      for (int u = 0; u < 19; ++u) {
        int k = u + rot2; k = k >= 28 ? k - 28 : k;
        dma16(u * 8192 + h * 2048, vw2, rw2, k * 28672u); dma16(u * 8192 + h * 2048 + 1024, vw2, rw2, k * 28672u + 7168u);
        if (u < THR_N) WAIT_VM(THR_V);
      }
    }
    WAIT_VM(0); lds_put(SY_RDY + h * 4, 1);
    TSW(4, 0);
    u32x4 PQ[2][7], KV[8];
    lds_wait(SY_HOK, 1, 1, lane);
    if (*(volatile LDS unsigned*)SY_HOK < 2u) tag_wait(l31 * 8u, rws, WS_FL + rt * 256u, 0xF80000ull, tag);
#pragma unroll
    for (int n = 0; n < 14; ++n) LD4(PQ[n / 7][n % 7], v16, rws, WS_PQ + ((rt * 2 + n / 7) * 28 + hg * 7 + n % 7) * 1024u, "sc1");
    FIN_LD;
    TSW(19, 0);
    lds_wait(SY_DONE2, 4, 1, lane);
#pragma unroll
    for (int n = 0; n < 18; ++n) { PIN(R[n]); *(LDS u32x4*)(h * 2048 + v16 + (n >> 1) * 8192 + (n & 1) * 1024) = R[n]; }
    WAIT_LDS; lds_put(SY_FULL2 + h * 4, 1);
    TSW(20, 0);
    bool kok = false;
#define KV_LD do { _Pragma("unroll") for (int n = 0; n < 8; ++n) LD4(KV[n], v16, rws, WS_KV + hg * 8192u + n * 1024u, "sc1"); } while (0)
#pragma unroll 1
    for (unsigned sp = 0; sp < SPIN_CAP; ++sp) {
      u32x2 v;
      asm volatile("buffer_load_dwordx2 %0, %1, %2, %3 offen sc1\n\ts_waitcnt vmcnt(0)" : "=&v"(v) : "v"((lane & 7) * 8u), "s"(rws), "s"(WS_KF + hg * 64u) : "memory");
      kok = __all(v.x == tag.x && v.y == tag.y);
      if (kok || __all(*(volatile LDS unsigned*)(SY_DONE2 + p * 4) >= 2u)) break;
      __builtin_amdgcn_s_sleep(TSLEEP);
    }
    if (kok) KV_LD;
    lds_wait(SY_DONE2, 4, 2, lane);
    const unsigned lo_ = (9 + 2 * h) * 8192;
#pragma unroll
    for (int n = 0; n < 14; ++n) { PIN(R[18 + n]); *(LDS u32x4*)(lo_ + v16 + n * 1024) = R[18 + n]; }
    bf16x8 WO[4][2][2];
    {
      const unsigned a0 = lo_ + (4 * hh + q) * 128 + (((q >> 1) & 1) << 6) + blk * 32 + p * 8, a1_ = a0 ^ 64u;
#pragma unroll
      for (int n = 0; n < 4; ++n)
#pragma unroll
        for (int sg = 0; sg < 2; ++sg) {
          const unsigned o = n * 3584 + sg * 2048;
          WO[n][sg][0] = tr_frag(a0 + o, a0 + o + 1024); WO[n][sg][1] = tr_frag(a1_ + o, a1_ + o + 1024);
        }
    }
    if (!kok) { tag_wait((lane & 7) * 8u, rws, WS_KF + hg * 64u, ~0ull, tag); KV_LD; }
    TSW(5, 0);
    WAIT_VM(0);
    TSW(16, 0);
    const unsigned km = hh ? 0u : ~0u;
#pragma unroll
    for (int n = 0; n < 8; ++n) PIN(KV[n]);
#pragma unroll
    for (int n = 0; n < 14; ++n) PIN(PQ[n / 7][n % 7]);
    u32x4 O[8][2];
#pragma unroll
    for (int pr = 0; pr < 4; ++pr) {
      f32x16 nm[2];
      const u32x4* P = PQ[pr >> 1];
#pragma unroll
      for (int j = 0; j < 2; ++j) { const int n = (pr & 1) * 2 + j; nm[j] = mfma(asbf(KV[2 * n]), asbf(P[2 * n]), z16); }
#pragma unroll
      for (int j = 0; j < 2; ++j) {
        const int n = (pr & 1) * 2 + j, lo = n < 3 ? 2 * n + 1 : 5, hi = n ? 2 * n - 1 : 1;
        nm[j] = mfma(asbf(KV[2 * n + 1]), asbf((u32x4){P[lo].x, P[lo].y, P[hi].z, P[hi].w}), nm[j]);
      }
#pragma unroll
      for (int j = 0; j < 2; ++j) {
        const unsigned dn = __float_as_uint(nm[j][12]);
        const float rd = __builtin_amdgcn_rcpf(__uint_as_float(__builtin_amdgcn_permlane32_swap(dn, dn, false, false)[1]) + 1e-6f);
        const f32x16 o = nm[j] * rd;
        O[2 * pr + j][0] = (u32x4){pk2(o[0], o[1]), pk2(o[2], o[3]), pk2(o[4], o[5]), pk2(o[6], o[7])};
        O[2 * pr + j][1] = (u32x4){pk2(o[8], o[9]), pk2(o[10], o[11]), pk2(o[12], o[13]) & km, pk2(o[14], o[15]) & km};
      }
    }
#pragma unroll
    for (int a = 0; a < 2; ++a) {
      f32x16 as[2] = {z16, z16};
#pragma unroll
      for (int n = 0; n < 4; ++n)
#pragma unroll
        for (int sg = 0; sg < 2; ++sg) { as[0] = mfma(WO[n][sg][0], asbf(O[4 * a + n][sg]), as[0]); as[1] = mfma(WO[n][sg][1], asbf(O[4 * a + n][sg]), as[1]); }
      PUT_TILES(lo_ + a * 8192, as);
    }
    TSW(17, 0);
  } else {
  const int cw = w - 4, wl = cw & 1, wh = cw >> 1;
  __builtin_amdgcn_s_setprio(PRIO_C);
  bf16x8 ones = asbf((u32x4){0x3f803f80u, 0x3f803f80u, 0x3f803f80u, 0x3f803f80u});
  PIN(ones);
  {
    {
      u64 ep;
      asm volatile("s_load_dwordx2 %0, %1, 0x0\n\ts_waitcnt lgkmcnt(0) vmcnt(0)" : "=&s"(ep) : "s"(ws) : "memory");
      tag.x = (unsigned)ep + 1u; tag.y = (unsigned)(ep >> 32) + 1u;
    }
    if (cw == 0) { asm volatile("" :: "v"(cpf)); *(volatile LDS u32x2*)SY_TAG = tag; }
    if (TOUCH && cw == 2) asm volatile("" :: "v"(tch[0]), "v"(tch[1]));
    if (cw == 3) {
      float sq[2] = {0.f, 0.f}, sm[2] = {0.f, 0.f};
      const bf16x2 one2 = __builtin_bit_cast(bf16x2, 0x3f803f80u);
#pragma unroll
      for (int k = 0; k < 7; ++k) {
        PIN(xs[k]);
#pragma unroll
        for (int e = 0; e < 4; ++e) {
          const unsigned u = xs[k][e];
          const bf16x2 v = __builtin_bit_cast(bf16x2, u);
          sq[e & 1] = __builtin_amdgcn_fdot2_f32_bf16(v, v, sq[e & 1], false); sm[e & 1] = __builtin_amdgcn_fdot2_f32_bf16(v, one2, sm[e & 1], false);
        }
      }
      const float m = sum8(sm[0] + sm[1]) * (1.f / D);
      const float var = fmaxf(sum8(sq[0] + sq[1]) * (1.f / D) - m * m, 0.f);
      const u32x2 gr = {pk2(-m, __builtin_amdgcn_sqrtf(rbf(rbf(var) + 1e-5f))), tag.x};
      if ((lane & 7) == 0) ST2(gr, (unsigned)lane, rws, WS_ST + (r * 128 + c * 8) * 8, "sc1");
    }
  }
  const unsigned fl_off = WS_FL + ((r * 2 + wl) * 32 + 2 * c + wh) * 8;
  const unsigned fa = SY_FULL + p * 4;
#define CHECK_FULL(v, need) do { if (__builtin_expect(__any((v) < (need)), 0)) lds_wait(SY_FULL, 4, need, lane); FENCE; } while (0)
  const bool do_cs = !(r & 1) && r != 14;
  unsigned da = SY_DONE + cw * 4;
  PIN(da);
  const unsigned xm = hh ? 0u : ~0u;
  if (!kvt) {
    const unsigned xa = (wl * 64 + l31) * 128 + ((hh ^ ((l31 >> 1) & 7)) << 4);
    const unsigned wr = 16384 + (8 * hh + q) * 128 + blk * 32 + p * 8;
    const unsigned wa0 = wr + wh * 8192 + ((wh ^ (q >> 1)) << 6), wa1 = wr + wh * 16384 + ((wh ^ 1 ^ (q >> 1)) << 6);
    f32x16 acc[2][3] = {{z16, z16, z16}, {z16, z16, z16}};
    bf16x8 X[4][2], W[4][3];
#define RDA(n, ks) do { \
      if ((n) == 0 || (n) == 2) X[ks][(n) >> 1] = ld_frag(ax[ks] + ((n) >> 1) * 4096); \
      else { const unsigned a_ = ((n) == 3 ? a1_ : a0_) + ((n) == 4 ? 8192 : 0) + (ks) * 2048; W[ks][(n) == 1 ? 0 : (n) - 2] = tr_frag(a_, a_ + 512); } } while (0)
#define MMA(ks, m) acc[(m) & 1][(m) >> 1] = mfma(W[ks][(m) >> 1], X[ks][(m) & 1], acc[(m) & 1][(m) >> 1])
    unsigned ax[4] = {xa, xa ^ 32u, xa ^ 64u, xa ^ 96u}, a0_ = wa0, a1_ = wa1;
#pragma unroll
    for (int n = 0; n < 6; ++n) PIN(acc[n & 1][n >> 1]);
    lds_wait(SY_FULL, 4, 1, lane);
    TSW(6, 4); TSC(24, 4);
    if (do_cs) {
#pragma unroll 1
      for (int b = wl; b < 3; b += 2) {
        const unsigned a = b == 1 ? wa1 : wa0 + b * 4096;
        f32x16 cs = z16;
#pragma unroll
        for (int ks = 0; ks < 4; ++ks) cs = mfma(ones, tr_frag(a + ks * 2048, a + ks * 2048 + 512), cs);
        const u32x2 gr = {__float_as_uint(cs[0]), tag.x};
        if (lane < 32) ST2(gr, v8, rws, WS_CS + (c * 7 + s0) * 2048 + (3 * wh + b) * 256, "sc1");
      }
    }
#pragma unroll
    for (int n = 0; n < 10; ++n) { RDA(n % 5, n / 5); SB; }
    unsigned tv = 1; int ts = 0;
    PIN(tv);
#pragma unroll 1
    do {
      unsigned fv; int dl;
#pragma unroll
      for (int m = 0; m < 12; ++m) {
        MMA(m / 6, m % 6); SB;
        if (m % 6 < 5) RDA(m % 6, 2 + m / 6);
        if (m == 0) { dl = ((0x24 >> ts) & 1) ? -81920 : 40960; ++ts; asm volatile("" : "+s"(dl), "+s"(ts)); }
        if (m == 2) { ax[0] += dl; ax[1] += dl; PIN(ax[0]); PIN(ax[1]); }
        if (m == 5) fv = *(volatile LDS unsigned*)fa;
        if (m == 10) *(volatile LDS unsigned*)da = tv;
        if (m == 11) { a0_ += dl; a1_ += dl; ++tv; CHECK_FULL(fv, tv); TSW(55 + ts, STW); }
        SB;
      }
#pragma unroll
      for (int m = 0; m < 12; ++m) {
        MMA(2 + m / 6, m % 6); SB;
        if (m % 6 < 5) RDA(m % 6, m / 6);
        if (m == 5) { ax[2] += dl; ax[3] += dl; }
        SB;
      }
    } while (ts < 7);
    TSW(7, 4); TSC(25, 4);
    lds_wait(SY_AUX, 2, 1, lane);
    unsigned st[2];
#pragma unroll
    for (int a = 0; a < 2; ++a) st[a] = *(LDS const unsigned*)(L_ST + (wl * 64 + a * 32 + l31) * 4);
#pragma unroll
    for (int b = 0; b < 3; ++b) {
      const u32x2 cv = *(LDS const u32x2*)(L_CS + ((3 * wh + b) * 32 + l31) * 8);
#pragma unroll
      for (int a = 0; a < 2; ++a)
        acc[a][b] = mfma(asbf((u32x4){cv.x, cv.y, cv.x, cv.y}), asbf((u32x4){((st[a] & 0xffffu) | (st[a] << 16)) & xm, st[a] & xm, 0u, 0u}), acc[a][b]);
    }
    const int R0 = r * 4 + wl * 2;
    asm volatile("s_nop 11" : "+v"(acc[0][0]), "+v"(acc[1][0]), "+v"(acc[0][1]), "+v"(acc[1][1]), "+v"(acc[0][2]), "+v"(acc[1][2]));
    TSW(18, 4);
    if (2 * c + wh < 19) {
#pragma unroll
      for (int b = 0; b < 3; ++b)
#pragma unroll
        for (int a = 0; a < 2; ++a) {
          const unsigned ho = WS_H + ((R0 + a) * 112 + 12 * c + 6 * wh + 2 * b) * 1024u;
#pragma unroll
          for (int sg = 0; sg < 2; ++sg) {
            u32x4 o;
#pragma unroll
            for (int e = 0; e < 4; ++e) {
              const float t0 = acc[a][b][8 * sg + 2 * e], t1 = acc[a][b][8 * sg + 2 * e + 1];
              o[e] = relu2(pk2(sqs(t0), sqs(t1)));
            }
            if (b < 2 || c != 9) ST4(o, v16, rws, ho + sg * 1024u, "sc1");
          }
        }
    } else {
      const int g0 = c == 9 ? 22 : (c - 10) * 12 + wh * 6;
#pragma unroll
      for (int a = 0; a < 2; ++a) {
        const float rs = __builtin_amdgcn_rcpf(bfhi(st[a]));
#pragma unroll
        for (int b = 0; b < 3; ++b)
#pragma unroll
          for (int sg = 0; sg < 2; ++sg) {
            u32x4 o;
#pragma unroll
            for (int e = 0; e < 4; ++e) o[e] = phi2((f32x2){acc[a][b][8 * sg + 2 * e], acc[a][b][8 * sg + 2 * e + 1]}, (f32x2){rs, rs});
            if (b > 0 || c != 9) ST4(o, v16, rws, WS_PQ + ((R0 + a) * 28 + g0 + 2 * b + sg) * 1024u, "sc1");
          }
      }
    }
  } else {
    const unsigned xa = l31 * 128 + ((hh ^ ((l31 >> 1) & 7)) << 4);
    const int ok_ = (c & 1) * 32 + 56 * cw + 8 * ((4 * blk + p + cw) & 7), ov_ = (c & 1) * 32 + 56 * cw + 32 * blk + 8 * p;
    const unsigned ka = 16384 + hh * 1024 + q * 256 + ((((ok_ >> 6) & 3) ^ q) << 6) + (ok_ & 63);
    const unsigned va = 32768 + hh * 1024 + q * 256 + ((((ov_ >> 6) & 3) ^ q) << 6) + (ov_ & 63);
    f32x16 acc[4][2] = {{z16, z16}, {z16, z16}, {z16, z16}, {z16, z16}};
    bf16x8 X[4][4], W[4][2];
#define RDK(n, ks) do { \
      if ((n) == 1 || (n) == 5) { const unsigned a_ = ((n) == 1 ? a0_ : a1_) + (ks) * 2048; W[ks][(n) >> 2] = tr_frag(a_, a_ + 8192); } \
      else X[ks][(n) ? (n) - 1 : 0] = ld_frag(ax[ks] + ((n) ? (n) - 1 : 0) * 4096); } while (0)
#define MMK(ks, m) acc[(m) & 3][(m) >> 2] = mfma(X[ks][(m) & 3], W[ks][(m) >> 2], acc[(m) & 3][(m) >> 2])
    unsigned ax[4] = {xa, xa ^ 32u, xa ^ 64u, xa ^ 96u}, a0_ = ka, a1_ = va;
#pragma unroll
    for (int n = 0; n < 8; ++n) PIN(acc[n & 3][n >> 2]);
    lds_wait(SY_FULL, 4, 1, lane);
    if (do_cs) {
#pragma unroll 1
      for (int b = 0; b < 2; ++b) {
        const unsigned a = b ? va : ka;
        f32x16 cs = z16;
#pragma unroll
        for (int ks = 0; ks < 4; ++ks) cs = mfma(ones, tr_frag(a + ks * 2048, a + ks * 2048 + 8192), cs);
        const u32x2 gr = {__float_as_uint(cs[0]), tag.x};
        if (lane < 32) ST2(gr, v8, rws, WS_CS + (c * 7 + s0) * 2048 + (2 * cw + b) * 256, "sc1");
      }
    }
#pragma unroll
    for (int n = 0; n < 12; ++n) { RDK(n % 6, n / 6); SB; }
    unsigned tv = 1; int ts = 0;
    PIN(tv);
#pragma unroll 1
    do {
      unsigned fv; int dl;
#pragma unroll
      for (int m = 0; m < 16; ++m) {
        MMK(m >> 3, m & 7); SB;
        if ((m & 7) >= 1 && (m & 7) < 7) RDK((m & 7) - 1, 2 + (m >> 3));
        if (m == 0) { dl = ((0x24 >> ts) & 1) ? -98304 : 49152; ++ts; asm volatile("" : "+s"(dl), "+s"(ts)); }
        if (m == 7) { ax[0] += dl; ax[1] += dl; PIN(ax[0]); PIN(ax[1]); }
        if (m == 8) fv = *(volatile LDS unsigned*)fa;
        if (m == 14) *(volatile LDS unsigned*)da = tv;
        if (m == 15) { a0_ += dl; a1_ += dl; ++tv; CHECK_FULL(fv, tv); TSW(55 + ts, STW); }
        SB;
      }
#pragma unroll
      for (int m = 0; m < 16; ++m) {
        MMK(2 + (m >> 3), m & 7); SB;
        if ((m & 7) >= 1 && (m & 7) < 7) RDK((m & 7) - 1, m >> 3);
        if (m == 7) { ax[2] += dl; ax[3] += dl; }
        SB;
      }
    } while (ts < 7);
    TSW(8, 4);
    lds_wait(SY_AUX, 2, 1, lane);
    {
      u32x2 cv[2];
#pragma unroll
      for (int b = 0; b < 2; ++b) cv[b] = *(LDS const u32x2*)(L_CS + ((2 * cw + b) * 32 + l31) * 8);
#pragma unroll
      for (int a = 0; a < 4; ++a) {
        const unsigned st = *(LDS const unsigned*)(L_ST + (a * 32 + l31) * 4);
#pragma unroll
        for (int b = 0; b < 2; ++b)
          acc[a][b] = mfma(asbf((u32x4){((st & 0xffffu) | (st << 16)) & xm, st & xm, 0u, 0u}), asbf((u32x4){cv[b].x, cv[b].y, cv[b].x, cv[b].y}), acc[a][b]);
      }
    }
    f32x16 kv = z16;
    const unsigned om = l31 == 28 ? ~0u : 0u;
    f32x4 rsv[16];
#pragma unroll
    for (int n = 0; n < 16; ++n) rsv[n] = *(LDS const f32x4*)(L_RS + (8 * n + 4 * hh) * 4);
    SB;
#pragma unroll
    for (int a = 0; a < 4; ++a)
#pragma unroll
      for (int sg = 0; sg < 2; ++sg) {
        u32x4 fk, fv;
#pragma unroll
        for (int j = 0; j < 4; ++j) {
          const f32x4 r4 = rsv[a * 4 + sg * 2 + (j >> 1)];
          const f32x2 rs = (j & 1) ? (f32x2){r4[2], r4[3]} : (f32x2){r4[0], r4[1]};
          const int e = 8 * sg + 2 * j;
          fk[j] = phi2((f32x2){acc[a][0][e], acc[a][0][e + 1]}, rs);
          const f32x2 vv = (f32x2){acc[a][1][e], acc[a][1][e + 1]} * rs;
          fv[j] = (pk2(vv.x, vv.y) & ~om) | (0x3f803f80u & om);
        }
        kv = mfma(asbf(fk), asbf(fv), kv);
      }
    asm volatile("s_nop 11" : "+v"(kv));
    const unsigned ko = WS_KVP + (((c - 12) * 4 + cw) * 16 + r) * 4096u;
#pragma unroll
    for (int e = 0; e < 4; ++e) {
      const f32x4 o = {kv[4 * e], kv[4 * e + 1], kv[4 * e + 2], kv[4 * e + 3]};
      ST4(o, v16, rws, ko + e * 1024u, "sc1");
    }
  }
  lds_put(da, 8);
  TSW(22, 4);
  WAIT_VM(0);
  if (lane == 0) ST2(tag, 0u + lane, rws, fl_off, "sc1");
  TSW(9, 4);

  if (pure) {
    const int id = xc * 4 + (ix >> 3), hd = id >> 1;
    tag_wait((unsigned)(((4 * cw + p) * 2 + (hd & 1)) * 32 + 24 + 2 * (hd >> 2) + ((hd >> 1) & 1)) * 8u, rws, WS_FL, ~0ull, tag);
    u32x4 pr[8];
#pragma unroll
    for (int n = 0; n < 8; ++n) LD4(pr[n], v16, rws, WS_KVP + (hd * 16 + 4 * cw + (n >> 1)) * 4096u + (id & 1) * 2048u + (n & 1) * 1024u, "sc1");
    WAIT_VM(0);
    f32x4 a = {0.f, 0.f, 0.f, 0.f}, b = a;
#pragma unroll
    for (int n = 0; n < 4; ++n) { PIN(pr[2 * n]); PIN(pr[2 * n + 1]); a += __builtin_bit_cast(f32x4, pr[2 * n]); b += __builtin_bit_cast(f32x4, pr[2 * n + 1]); }
    *(LDS f32x4*)(147456 + cw * 2048 + v16) = a; *(LDS f32x4*)(147456 + cw * 2048 + 1024 + v16) = b;
    BARRIER;
    if (cw == 0) {
#pragma unroll
      for (int n = 1; n < 4; ++n) { a += *(LDS const f32x4*)(147456 + n * 2048 + v16); b += *(LDS const f32x4*)(147456 + n * 2048 + 1024 + v16); }
      u32x4 o = {pk2(a[0], a[1]), pk2(a[2], a[3]), pk2(b[0], b[1]), pk2(b[2], b[3])};
      if ((id & 1) && hh != (hd & 1)) { if (hd & 2) { o.x = 0; o.y = 0; } else { o.z = 0; o.w = 0; } }
      ST4(o, v16, rws, WS_KV + id * 1024u, "sc1");
      WAIT_VM(0);
      if (lane == 0) ST2(tag, 0u + lane, rws, WS_KF + id * 8u, "sc1");
      TSW(10, 4); TS_DUMP;
    } else if (cw == 1 && bid == 255) {
#pragma unroll 1
      for (int n = 0; n < 4; ++n) tag_wait((unsigned)((lane >> 4) * 64 + (lane & 15) * 2) * 8u, rws, WS_FL + n * 2048u, ~0ull, tag);
      if (lane == 0) ST2(tag, 0u + lane, rws, WS_EP, "sc1");
    }
    return;
  }

  {
    const int kh = wh, R = rt * 2 + wl;
    if (cw == 0) { const u64 bl = tag_wait(l31 * 8u, rws, WS_FL + rt * 256u, 0x7FFFFull, tag); lds_put(SY_HOK, (bl & 0xF80000ull) == 0xF80000ull ? 2 : 1); }
    else lds_wait(SY_HOK, 1, 1, lane);
    TSW(12, 4);
    const unsigned hs = WS_H + (R * 112 + kh * 56) * 1024u;
    constexpr int NH = NHW, E = 28 - NH;
    u32x4 hf[NH][2];
#define HLD(u) do { int k_ = (u) + rot2; k_ = k_ >= 28 ? k_ - 28 : k_; const unsigned o_ = hs + k_ * 2048u; \
      LD4(hf[(u) % NH][0], v16, rws, o_, "sc1"); LD4(hf[(u) % NH][1], v16, rws, o_, "offset:1024 sc1"); } while (0)
#pragma unroll
    for (int u = 0; u < NH; ++u) HLD(u);
    unsigned ra[3][2];
#pragma unroll
    for (int cb = 0; cb < 2; ++cb) {
      ra[0][cb] = kh * 4096 + (4 * hh + q) * 128 + ((cb ^ (q >> 1)) << 6) + blk * 32 + p * 8; ra[1][cb] = ra[0][cb] + 65536; ra[2][cb] = ra[0][cb] + 131072;
      PIN(ra[0][cb]); PIN(ra[1][cb]); PIN(ra[2][cb]);
    }
    f32x16 af[2] = {z16, z16};
    bf16x8 W[2][4];
#define RDW(buf, n, reg) do { const unsigned a_ = ra[(reg) >> 3][(n) & 1] + ((reg) & 7) * 8192 + ((n) >> 1) * 2048; W[buf][n] = tr_frag(a_, a_ + 1024); } while (0)
    lds_wait(SY_RDY, 4, 1, lane);
    TSW(13, 4); TSC(26, 4);
#pragma unroll
    for (int n = 0; n < 4; ++n) { RDW(0, n, 0); SB; }
#pragma unroll
    for (int u = 0; u < 28; ++u) {
      constexpr auto total = [](int u_) { return 2 * NH + 2 * (u_ < E ? u_ : E) + (u_ > E ? 3 : 0); };
      asm volatile("s_waitcnt vmcnt(%2)" : "+v"(hf[u % NH][0]), "+v"(hf[u % NH][1]) : "n"(total(u) - 2 - 2 * u) : "memory");
      if (u == 18) lds_wait(SY_FULL2, 4, 1, lane);
#pragma unroll
      for (int n = 0; n < 4; ++n) {
        af[n & 1] = mfma(W[u & 1][n], asbf(hf[u % NH][n >> 1]), af[n & 1]); SB;
        if (u < 27) RDW((u + 1) & 1, n, (u + 1) % 19);
        SB;
      }
      if (u == 7) lds_put(SY_DONE2 + cw * 4, 1);
      if (u == 15) lds_put(SY_DONE2 + cw * 4, 2);
      if (u == 21) lds_put(SY_DONE2 + cw * 4, 3);
      if (u < E) HLD(u + NH);
      else if (u == E) FIN_LD;
    }
    TSW(14, 4); TSC(27, 4);
    lds_wait(SY_DONE2, 4, 3, lane);
    PUT_TILES(cw * 8192, af);
    WAIT_VM(0);
  }
  }
  BARRIER;
  TSW(15, 4);
  {
    const unsigned a0 = (tid >> 8) * 8192 + (tid & 7) * 1024 + ((((tid >> 3) & 31) ^ ((tid & 7) * 2)) << 4);
    f32x4 f[2][2], sp[4][2];
#pragma unroll
    for (int n = 0; n < 2; ++n) { f[n][0] = *(LDS const f32x4*)(a0 + n * 16384); f[n][1] = *(LDS const f32x4*)(a0 + n * 16384 + 512); }
#pragma unroll
    for (int n = 0; n < 4; ++n) { sp[n][0] = *(LDS const f32x4*)(a0 + 73728 + n * 16384); sp[n][1] = *(LDS const f32x4*)(a0 + 73728 + n * 16384 + 512); }
    SB;
    PIN(xr); PIN(br); PIN(sg_);
    const float rs = __builtin_amdgcn_rcpf(bfhi(sg_)), rs2 = rs * rs;
    u32x4 ov;
#pragma unroll
    for (int e = 0; e < 4; ++e) {
      const int hf_ = e >> 1, j = (e & 1) * 2;
      const float t0 = fmaf(f[0][hf_][j] + f[1][hf_][j], rs2, bflo(br[e])), t1 = fmaf(f[0][hf_][j + 1] + f[1][hf_][j + 1], rs2, bfhi(br[e]));
      const float g0 = t0 * __builtin_amdgcn_rcpf(1.f + __expf(-t0)), g1 = t1 * __builtin_amdgcn_rcpf(1.f + __expf(-t1));
      const float s0_ = (sp[0][hf_][j] + sp[1][hf_][j]) + (sp[2][hf_][j] + sp[3][hf_][j]);
      const float s1_ = (sp[0][hf_][j + 1] + sp[1][hf_][j + 1]) + (sp[2][hf_][j + 1] + sp[3][hf_][j + 1]);
      ov[e] = pk2(fmaf(s0_, g0, bflo(xr[e])), fmaf(s1_, g1, bfhi(xr[e])));
    }
    *(u32x4*)((char*)out + fxo) = ov;
    TSW(11, 4);
    if (w == 4) TS_DUMP;
  }
}

extern "C" void kernel_launch(void* const* d_in, const int* in_sizes, int n_in, void* d_out, int out_size, void* d_ws,
                              size_t ws_size, hipStream_t stream) {
  (void)hipFuncSetAttribute((const void*)fused, hipFuncAttributeMaxDynamicSharedMemorySize, LDS_BYTES);
  fused<<<256, 512, LDS_BYTES, stream>>>((const u16*)d_in[0], (const u16*)d_in[1], (const u16*)d_in[4], (char*)d_ws, (const u16*)d_in[2],
                                         (const u16*)d_in[5], (const u16*)d_in[6], (const u16*)d_in[7], (u16*)d_out);
}
```

```cpp
#include <hip/hip_runtime.h>
#include <hip/hip_bf16.h>
#include <stdint.h>
#pragma clang diagnostic ignored "-Wint-to-pointer-cast"

#define HMOD "sc1"
#define PRIO_H 3
#define PRIO_C 2
#define LSLEEP 1
#define STW 99
#define PF0 2
#define PFN 0
#define FILLAT 8
#define THR_N 0
#define THR_V 4
#define NHW 7
#define TSLEEP 4
#define TOUCH 0
#define S0MODE 1
#define FS 128u
#define POLL_LATE 0
#define TCH2 5
#define PRIO_S 3
#define RSLEEP 4
#define NX 9
#define PFA 9
#define PRIO_P 0
#define PRIO_W 0
#define EARLY 0
#define CTROT 1
#define ROTM 1
#define LA 2
#define NOKV 0
#define PRIO_M 0
typedef unsigned short u16;
typedef unsigned long long u64;
typedef __bf16 bf16x8 __attribute__((ext_vector_type(8)));
typedef __bf16 bf16x2 __attribute__((ext_vector_type(2)));
typedef short s16x4 __attribute__((ext_vector_type(4)));
typedef float f32x4 __attribute__((ext_vector_type(4)));
typedef float f32x16 __attribute__((ext_vector_type(16)));
typedef float f32x2 __attribute__((ext_vector_type(2)));
typedef unsigned u32x4 __attribute__((ext_vector_type(4)));
typedef unsigned u32x2 __attribute__((ext_vector_type(2)));
#define LDS __attribute__((address_space(3)))
#define DI __device__ __forceinline__

constexpr int S = 2048, D = 448, F = 1792;
constexpr unsigned WS_EP = 0;
constexpr unsigned WS_KF = 1024;
constexpr unsigned WS_FL = WS_KF + 256 * FS;
constexpr unsigned WS_ST = WS_FL + 1024 * FS;
constexpr unsigned WS_CS = WS_ST + 16384;
constexpr unsigned WS_KV = WS_CS + 16 * 7 * 2048;
constexpr unsigned WS_KVP = WS_KV + 32768;
constexpr unsigned WS_PQ = WS_KVP + 16 * 16 * 4096;
constexpr unsigned WS_H = WS_PQ + 64 * 28 * 1024;
constexpr unsigned WS_END = WS_H + 64 * 112 * 1024;
constexpr unsigned L_CS = 155648, L_ST = L_CS + 2048, L_RS = L_ST + 512, L_SY = L_RS + 512, L_END = L_SY + 256;
constexpr unsigned SY_FULL = L_SY, SY_AUX = L_SY + 16, SY_DONE = L_SY + 32, SY_RDY = L_SY + 48, SY_FULL2 = L_SY + 64, SY_DONE2 = L_SY + 80,
                   SY_GO = L_SY + 112, SY_WOK = L_SY + 128, SY_KOK = L_SY + 96, SY_HOK = L_SY + 100, SY_TAG = L_SY + 104;
constexpr unsigned CODE_BYTES = 32768;
constexpr unsigned SPIN_CAP = 1u << 17;

#define LDS_BYTES L_END
#define TSW(n, wv)
#define TSC(n, wv)
#define TS_INIT
#define TS_DUMP

DI u32x4 mk_rsrc(const void* p, unsigned bytes) {
  uint64_t a = (uint64_t)p;
  u32x4 r; r.x = (unsigned)a; r.y = (unsigned)(a >> 32) & 0xffffu; r.z = bytes; r.w = 0x00020000u; return r;
}
DI void dma16(unsigned lds_addr, unsigned voff, u32x4 rsrc, unsigned soff) {
  asm volatile("s_mov_b32 m0, %0\n\ts_nop 0\n\tbuffer_load_dwordx4 %1, %2, %3 offen lds"
               :: "s"(lds_addr), "v"(voff), "s"(rsrc), "s"(soff) : "memory");
}
template <unsigned OFF> DI void dma16o(unsigned lds_base, unsigned voff, u32x4 rsrc, unsigned soff) {
  asm volatile("s_add_u32 m0, %0, %4\n\ts_nop 0\n\tbuffer_load_dwordx4 %1, %2, %3 offen lds"
               :: "s"(lds_base), "v"(voff), "s"(rsrc), "s"(soff), "n"(OFF) : "memory", "scc");
}
#define SB __builtin_amdgcn_sched_barrier(0)
#define FENCE asm volatile("" ::: "memory")
#define PIN(a) asm volatile("" : "+v"(a))
#define WAIT_VM(n) asm volatile("s_waitcnt vmcnt(%0)" :: "n"(n) : "memory")
#define WAIT_LDS asm volatile("s_waitcnt lgkmcnt(0)" ::: "memory")
#define BARRIER asm volatile("s_waitcnt lgkmcnt(0)\n\ts_barrier" ::: "memory")
#define LD4(dst, voff, rsrc, soff, mod) asm volatile("buffer_load_dwordx4 %0, %1, %2, %3 offen " mod : "=v"(dst) : "v"(voff), "s"(rsrc), "s"(soff) : "memory")
#define LD2(dst, voff, rsrc, soff, mod) asm volatile("buffer_load_dwordx2 %0, %1, %2, %3 offen " mod : "=v"(dst) : "v"(voff), "s"(rsrc), "s"(soff) : "memory")
#define LD1(dst, voff, rsrc, soff, mod) asm volatile("buffer_load_dword %0, %1, %2, %3 offen " mod : "=v"(dst) : "v"(voff), "s"(rsrc), "s"(soff) : "memory")
#define ST4(src, voff, rsrc, soff, mod) asm volatile("buffer_store_dwordx4 %0, %1, %2, %3 offen " mod "\n\ts_nop 1" :: "v"(src), "v"(voff), "s"(rsrc), "s"(soff) : "memory")
#define ST2(src, voff, rsrc, soff, mod) asm volatile("buffer_store_dwordx2 %0, %1, %2, %3 offen " mod :: "v"(src), "v"(voff), "s"(rsrc), "s"(soff) : "memory")
DI f32x4 mfma16(bf16x8 a, bf16x8 b, f32x4 c) { return __builtin_amdgcn_mfma_f32_16x16x32_bf16(a, b, c, 0, 0, 0); }
DI f32x16 mfma(bf16x8 a, bf16x8 b, f32x16 c) { return __builtin_amdgcn_mfma_f32_32x32x16_bf16(a, b, c, 0, 0, 0); }
DI bf16x8 asbf(u32x4 v) { return __builtin_bit_cast(bf16x8, v); }
DI bf16x8 ld_frag(unsigned a) { return *(LDS const bf16x8*)a; }
DI bf16x8 tr_frag(unsigned a0, unsigned a1) {
  s16x4 lo = __builtin_amdgcn_ds_read_tr16_b64_v4i16((LDS s16x4*)a0);
  s16x4 hi = __builtin_amdgcn_ds_read_tr16_b64_v4i16((LDS s16x4*)a1);
  return __builtin_bit_cast(bf16x8, __builtin_shufflevector(lo, hi, 0, 1, 2, 3, 4, 5, 6, 7));
}
DI unsigned pk2(float a, float b) { f32x2 v = {a, b}; return __builtin_bit_cast(unsigned, __builtin_convertvector(v, bf16x2)); }
DI float bflo(unsigned u) { return __uint_as_float(u << 16); }
DI float bfhi(unsigned u) { return __uint_as_float(u & 0xffff0000u); }
DI float rbf(float f) { return bflo(pk2(f, 0.f)); }
DI f32x2 mul_clamp(f32x2 a, f32x2 b) { f32x2 r; asm("v_pk_mul_f32 %0, %1, %2 clamp" : "=v"(r) : "v"(a), "v"(b)); return r; }
constexpr float DN = 0x1p-40f, UP = 0x1p40f, LOG2E = 1.44269504f;
#define PHI_A(u) f32x2 pm[4], pe[4]; _Pragma("unroll") for (int j_ = 0; j_ < 4; ++j_) pm[j_] = mul_clamp(u[j_], (f32x2){DN, DN}); SB
#define PHI_E(j0) _Pragma("unroll") for (int j_ = (j0); j_ < (j0) + 2; ++j_) pe[j_] = (f32x2){__builtin_amdgcn_fmed3f(__builtin_amdgcn_exp2f(u[j_].x), 0.f, 1.f), \
                                                                                     __builtin_amdgcn_fmed3f(__builtin_amdgcn_exp2f(u[j_].y), 0.f, 1.f)}; SB
#define PHI_S _Pragma("unroll") for (int j_ = 0; j_ < 4; ++j_) pm[j_] = __builtin_elementwise_fma(pm[j_], (f32x2){UP / LOG2E, UP / LOG2E}, (f32x2){1e-6f, 1e-6f}); SB; \
              _Pragma("unroll") for (int j_ = 0; j_ < 4; ++j_) pm[j_] += pe[j_]; SB
#define PHI_B(o) PHI_S; const u32x4 o = {pk2(pm[0].x, pm[0].y), pk2(pm[1].x, pm[1].y), pk2(pm[2].x, pm[2].y), pk2(pm[3].x, pm[3].y)}
DI unsigned relu2(unsigned v) {
  typedef short s16x2 __attribute__((ext_vector_type(2)));
  return __builtin_bit_cast(unsigned, __builtin_elementwise_max(__builtin_bit_cast(s16x2, v), (s16x2){0, 0}));
}
DI float mul1(float a, float b) { float r; asm("v_mul_f32_e32 %0, %1, %2" : "=v"(r) : "v"(a), "v"(b)); return r; }
DI float sum8(float v) {
  v += __uint_as_float(__builtin_amdgcn_update_dpp(0u, __float_as_uint(v), 0xB1, 0xf, 0xf, true));
  v += __uint_as_float(__builtin_amdgcn_update_dpp(0u, __float_as_uint(v), 0x4E, 0xf, 0xf, true));
  v += __uint_as_float(__builtin_amdgcn_update_dpp(0u, __float_as_uint(v), 0x141, 0xf, 0xf, true));
  return v;
}
DI void lds_put(unsigned a, unsigned v) { FENCE; *(volatile LDS unsigned*)a = v; FENCE; }
DI void lds_wait(unsigned a, int n, unsigned need, int lane) {
  const unsigned ad = a + (lane & (n - 1)) * 4;
#pragma unroll 1
  for (unsigned sp = 0; sp < SPIN_CAP; ++sp) {
    FENCE; const unsigned v = *(volatile LDS unsigned*)ad; FENCE;
    if (__all(v >= need)) break;
    __builtin_amdgcn_s_sleep(LSLEEP);
  }
}
DI u64 tag_wait(unsigned voff, u32x4 rs, unsigned soff, u64 mask, u32x2 tag) {
  u64 bl = 0;
#pragma unroll 1
  for (unsigned sp = 0; sp < SPIN_CAP; ++sp) {
    u32x2 v;
    asm volatile("buffer_load_dwordx2 %0, %1, %2, %3 offen sc1\n\ts_waitcnt vmcnt(0)" : "=&v"(v) : "v"(voff), "s"(rs), "s"(soff) : "memory");
    bl = __ballot(v.x == tag.x && v.y == tag.y);
    if ((bl & mask) == mask) break;
    __builtin_amdgcn_s_sleep(TSLEEP);
  }
  return bl;
}

__global__ __launch_bounds__(512, 2) void fused(const u16* __restrict__ x, const u16* __restrict__ wqkv, const u16* __restrict__ w1, char* ws,
                                                const u16* __restrict__ wo, const u16* __restrict__ b1, const u16* __restrict__ w2,
                                                const u16* __restrict__ b2, u16* __restrict__ out) {
  const int bid = blockIdx.x, tid = threadIdx.x, lane = tid & 63;
  unsigned cpf;
  {
    u64 pc; asm volatile("s_getpc_b64 %0" : "=s"(pc));
    const u32x4 rc = mk_rsrc((const void*)(pc & ~255ull), CODE_BYTES);
    if ((tid >> 3) == 32) LD1(cpf, (unsigned)((bid >> 3) + (tid & 7) * 32) * 128u, rc, 0u, "");
  }
  const int w = __builtin_amdgcn_readfirstlane(tid >> 6);
  const int xc = bid & 7, ix = bid >> 3, sl = ix & 7;
  const int r = (xc >> 1) * 4 + (ix >> 3);
  const int c = (int)(((xc & 1) ? 0xFEB98765u : 0xDCA43210u) >> (4 * sl)) & 15;
  const bool kvt = sl >= 6, pure = sl == 7;
  const int s0 = r >= 14 ? 0 : r >> 1;
  const int rt = xc * 4 + (ix >> 3), ct = CTROT ? (sl + 2 * (ix >> 3)) % 7 : sl, rot2 = ROTM ? (7 * (ix >> 3) + xc) % 28 : (xc * 7) >> 1;
  const int l31 = lane & 31, hh = lane >> 5, q = (lane >> 2) & 3, p = lane & 3, blk = (lane >> 4) & 1;
  const unsigned slot_b = kvt ? 49152u : 40960u;
  const u32x4 rws = mk_rsrc(ws, WS_END), rx = mk_rsrc(x, S * D * 2);
  const unsigned v16 = lane * 16, v8 = lane * 8;
  TS_INIT; TSW(0, 0);
  u32x2 tag;
  const unsigned swz = (unsigned)(((lane & 7) ^ ((lane >> 2) & 4)) << 4);
  f32x16 z16;
#pragma unroll
  for (int e = 0; e < 16; ++e) z16[e] = 0.f;

  u32x4 xs[7];
  unsigned tch[2];
  if (w >= 4) {
    if (w == 7) {
      const unsigned va = (unsigned)(r * 128 + c * 8 + (lane >> 3)) * 896u + (lane & 7) * 16;
#pragma unroll
      for (int k = 0; k < 7; ++k) LD4(xs[k], va, rx, k * 128u, "");
    }
    if (TOUCH && w == 6) {
      if (lane < 49) LD1(tch[0], lane * 128u, mk_rsrc(w1, D * F * 2), bid * 6272u, "");
      if (lane < 37) LD1(tch[1], lane * 128u, mk_rsrc(wqkv, D * 3 * D * 2), bid * 4736u, "");
    }
    *(LDS unsigned*)(L_SY + lane * 4) = (lane >> 1) == 1; BARRIER;
  }
  u32x4 xr, br; unsigned sg_;
  const unsigned fxo = (unsigned)(rt * 64 + (tid >> 3)) * 896u + ct * 128 + (tid & 7) * 16;
#define FIN_LD do { LD4(xr, fxo, rx, 0u, ""); LD4(br, (tid & 7) * 16u, mk_rsrc(b2, D * 2), ct * 128u, ""); LD1(sg_, (unsigned)(tid & ~7), rws, WS_ST + rt * 512u, "sc1"); } while (0)
#define PUT_TILES(base, t) do { _Pragma("unroll") for (int n_ = 0; n_ < 8; ++n_) *(LDS f32x4*)((base) + n_ * 1024 + (v16 ^ (n_ * 32))) = \
      (f32x4){t[n_ >> 2][4 * (n_ & 3)], t[n_ >> 2][4 * (n_ & 3) + 1], t[n_ >> 2][4 * (n_ & 3) + 2], t[n_ >> 2][4 * (n_ & 3) + 3]}; } while (0)
  if (__builtin_expect(w < 4, 1)) {
    const int h = w;
    const u32x4 rw2 = mk_rsrc(w2, F * D * 2), rwo = mk_rsrc(wo, D * D * 2);
    const unsigned vwo = (unsigned)(lane >> 3) * 896u + ct * 128 + swz;
    const unsigned vw2 = vwo + (unsigned)((h >> 1) * 896 + (h & 1) * 16) * 896u;
    const int hg = (h + xc + (ROTM ? (ix >> 3) : 0)) & 3;
    u32x4 R[18];
#define ROT(u) ((u) + rot2 >= 28 ? (u) + rot2 - 28 : (u) + rot2)
#define PFLD(n) LD4(R[n], vw2, rw2, ROT(19 + ((n) >> 1)) * 28672u + ((n) & 1) * 7168u, "")
    auto fill = [&](int u, int g, int cnt) {
#pragma unroll 1
      for (int e = u + cnt; u < e; ++u, ++g) {
        const unsigned k = ROT(u) * 28672u;
        dma16(g * 8192 + h * 2048, vw2, rw2, k); dma16(g * 8192 + h * 2048 + 1024, vw2, rw2, k + 7168u);
      }
    };
    {
      __builtin_amdgcn_s_setprio(PRIO_H);
      const int hp = h >> 1, hs_ = h & 1;
      const unsigned vx0 = (unsigned)(r * 128 + hs_ * 64 + (lane >> 3)) * 896u + (unsigned)(((lane & 7) ^ (lane >> 4)) << 4), vx1 = (vx0 ^ 64u) + 7168u;
      const bool a1 = c < 10, b1_ = c < 9;
      const u32x4 r0 = mk_rsrc(a1 ? w1 : wqkv, a1 ? D * F * 2 : D * 3 * D * 2), r1 = mk_rsrc(b1_ ? w1 : wqkv, b1_ ? D * F * 2 : D * 3 * D * 2);
      const unsigned p0 = a1 ? 3584u : 2688u, p1 = b1_ ? 3584u : 2688u;
      unsigned V0, V1, V2, V3;
      {
        const unsigned kl = 32 * hs_ + (lane >> 3);
        const unsigned c0 = (a1 ? c : c - 10) * 384, c1 = c == 9 ? 512 : c0;
        const unsigned vk = (32 * hs_ + (lane >> 4)) * 2688 + ((224 * c - 1792) & ~63) + ((q ^ (lane >> 4)) << 6) + p * 16;
        V0 = kvt ? vk : kl * p0 + c0 + swz;
        V1 = kvt ? vk + 4 * 2688 : kl * p1 + c1 + swz + 128;
        V2 = kvt ? V0 + 896 : V1 + 128;
        V3 = V1 + 896;
      }
      int sg = (S0MODE ? s0 : 0) + hp; sg = sg == 7 ? 0 : sg;
      unsigned so = hp * slot_b;
#define GROUP(n) do { const unsigned kk = sg * 64 + 8 * (n), sa = kk * p0, sb_ = kk * p1; \
          dma16o<(n) * 2048>(lx, vx0, rx, sg * 128 + (n) * 14336); \
          dma16o<16384 + (n) * 1024>(lw, V0, r0, sa); \
          dma16o<24576 + (n) * 1024>(lw, V1, r1, sb_); \
          dma16o<(n) * 2048 + 1024>(lx, vx1, rx, sg * 128 + (n) * 14336); \
          dma16o<32768 + (n) * 1024>(lw, V2, r1, sb_); \
          if (kvt) dma16o<40960 + (n) * 1024>(lw, V3, r1, sb_); } while (0)
      if (hp) BARRIER;
      unsigned t2;
#pragma unroll
      for (int i = 0; i < 4; ++i) {
        const int t = hp + 2 * i;
        if (i == 3 && hp) break;
        if (i >= 2 || (i == 1 && hp)) lds_wait(SY_DONE, 4, t - 2, lane);
        TSW(32 + t, 2 * hp);
        const unsigned lx = so + hs_ * 8192, lw = so + hs_ * 4096;
        GROUP(0); GROUP(1); GROUP(2); GROUP(3);
        TSW(40 + t, 2 * hp);
        if (i == 0 && !hp) BARRIER;
        if (i == 2 && hp) break;
        if (i == 3) lds_put(SY_GO, 1);
        if (i == 1) {
          LD1(t2, lane < (hp ? 0 : hs_ ? 13 : 49) ? lane * 128u : 0x80000000u, hs_ ? rwo : rw2, bid * (hs_ ? 1664u : 6272u), "");
          WAIT_VM(1);
        } else WAIT_VM(0);
        if (i == 2) asm volatile("" :: "v"(t2));
        lds_put(SY_FULL + h * 4, t + 2);
        TSW(48 + t, 2 * hp);
        if (!pure && ((i == 1 && hp) || i == 2)) {
#pragma unroll
          for (int n = 0; n < PFA; ++n) PFLD(n);
        }
        so = so == 0 ? 2 * slot_b : so - slot_b;
        sg = sg >= 5 ? sg - 5 : sg + 2;
      }
      tag = *(volatile LDS u32x2*)SY_TAG;
      if (hp) {
        u32x4 xf[8];
        unsigned bi;
        const bool chk = !hs_ || kvt || lane < 32;
        const u32x4 rb1 = mk_rsrc(b1, c < 10 ? F * 2 : 0);
#pragma unroll 1
        for (unsigned sp = 0; sp < SPIN_CAP; ++sp) {
#pragma unroll
          for (int k = 0; k < 7; ++k) LD4(xf[k], v16, rws, WS_CS + c * 14336u + hs_ * 1024u + k * 2048u, "sc1");
          LD4(xf[7], v16, rws, WS_ST + r * 1024u, "sc1");
          LD1(bi, lane * 4u, rb1, c * 384u + hs_ * 256u, "");
          WAIT_VM(9); lds_put(SY_FULL + h * 4, 7);
          TSW(53, 2);
          WAIT_VM(0);
          PIN(xf[0]); PIN(xf[7]); PIN(bi);
          if (kvt && l31 == 30) bi = 0x3f80u;
          unsigned bad = (xf[0].y ^ tag.x) | (xf[0].w ^ tag.x);
          float ax_ = __uint_as_float(xf[0].x), ay_ = __uint_as_float(xf[0].z);
#pragma unroll
          for (int k = 1; k < 7; ++k) {
            PIN(xf[k]);
            ax_ += __uint_as_float(xf[k].x); ay_ += __uint_as_float(xf[k].z); PIN(ax_); PIN(ay_);
            bad |= (xf[k].y ^ tag.x) | (xf[k].w ^ tag.x); PIN(bad);
          }
          const unsigned hi = pk2(ax_, ay_); ax_ -= bflo(hi); PIN(ax_); ay_ -= bfhi(hi);
          const unsigned mi = pk2(ax_, ay_); ax_ -= bflo(mi); PIN(ax_); ay_ -= bfhi(mi);
          const unsigned lo = pk2(ax_, ay_);
          *(LDS u32x4*)(L_CS + hs_ * 1024 + v16) = (u32x4){(hi & 0xffffu) | (mi << 16), (lo & 0xffffu) | (bi << 16),
                                                           (hi >> 16) | (mi & 0xffff0000u), (lo >> 16) | (bi & 0xffff0000u)};
          if (hs_) *(LDS u32x2*)(L_ST + v8) = (u32x2){xf[7].x, xf[7].z};
          else { float r0_ = __builtin_amdgcn_rcpf(bfhi(xf[7].x)) * LOG2E; PIN(r0_); *(LDS f32x2*)(L_RS + v8) = (f32x2){r0_, __builtin_amdgcn_rcpf(bfhi(xf[7].z)) * LOG2E}; }
          if (__builtin_expect(__all((bad == 0 || !chk) && xf[7].y == tag.x && xf[7].w == tag.x), 1)) break;
          __builtin_amdgcn_s_sleep(8);
        }
        lds_put(SY_FULL + h * 4, 8);
        TSW(3, 2);
        if (!pure) {
#pragma unroll
          for (int n = PFA; n < 18; ++n) PFLD(n);
        }
        lds_wait(SY_GO, 1, 1, lane);
        asm volatile("" :: "v"(t2));
      }
      __builtin_amdgcn_s_setprio(PRIO_P);
    }
    if (!pure) {
#pragma unroll
    for (int n = PFA; n < 18; ++n) { if (h < 2) PFLD(n); }
    const unsigned wb = 49152 + (h ^ 2) * 14336;
    lds_wait(SY_DONE, 4, h < 2 ? 6 : 5, lane);
#pragma unroll 1
    for (int n = 0; n < 14; ++n) dma16(wb + n * 1024, vwo, rwo, hg * 100352u + n * 7168u);
    lds_wait(SY_DONE, 4, 6, lane);
    fill(0, 16, 3);
    TSW(23, 0);
    WAIT_VM(6);
    bf16x8 WO[4][2][2];
    {
      const unsigned a0 = wb + (4 * hh + q) * 128 + (((q >> 1) & 1) << 6) + blk * 32 + p * 8, a1_ = a0 ^ 64u;
#pragma unroll
      for (int n = 0; n < 4; ++n)
#pragma unroll
        for (int sg = 0; sg < 2; ++sg) {
          const unsigned o = n * 3584 + sg * 2048;
          WO[n][sg][0] = tr_frag(a0 + o, a0 + o + 1024); WO[n][sg][1] = tr_frag(a1_ + o, a1_ + o + 1024);
        }
    }
#pragma unroll
    for (int n = 0; n < 16; ++n) PIN(WO[n >> 2][(n >> 1) & 1][n & 1]);
    lds_put(SY_WOK + h * 4, 1);
    TSW(29, 0);
    if (EARLY) fill(16, 13, 3);
    lds_wait(SY_DONE, 4, c < 10 ? FILLAT : 7, lane);
    fill(3, 0, 6);
    lds_wait(SY_WOK, 4, 1, lane);
    fill(9, 6, 3);
    WAIT_VM(6); lds_put(SY_FULL2 + h * 4, 1);
    TSW(31, 0);
    fill(12, 9, EARLY ? 4 : 7);
    TSW(20, 0);
    lds_wait(SY_DONE, 4, 9, lane);
#pragma unroll
    for (int n = 0; n < 18; ++n) { PIN(R[n]); *(LDS u32x4*)((n < 6 ? 131072 : -24576) + h * 2048 + v16 + (n >> 1) * 8192 + (n & 1) * 1024) = R[n]; }
    WAIT_VM(0); lds_put(SY_RDY + h * 4, 1);
    TSW(4, 0);
    u32x4 PQ[2][7], KV[8];
    __builtin_amdgcn_s_setprio(PRIO_S);
    lds_wait(SY_HOK, 1, 1, lane);
    if (*(volatile LDS unsigned*)SY_HOK < 2u) tag_wait(min(lane, 4) * FS, rws, WS_FL + (rt * 32 + 19) * FS, ~0ull, tag);
#pragma unroll
    for (int n = 0; n < 14; ++n) LD4(PQ[n / 7][n % 7], v16, rws, WS_PQ + ((rt * 2 + n / 7) * 28 + hg * 7 + n % 7) * 1024u, "sc1");
    FIN_LD;
    TSW(19, 0);
    if (!NOKV) tag_wait(min(lane, 7) * FS, rws, WS_KF + (xc * 32 + hg * 8) * FS, ~0ull, tag);
#pragma unroll
    for (int n = 0; n < 8; ++n) LD4(KV[n], v16, rws, WS_KV + hg * 8192u + n * 1024u, "sc1");
    __builtin_amdgcn_s_setprio(PRIO_M);
    TSW(5, 0);
    WAIT_VM(0);
    TSW(16, 0);
    const unsigned km = hh ? 0u : ~0u;
#pragma unroll
    for (int n = 0; n < 8; ++n) PIN(KV[n]);
#pragma unroll
    for (int n = 0; n < 14; ++n) PIN(PQ[n / 7][n % 7]);
    f32x16 nm[3], as[2][2] = {{z16, z16}, {z16, z16}};
#define NUM(u) do { const u32x4* P = PQ[(u) >> 2]; const int n = (u) & 3, lo = n < 3 ? 2 * n + 1 : 5, hi = n ? 2 * n - 1 : 1; \
      nm[(u) % 3] = mfma(asbf(KV[2 * n]), asbf(P[2 * n]), z16); \
      nm[(u) % 3] = mfma(asbf(KV[2 * n + 1]), asbf((u32x4){P[lo].x, P[lo].y, P[hi].z, P[hi].w}), nm[(u) % 3]); } while (0)
    NUM(0); NUM(1); SB;
    unsigned d2 = 0; bool put0 = false;
#pragma unroll
    for (int u = 0; u < 8; ++u) {
      if (u < 6) NUM(u + 2);
      const unsigned dn = __float_as_uint(nm[u % 3][12]);
      float rd = __builtin_amdgcn_rcpf(__uint_as_float(__builtin_amdgcn_permlane32_swap(dn, dn, false, false)[1]) + 1e-6f);
      asm volatile("s_nop 0" : "+v"(rd));
      float o[16];
#pragma unroll
      for (int e = 0; e < 16; ++e) o[e] = mul1(nm[u % 3][e], rd);
      const u32x4 o0 = {pk2(o[0], o[1]), pk2(o[2], o[3]), pk2(o[4], o[5]), pk2(o[6], o[7])};
      const u32x4 o1 = {pk2(o[8], o[9]), pk2(o[10], o[11]), pk2(o[12], o[13]) & km, pk2(o[14], o[15]) & km};
      if (u == 3) d2 = *(volatile LDS unsigned*)(SY_DONE2 + p * 4);
      SB;
      as[u >> 2][0] = mfma(WO[u & 3][0][0], asbf(o0), as[u >> 2][0]); as[u >> 2][1] = mfma(WO[u & 3][0][1], asbf(o0), as[u >> 2][1]);
      as[u >> 2][0] = mfma(WO[u & 3][1][0], asbf(o1), as[u >> 2][0]); as[u >> 2][1] = mfma(WO[u & 3][1][1], asbf(o1), as[u >> 2][1]);
      if (u == 4) {
        put0 = __all(d2 >= 1u);
        if (put0) PUT_TILES(49152 + h * 16384, as[0]);
      }
    }
    TSW(21, 0);
    if (!put0) { lds_wait(SY_DONE2, 4, 1, lane); PUT_TILES(49152 + h * 16384, as[0]); }
    PUT_TILES(57344 + h * 16384, as[1]);
    TSW(17, 0);
    }
  } else {
  const int cw = w - 4, wl = cw & 1, wh = cw >> 1;
  __builtin_amdgcn_s_setprio(PRIO_C);
  bf16x8 ones = asbf((u32x4){0x3f803f80u, 0x3f803f80u, 0x3f803f80u, 0x3f803f80u});
  PIN(ones);
  {
    {
      u64 ep;
      asm volatile("s_load_dwordx2 %0, %1, 0x0\n\ts_waitcnt lgkmcnt(0) vmcnt(0)" : "=&s"(ep) : "s"(ws) : "memory");
      tag.x = (unsigned)ep + 1u; tag.y = (unsigned)(ep >> 32) + 1u;
    }
    if (cw == 0) { asm volatile("" :: "v"(cpf)); *(volatile LDS u32x2*)SY_TAG = tag; }
    if (TOUCH && cw == 2) asm volatile("" :: "v"(tch[0]), "v"(tch[1]));
    if (cw == 3) {
      float sq[2] = {0.f, 0.f}, sm[2] = {0.f, 0.f};
      const bf16x2 one2 = __builtin_bit_cast(bf16x2, 0x3f803f80u);
#pragma unroll
      for (int k = 0; k < 7; ++k) {
        PIN(xs[k]);
#pragma unroll
        for (int e = 0; e < 4; ++e) {
          const unsigned u = xs[k][e];
          const bf16x2 v = __builtin_bit_cast(bf16x2, u);
          sq[e & 1] = __builtin_amdgcn_fdot2_f32_bf16(v, v, sq[e & 1], false); sm[e & 1] = __builtin_amdgcn_fdot2_f32_bf16(v, one2, sm[e & 1], false);
        }
      }
      const float m = sum8(sm[0] + sm[1]) * (1.f / D);
      const float var = fmaxf(sum8(sq[0] + sq[1]) * (1.f / D) - m * m, 0.f);
      const u32x2 gr = {pk2(-m, __builtin_amdgcn_sqrtf(rbf(rbf(var) + 1e-5f))), tag.x};
      if ((lane & 7) == 0) ST2(gr, (unsigned)lane, rws, WS_ST + (r * 128 + c * 8) * 8, "sc1");
    }
  }
  const unsigned fl_off = WS_FL + ((r * 2 + wl) * 32 + 2 * c + wh) * FS;
  const unsigned fa = SY_FULL + p * 4;
#define CHECK_FULL(v, need) do { if (__builtin_expect(__any((v) < (need)), 0)) lds_wait(SY_FULL, 4, need, lane); FENCE; } while (0)
  const bool do_cs = !(r & 1) && r != 14;
  unsigned da = SY_DONE + cw * 4;
  PIN(da);
  const unsigned xm = hh ? 0u : ~0u;
  if (!kvt) {
    const unsigned xa = (wl * 64 + l31) * 128 + ((hh ^ ((l31 >> 1) & 7)) << 4);
    const unsigned wr = 16384 + (8 * hh + q) * 128 + blk * 32 + p * 8;
    const unsigned wa0 = wr + wh * 8192 + ((wh ^ (q >> 1)) << 6), wa1 = wr + wh * 16384 + ((wh ^ 1 ^ (q >> 1)) << 6);
    f32x16 acc[2][3] = {{z16, z16, z16}, {z16, z16, z16}};
    bf16x8 X[4][2], W[4][3];
#define RDA(n, ks) do { \
      if ((n) == 0 || (n) == 2) X[ks][(n) >> 1] = ld_frag(ax[ks] + ((n) >> 1) * 4096); \
      else { const unsigned a_ = ((n) == 3 ? a1_ : a0_) + ((n) == 4 ? 8192 : 0) + (ks) * 2048; W[ks][(n) == 1 ? 0 : (n) - 2] = tr_frag(a_, a_ + 512); } } while (0)
#define MMA(ks, m) acc[(m) & 1][(m) >> 1] = mfma(W[ks][(m) >> 1], X[ks][(m) & 1], acc[(m) & 1][(m) >> 1])
    unsigned ax[4] = {xa, xa ^ 32u, xa ^ 64u, xa ^ 96u}, a0_ = wa0, a1_ = wa1;
#pragma unroll
    for (int n = 0; n < 6; ++n) PIN(acc[n & 1][n >> 1]);
    lds_wait(SY_FULL, 4, 1, lane);
    TSW(6, 4); TSC(24, 4);
    if (do_cs) {
#pragma unroll 1
      for (int b = wl; b < 3; b += 2) {
        const unsigned a = b == 1 ? wa1 : wa0 + b * 4096;
        f32x16 cs = z16;
#pragma unroll
        for (int ks = 0; ks < 4; ++ks) cs = mfma(ones, tr_frag(a + ks * 2048, a + ks * 2048 + 512), cs);
        const u32x2 gr = {__float_as_uint(cs[0]), tag.x};
        if (lane < 32) ST2(gr, v8, rws, WS_CS + (c * 7 + s0) * 2048 + (3 * wh + b) * 256, "sc1");
      }
    }
#pragma unroll
    for (int n = 0; n < 10; ++n) { RDA(n % 5, n / 5); SB; }
    unsigned tv = 1; int ts = 0;
    PIN(tv);
#pragma unroll 1
    for (;;) {
      unsigned fv; int dl;
#pragma unroll
      for (int m = 0; m < 12; ++m) {
        MMA(m / 6, m % 6); SB;
        if (m % 6 < 5) RDA(m % 6, 2 + m / 6);
        if (m == 0) { dl = ((0x24 >> ts) & 1) ? -81920 : 40960; ++ts; asm volatile("" : "+s"(dl), "+s"(ts)); }
        if (m == 2) { ax[0] += dl; ax[1] += dl; PIN(ax[0]); PIN(ax[1]); }
        if (m == 5) fv = *(volatile LDS unsigned*)fa;
        if (m == 10) *(volatile LDS unsigned*)da = tv;
        if (m == 11) { a0_ += dl; a1_ += dl; ++tv; CHECK_FULL(fv, tv); TSW(55 + ts, STW); }
        SB;
      }
      if (__builtin_expect(ts == 7, 0)) break;
#pragma unroll
      for (int m = 0; m < 12; ++m) {
        MMA(2 + m / 6, m % 6); SB;
        if (m % 6 < 5) RDA(m % 6, m / 6);
        if (m == 5) { ax[2] += dl; ax[3] += dl; }
        SB;
      }
    }
    TSW(7, 4); TSC(25, 4);
    unsigned st[2]; u32x2 cv[3];
#pragma unroll
    for (int a = 0; a < 2; ++a) st[a] = *(LDS const unsigned*)(L_ST + (wl * 64 + a * 32 + l31) * 4);
#pragma unroll
    for (int b = 0; b < 3; ++b) cv[b] = *(LDS const u32x2*)(L_CS + ((3 * wh + b) * 32 + l31) * 8);
    SB;
    MMA(2, 0); MMA(2, 1); MMA(3, 0); MMA(3, 1);
    SB;
    bf16x8 xe[2];
#pragma unroll
    for (int a = 0; a < 2; ++a) xe[a] = asbf((u32x4){((st[a] & 0xffffu) | (st[a] << 16)) & xm, st[a] & xm, 0u, 0u});
#define MME(m) acc[(m) & 1][(m) >> 1] = mfma(asbf((u32x4){cv[(m) >> 1].x, cv[(m) >> 1].y, cv[(m) >> 1].x, cv[(m) >> 1].y}), xe[(m) & 1], acc[(m) & 1][(m) >> 1])
    MME(0); MME(1);
    SB;
    TSW(18, 4);
    const int R0 = r * 4 + wl * 2;
    const unsigned sk = c == 9 ? 0x40000000u : 0u;
#define MM6(b, j) do { if ((j) < 4) MMA(2 + ((j) >> 1), 2 * (b) + ((j) & 1)); else MME(2 * (b) + ((j) & 1)); } while (0)
    if (2 * c + wh < 19) {
      const unsigned ob = WS_H + (R0 * 112 + 12 * c + 6 * wh) * 1024u;
#pragma unroll
      for (int b = 0; b < 3; ++b)
#pragma unroll
        for (int n = 0; n < 4; ++n) {
          const int a = n >> 1, e = 8 * (n & 1);
          float t[8]; u32x4 o;
          if (b < 2) MM6(b + 1, n < 2 ? 2 * n : n + 2);
          SB;
          if (n == 0) asm volatile("s_nop 10" : "+v"(acc[0][b]), "+v"(acc[1][b]));
          asm volatile("v_mul_f32_e64 %0, %8, |%8|\n\tv_mul_f32_e64 %1, %9, |%9|\n\tv_mul_f32_e64 %2, %10, |%10|\n\tv_mul_f32_e64 %3, %11, |%11|\n\t"
                       "v_mul_f32_e64 %4, %12, |%12|\n\tv_mul_f32_e64 %5, %13, |%13|\n\tv_mul_f32_e64 %6, %14, |%14|\n\tv_mul_f32_e64 %7, %15, |%15|"
                       : "=&v"(t[0]), "=&v"(t[1]), "=&v"(t[2]), "=&v"(t[3]), "=&v"(t[4]), "=&v"(t[5]), "=&v"(t[6]), "=&v"(t[7])
                       : "v"(acc[a][b][e]), "v"(acc[a][b][e + 1]), "v"(acc[a][b][e + 2]), "v"(acc[a][b][e + 3]), "v"(acc[a][b][e + 4]), "v"(acc[a][b][e + 5]),
                         "v"(acc[a][b][e + 6]), "v"(acc[a][b][e + 7]));
          SB;
          if (b < 2 && n < 2) MM6(b + 1, 2 * n + 1);
          SB;
#pragma unroll
          for (int j = 0; j < 4; ++j) o[j] = pk2(t[2 * j], t[2 * j + 1]);
#pragma unroll
          for (int j = 0; j < 4; ++j) o[j] = relu2(o[j]);
          ST4(o, v16, rws, ob + a * (112 * 1024u) + (2 * b + (n & 1)) * 1024u + (b == 2 ? sk : 0u), "sc1");
          SB;
        }
    } else {
#pragma unroll
      for (int j = 0; j < 12; ++j) MM6(1 + j / 6, j % 6);
      const unsigned ob = WS_PQ + (R0 * 28 + (c == 9 ? 22 : (c - 10) * 12 + wh * 6)) * 1024u;
      SB;
#pragma unroll
      for (int a = 0; a < 2; ++a) {
        const float rl = __builtin_amdgcn_rcpf(bfhi(st[a])) * LOG2E;
#pragma unroll
        for (int n = 0; n < 6; ++n) {
          f32x2 u[4];
#pragma unroll
          for (int j = 0; j < 4; ++j) u[j] = (f32x2){acc[a][n >> 1][8 * (n & 1) + 2 * j], acc[a][n >> 1][8 * (n & 1) + 2 * j + 1]} * rl;
          PHI_A(u); PHI_E(0); PHI_E(2); PHI_B(o);
          ST4(o, v16, rws, ob + a * (28 * 1024u) + n * 1024u + (n < 2 ? sk : 0u), "sc1");
          SB;
        }
      }
    }
  } else {
    const unsigned xa = l31 * 128 + ((hh ^ ((l31 >> 1) & 7)) << 4);
    const int ok_ = (c & 1) * 32 + 56 * cw + 8 * ((4 * blk + p + cw) & 7), ov_ = (c & 1) * 32 + 56 * cw + 32 * blk + 8 * p;
    const unsigned ka = 16384 + hh * 1024 + q * 256 + ((((ok_ >> 6) & 3) ^ q) << 6) + (ok_ & 63);
    const unsigned va = 32768 + hh * 1024 + q * 256 + ((((ov_ >> 6) & 3) ^ q) << 6) + (ov_ & 63) + ((lane & 19) == 19 ? 0x100000 : 0);
    f32x16 acc[4][2] = {{z16, z16}, {z16, z16}, {z16, z16}, {z16, z16}};
    bf16x8 X[4][4], W[4][2];
#define RDK(n, ks) do { \
      if ((n) == 1 || (n) == 5) { const unsigned a_ = ((n) == 1 ? a0_ : a1_) + (ks) * 2048; W[ks][(n) >> 2] = tr_frag(a_, a_ + 8192); } \
      else X[ks][(n) ? (n) - 1 : 0] = ld_frag(ax[ks] + ((n) ? (n) - 1 : 0) * 4096); } while (0)
#define MMK(ks, m) acc[(m) & 3][(m) >> 2] = mfma(X[ks][(m) & 3], W[ks][(m) >> 2], acc[(m) & 3][(m) >> 2])
    unsigned ax[4] = {xa, xa ^ 32u, xa ^ 64u, xa ^ 96u}, a0_ = ka, a1_ = va;
#pragma unroll
    for (int n = 0; n < 8; ++n) PIN(acc[n & 3][n >> 2]);
    lds_wait(SY_FULL, 4, 1, lane);
    if (do_cs) {
#pragma unroll 1
      for (int b = 0; b < 2; ++b) {
        const unsigned a = b ? va : ka;
        f32x16 cs = z16;
#pragma unroll
        for (int ks = 0; ks < 4; ++ks) cs = mfma(ones, tr_frag(a + ks * 2048, a + ks * 2048 + 8192), cs);
        const u32x2 gr = {__float_as_uint(cs[0]), tag.x};
        if (lane < 32) ST2(gr, v8, rws, WS_CS + (c * 7 + s0) * 2048 + (2 * cw + b) * 256, "sc1");
      }
    }
#pragma unroll
    for (int n = 0; n < 12; ++n) { RDK(n % 6, n / 6); SB; }
    unsigned tv = 1; int ts = 0;
    PIN(tv);
#pragma unroll 1
    for (;;) {
      unsigned fv; int dl;
#pragma unroll
      for (int m = 0; m < 16; ++m) {
        MMK(m >> 3, m & 7); SB;
        if ((m & 7) >= 1 && (m & 7) < 7) RDK((m & 7) - 1, 2 + (m >> 3));
        if (m == 0) { dl = ((0x24 >> ts) & 1) ? -98304 : 49152; ++ts; asm volatile("" : "+s"(dl), "+s"(ts)); }
        if (m == 7) { ax[0] += dl; ax[1] += dl; PIN(ax[0]); PIN(ax[1]); }
        if (m == 8) fv = *(volatile LDS unsigned*)fa;
        if (m == 14) *(volatile LDS unsigned*)da = tv;
        if (m == 15) { a0_ += dl; a1_ += dl; ++tv; CHECK_FULL(fv, tv); TSW(55 + ts, STW); }
        SB;
      }
      if (__builtin_expect(ts == 7, 0)) break;
#pragma unroll
      for (int m = 0; m < 16; ++m) {
        MMK(2 + (m >> 3), m & 7); SB;
        if ((m & 7) >= 1 && (m & 7) < 7) RDK((m & 7) - 1, m >> 3);
        if (m == 7) { ax[2] += dl; ax[3] += dl; }
        SB;
      }
    }
    TSW(8, 4);
    unsigned st[4]; u32x2 cv[2]; f32x4 rsv[2][4];
#pragma unroll
    for (int a = 0; a < 4; ++a) st[a] = *(LDS const unsigned*)(L_ST + (a * 32 + l31) * 4);
#pragma unroll
    for (int b = 0; b < 2; ++b) cv[b] = *(LDS const u32x2*)(L_CS + ((2 * cw + b) * 32 + l31) * 8);
    unsigned rsa = L_RS + hh * 16;
    PIN(rsa);
#define MK6(a, j) do { if ((j) < 4) MMK(2 + ((j) >> 1), (a) + 4 * ((j) & 1)); else acc[a][(j) & 1] = mfma(asbf((u32x4){((st[a] & 0xffffu) | (st[a] << 16)) & xm, st[a] & xm, 0u, 0u}), \
        asbf((u32x4){cv[(j) & 1].x, cv[(j) & 1].y, cv[(j) & 1].x, cv[(j) & 1].y}), acc[a][(j) & 1]); } while (0)
#pragma unroll
    for (int n = 0; n < 4; ++n) rsv[0][n] = *(LDS const f32x4*)(rsa + n * 32);
    SB;
#pragma unroll
    for (int j = 0; j < 6; ++j) MK6(0, j);
    SB;
    f32x16 kv = z16;
    u32x4 fk, fv;
#pragma unroll
    for (int gi = 0; gi < 8; ++gi) {
      const int a = gi >> 1, sg = gi & 1, e = 8 * sg;
      if (a < 3 && sg == 0) {
#pragma unroll
        for (int n = 0; n < 4; ++n) rsv[(a + 1) & 1][n] = *(LDS const f32x4*)(rsa + (a + 1) * 128 + n * 32);
      }
      const f32x4 ra_ = rsv[a & 1][2 * sg], rb_ = rsv[a & 1][2 * sg + 1];
      const f32x2 rl[4] = {{ra_[0], ra_[1]}, {ra_[2], ra_[3]}, {rb_[0], rb_[1]}, {rb_[2], rb_[3]}};
      f32x2 u[4], vv[4];
#pragma unroll
      for (int j = 0; j < 4; ++j) u[j] = (f32x2){acc[a][0][e + 2 * j], acc[a][0][e + 2 * j + 1]} * rl[j];
#pragma unroll
      for (int j = 0; j < 4; ++j) vv[j] = (f32x2){acc[a][1][e + 2 * j], acc[a][1][e + 2 * j + 1]} * rl[j];
      PHI_A(u);
      if (gi) kv = mfma(asbf(fk), asbf(fv), kv);
      SB;
      PHI_E(0);
      if (a < 3) MK6(a + 1, 3 * sg);
      SB;
      PHI_E(2);
      if (a < 3) MK6(a + 1, 3 * sg + 1);
      SB;
      fv = (u32x4){pk2(vv[0].x, vv[0].y), pk2(vv[1].x, vv[1].y), pk2(vv[2].x, vv[2].y), pk2(vv[3].x, vv[3].y)};
      SB;
      if (a < 3) MK6(a + 1, 3 * sg + 2);
      SB;
      float po[8];
#pragma unroll
      for (int j = 0; j < 8; ++j) { po[j] = pe[j >> 1][j & 1] + 1e-6f; PIN(po[j]); }
#pragma unroll
      for (int j = 0; j < 4; ++j) pm[j] = __builtin_elementwise_fma(pm[j], (f32x2){UP / LOG2E, UP / LOG2E}, (f32x2){po[2 * j], po[2 * j + 1]});
      fk = (u32x4){pk2(pm[0].x, pm[0].y), pk2(pm[1].x, pm[1].y), pk2(pm[2].x, pm[2].y), pk2(pm[3].x, pm[3].y)};
      SB;
    }
    kv = mfma(asbf(fk), asbf(fv), kv);
    kv = kv * (l31 == 28 ? 1.f / 1.4453125f : 1.f / LOG2E);
    const unsigned ko = WS_KVP + (((c - 12) * 4 + cw) * 16 + r) * 4096u;
#pragma unroll
    for (int e = 0; e < 4; ++e) {
      const u32x4 o = {pk2(kv[4 * e], kv[4 * e + 1]), tag.x, pk2(kv[4 * e + 2], kv[4 * e + 3]), tag.x};
      ST4(o, v16, rws, ko + e * 1024u, "sc1");
    }
  }
  lds_put(da, 8);
  TSW(22, 4);
  bf16x8 WH[9][4];
  unsigned ra[3][2];
#pragma unroll
  for (int cb = 0; cb < 2; ++cb) {
    ra[0][cb] = wh * 4096 + (4 * hh + q) * 128 + ((cb ^ (q >> 1)) << 6) + blk * 32 + p * 8; ra[1][cb] = ra[0][cb] + 65536; ra[2][cb] = ra[0][cb] + 131072;
    PIN(ra[0][cb]); PIN(ra[1][cb]); PIN(ra[2][cb]);
  }
#define RDW(dst, n, reg) do { const unsigned a_ = ra[(reg) >> 3][(n) & 1] + ((reg) & 7) * 8192 + ((n) >> 1) * 2048; dst[n] = tr_frag(a_, a_ + 1024); } while (0)
  if (!kvt) WAIT_VM(0);
  if (lane == 0) ST2(tag, 0u + lane, rws, fl_off, "sc1");
  TSW(9, 4);
  if (!pure) {
    lds_wait(SY_FULL2, 4, 1, lane);
#pragma unroll
    for (int n = 0; n < 36; ++n) RDW(WH[n >> 2], n & 3, (16 + (n >> 2)) % 19);
    lds_put(da, 9);
  }
  TSW(30, 4);

  if (!pure) {
    const int kh = wh, R = rt * 2 + wl;
    if (cw == 0) {
      const u64 bl = tag_wait(min(lane, 23) * FS, rws, WS_FL + rt * 32 * FS, 0x7FFFFull, tag);
      lds_put(SY_HOK, (bl & 0xF80000ull) == 0xF80000ull ? 2 : 1);
    } else lds_wait(SY_HOK, 1, 1, lane);
    WAIT_VM(0);
    TSW(12, 4);
    const unsigned hs = WS_H + (R * 112 + kh * 56) * 1024u;
    constexpr int NH = NHW, E = 28 - NH;
    u32x4 hf[NH][2];
#define HLD(u) do { const unsigned o_ = hs + ROT(u) * 2048u; \
      LD4(hf[(u) % NH][0], v16, rws, o_, "sc1"); LD4(hf[(u) % NH][1], v16, rws, o_, "offset:1024 sc1"); } while (0)
#pragma unroll
    for (int u = 0; u < NH; ++u) HLD(u);
    f32x16 af[2] = {z16, z16};
    bf16x8 W[LA + 1][4];
    TSW(13, 4); TSC(26, 4);
#pragma unroll
    for (int u = 0; u < 28; ++u) {
      constexpr auto total = [](int u_) { return 2 * NH + 2 * (u_ < E ? u_ : E) + (u_ > E ? 3 : 0); };
      asm volatile("s_waitcnt vmcnt(%2)" : "+v"(hf[u % NH][0]), "+v"(hf[u % NH][1]) : "n"(total(u) - 2 - 2 * u) : "memory");
      if (u == 9 - LA) { lds_wait(SY_RDY, 4, 1, lane); TSW(28, 4); }
#pragma unroll
      for (int n = 0; n < 4; ++n) {
        af[n & 1] = mfma(u < 9 ? WH[u < 9 ? u : 0][n] : W[u % (LA + 1)][n], asbf(hf[u % NH][n >> 1]), af[n & 1]); SB;
        if (u + LA >= 9 && u + LA < 28) RDW(W[(u + LA) % (LA + 1)], n, (u + LA - 3) % 19);
        SB;
      }
      if (u == 16 - LA) lds_put(SY_DONE2 + cw * 4, 1);
      if (u == 20 - LA) lds_put(SY_DONE2 + cw * 4, 2);
      if (u < E) HLD(u + NH);
      else if (u == E) FIN_LD;
    }
    TSW(14, 4); TSC(27, 4);
    lds_wait(SY_DONE2, 4, 2, lane);
    PUT_TILES(114688 + cw * 8192, af);
    WAIT_VM(0);
  }
  }
  if (pure) {
    const int id = xc * 4 + (ix >> 3), hd = id >> 1;
    lds_wait(SY_DONE, 4, 8, lane);
    f32x4 a, b;
#pragma unroll 1
    for (unsigned sp = 0; sp < SPIN_CAP; ++sp) {
      u32x4 pr[4];
#pragma unroll
      for (int n = 0; n < 4; ++n) LD4(pr[n], v16, rws, WS_KVP + (hd * 16 + 2 * w + (n >> 1)) * 4096u + (id & 1) * 2048u + (n & 1) * 1024u, "sc1");
      WAIT_VM(0);
      unsigned bad = 0;
#pragma unroll
      for (int n = 0; n < 4; ++n) { PIN(pr[n]); bad |= (pr[n].y ^ tag.x) | (pr[n].w ^ tag.x); }
#define F4(n) (f32x4){bflo(pr[n].x), bfhi(pr[n].x), bflo(pr[n].z), bfhi(pr[n].z)}
      a = F4(0) + F4(2); b = F4(1) + F4(3);
      if (__all(bad == 0)) break;
      __builtin_amdgcn_s_sleep(RSLEEP);
    }
    TSW(19, 4);
    *(LDS f32x4*)(w * 2048 + v16) = a; *(LDS f32x4*)(w * 2048 + 1024 + v16) = b;
    BARRIER;
    TSW(20, 4);
    if (w == 4) {
      a = *(LDS const f32x4*)v16; b = *(LDS const f32x4*)(1024 + v16);
#pragma unroll
      for (int n = 1; n < 8; ++n) { a += *(LDS const f32x4*)(n * 2048 + v16); b += *(LDS const f32x4*)(n * 2048 + 1024 + v16); }
      u32x4 o = {pk2(a[0], a[1]), pk2(a[2], a[3]), pk2(b[0], b[1]), pk2(b[2], b[3])};
      if ((id & 1) && hh != (hd & 1)) { if (hd & 2) { o.x = 0; o.y = 0; } else { o.z = 0; o.w = 0; } }
      ST4(o, v16, rws, WS_KV + id * 1024u, "sc1");
      TSW(21, 4);
      WAIT_VM(0);
      if (lane < 8) ST2(tag, lane * 32 * FS, rws, WS_KF + id * FS, "sc1");
      TSW(10, 4); TS_DUMP;
    } else if (w == 5 && bid == 255) {
#pragma unroll 1
      for (int n = 0; n < 4; ++n) tag_wait((unsigned)((lane >> 4) * 64 + (lane & 15) * 2) * FS, rws, WS_FL + n * 256 * FS, ~0ull, tag);
      if (lane == 0) ST2(tag, 0u + lane, rws, WS_EP, "sc1");
    }
    return;
  }
  BARRIER;
  TSW(15, 4);
  {
    const unsigned a0 = 49152 + (tid >> 8) * 8192 + (tid & 7) * 1024 + ((((tid >> 3) & 31) ^ ((tid & 7) * 2)) << 4);
    f32x4 f[2][2], sp[4][2];
#pragma unroll
    for (int n = 0; n < 2; ++n) { f[n][0] = *(LDS const f32x4*)(a0 + 65536 + n * 16384); f[n][1] = *(LDS const f32x4*)(a0 + 65536 + n * 16384 + 512); }
#pragma unroll
    for (int n = 0; n < 4; ++n) { sp[n][0] = *(LDS const f32x4*)(a0 + n * 16384); sp[n][1] = *(LDS const f32x4*)(a0 + n * 16384 + 512); }
    SB;
    PIN(xr); PIN(br); PIN(sg_);
    const float rs = __builtin_amdgcn_rcpf(bfhi(sg_)), rs2 = rs * rs;
    u32x4 ov;
#pragma unroll
    for (int e = 0; e < 4; ++e) {
      const int hf_ = e >> 1, j = (e & 1) * 2;
      const float t0 = fmaf(f[0][hf_][j] + f[1][hf_][j], rs2, bflo(br[e])), t1 = fmaf(f[0][hf_][j + 1] + f[1][hf_][j + 1], rs2, bfhi(br[e]));
      const float g0 = t0 * __builtin_amdgcn_rcpf(1.f + __expf(-t0)), g1 = t1 * __builtin_amdgcn_rcpf(1.f + __expf(-t1));
      const float s0_ = (sp[0][hf_][j] + sp[1][hf_][j]) + (sp[2][hf_][j] + sp[3][hf_][j]);
      const float s1_ = (sp[0][hf_][j + 1] + sp[1][hf_][j + 1]) + (sp[2][hf_][j + 1] + sp[3][hf_][j + 1]);
      ov[e] = pk2(fmaf(s0_, g0, bflo(xr[e])), fmaf(s1_, g1, bfhi(xr[e])));
    }
    ST4(ov, fxo, mk_rsrc(out, S * D * 2), 0u, "sc0 sc1");
    TSW(11, 4);
    if (w == 4) TS_DUMP;
  }
}

extern "C" void kernel_launch(void* const* d_in, const int* in_sizes, int n_in, void* d_out, int out_size, void* d_ws,
                              size_t ws_size, hipStream_t stream) {
  (void)hipFuncSetAttribute((const void*)fused, hipFuncAttributeMaxDynamicSharedMemorySize, LDS_BYTES);
  fused<<<256, 512, LDS_BYTES, stream>>>((const u16*)d_in[0], (const u16*)d_in[1], (const u16*)d_in[4], (char*)d_ws, (const u16*)d_in[2],
                                         (const u16*)d_in[5], (const u16*)d_in[6], (const u16*)d_in[7], (u16*)d_out);
}
```

```cpp
#include <hip/hip_runtime.h>
#include <hip/hip_bf16.h>
#include <stdint.h>
#pragma clang diagnostic ignored "-Wint-to-pointer-cast"

#define HMOD "sc1"
#define PRIO_H 3
#define PRIO_C 2
#define LSLEEP 1
#define STW 99
#define PF0 2
#define PFN 0
#define FILLAT 8
#define THR_N 0
#define THR_V 4
#define NHW 7
#define TSLEEP 4
#define TOUCH 0
#define S0MODE 1
#define FS 128u
#define POLL_LATE 0
#define TCH2 5
#define PRIO_S 1
#define RSLEEP 4
#define NX 9
#define PFA 9
#define PRIO_P 0
#define PRIO_W 0
#define EARLY 0
#define CTROT 1
#define ROTM 1
#define LA 2
#define NOKV 0
#define PRIO_M 0
#define PRIO_K 3
#define PFE 4
typedef unsigned short u16;
typedef unsigned long long u64;
typedef __bf16 bf16x8 __attribute__((ext_vector_type(8)));
typedef __bf16 bf16x2 __attribute__((ext_vector_type(2)));
typedef short s16x4 __attribute__((ext_vector_type(4)));
typedef float f32x4 __attribute__((ext_vector_type(4)));
typedef float f32x16 __attribute__((ext_vector_type(16)));
typedef float f32x2 __attribute__((ext_vector_type(2)));
typedef unsigned u32x4 __attribute__((ext_vector_type(4)));
typedef unsigned u32x2 __attribute__((ext_vector_type(2)));
#define LDS __attribute__((address_space(3)))
#define DI __device__ __forceinline__

constexpr int S = 2048, D = 448, F = 1792;
constexpr unsigned WS_EP = 0;
constexpr unsigned WS_KF = 1024;
constexpr unsigned WS_FL = WS_KF + 256 * FS;
constexpr unsigned WS_ST = WS_FL + 1024 * FS;
constexpr unsigned WS_CS = WS_ST + 16384;
constexpr unsigned WS_KV = WS_CS + 16 * 7 * 2048;
constexpr unsigned WS_KVP = WS_KV + 32768;
constexpr unsigned WS_PQ = WS_KVP + 16 * 16 * 4096;
constexpr unsigned WS_H = WS_PQ + 64 * 28 * 1024;
constexpr unsigned WS_END = WS_H + 64 * 112 * 1024;
constexpr unsigned L_CS = 155648, L_ST = L_CS + 2048, L_RS = L_ST + 512, L_SY = L_RS + 512, L_END = L_SY + 256;
constexpr unsigned SY_FULL = L_SY, SY_AUX = L_SY + 16, SY_DONE = L_SY + 32, SY_RDY = L_SY + 48, SY_FULL2 = L_SY + 64, SY_DONE2 = L_SY + 80,
                   SY_GO = L_SY + 112, SY_WOK = L_SY + 128, SY_KOK = L_SY + 96, SY_HOK = L_SY + 100, SY_TAG = L_SY + 104;
constexpr unsigned CODE_BYTES = 33024;
constexpr unsigned SPIN_CAP = 1u << 17;

#define LDS_BYTES L_END
#define TSW(n, wv)
#define TSC(n, wv)
#define TS_TOP
#define TS_INIT
#define TS_DUMP

DI u32x4 mk_rsrc(const void* p, unsigned bytes) {
  uint64_t a = (uint64_t)p;
  u32x4 r; r.x = (unsigned)a; r.y = (unsigned)(a >> 32) & 0xffffu; r.z = bytes; r.w = 0x00020000u; return r;
}
DI void dma16(unsigned lds_addr, unsigned voff, u32x4 rsrc, unsigned soff) {
  asm volatile("s_mov_b32 m0, %0\n\ts_nop 0\n\tbuffer_load_dwordx4 %1, %2, %3 offen lds"
               :: "s"(lds_addr), "v"(voff), "s"(rsrc), "s"(soff) : "memory");
}
template <unsigned OFF> DI void dma16o(unsigned lds_base, unsigned voff, u32x4 rsrc, unsigned soff) {
  asm volatile("s_add_u32 m0, %0, %4\n\ts_nop 0\n\tbuffer_load_dwordx4 %1, %2, %3 offen lds"
               :: "s"(lds_base), "v"(voff), "s"(rsrc), "s"(soff), "n"(OFF) : "memory", "scc");
}
#define SB __builtin_amdgcn_sched_barrier(0)
#define FENCE asm volatile("" ::: "memory")
#define PIN(a) asm volatile("" : "+v"(a))
#define WAIT_VM(n) asm volatile("s_waitcnt vmcnt(%0)" :: "n"(n) : "memory")
#define WAIT_LDS asm volatile("s_waitcnt lgkmcnt(0)" ::: "memory")
#define BARRIER asm volatile("s_waitcnt lgkmcnt(0)\n\ts_barrier" ::: "memory")
#define LD4(dst, voff, rsrc, soff, mod) asm volatile("buffer_load_dwordx4 %0, %1, %2, %3 offen " mod : "=v"(dst) : "v"(voff), "s"(rsrc), "s"(soff) : "memory")
#define LD2(dst, voff, rsrc, soff, mod) asm volatile("buffer_load_dwordx2 %0, %1, %2, %3 offen " mod : "=v"(dst) : "v"(voff), "s"(rsrc), "s"(soff) : "memory")
#define LD1(dst, voff, rsrc, soff, mod) asm volatile("buffer_load_dword %0, %1, %2, %3 offen " mod : "=v"(dst) : "v"(voff), "s"(rsrc), "s"(soff) : "memory")
#define ST4(src, voff, rsrc, soff, mod) asm volatile("buffer_store_dwordx4 %0, %1, %2, %3 offen " mod "\n\ts_nop 1" :: "v"(src), "v"(voff), "s"(rsrc), "s"(soff) : "memory")
#define ST2(src, voff, rsrc, soff, mod) asm volatile("buffer_store_dwordx2 %0, %1, %2, %3 offen " mod :: "v"(src), "v"(voff), "s"(rsrc), "s"(soff) : "memory")
DI f32x4 mfma16(bf16x8 a, bf16x8 b, f32x4 c) { return __builtin_amdgcn_mfma_f32_16x16x32_bf16(a, b, c, 0, 0, 0); }
DI f32x16 mfma(bf16x8 a, bf16x8 b, f32x16 c) { return __builtin_amdgcn_mfma_f32_32x32x16_bf16(a, b, c, 0, 0, 0); }
DI bf16x8 asbf(u32x4 v) { return __builtin_bit_cast(bf16x8, v); }
DI bf16x8 ld_frag(unsigned a) { return *(LDS const bf16x8*)a; }
DI bf16x8 tr_frag(unsigned a0, unsigned a1) {
  s16x4 lo = __builtin_amdgcn_ds_read_tr16_b64_v4i16((LDS s16x4*)a0);
  s16x4 hi = __builtin_amdgcn_ds_read_tr16_b64_v4i16((LDS s16x4*)a1);
  return __builtin_bit_cast(bf16x8, __builtin_shufflevector(lo, hi, 0, 1, 2, 3, 4, 5, 6, 7));
}
DI unsigned pk2(float a, float b) { f32x2 v = {a, b}; return __builtin_bit_cast(unsigned, __builtin_convertvector(v, bf16x2)); }
DI float bflo(unsigned u) { return __uint_as_float(u << 16); }
DI float bfhi(unsigned u) { return __uint_as_float(u & 0xffff0000u); }
DI float rbf(float f) { return bflo(pk2(f, 0.f)); }
DI f32x2 mul_clamp(f32x2 a, f32x2 b) { f32x2 r; asm("v_pk_mul_f32 %0, %1, %2 clamp" : "=v"(r) : "v"(a), "v"(b)); return r; }
constexpr float DN = 0x1p-40f, UP = 0x1p40f, LOG2E = 1.44269504f;
#define PHI_A(u) f32x2 pm[4], pe[4]; _Pragma("unroll") for (int j_ = 0; j_ < 4; ++j_) pm[j_] = mul_clamp(u[j_], (f32x2){DN, DN}); SB
#define PHI_E(j0) _Pragma("unroll") for (int j_ = (j0); j_ < (j0) + 2; ++j_) pe[j_] = (f32x2){__builtin_amdgcn_fmed3f(__builtin_amdgcn_exp2f(u[j_].x), 0.f, 1.f), \
                                                                                     __builtin_amdgcn_fmed3f(__builtin_amdgcn_exp2f(u[j_].y), 0.f, 1.f)}; SB
#define PHI_S _Pragma("unroll") for (int j_ = 0; j_ < 4; ++j_) pm[j_] = __builtin_elementwise_fma(pm[j_], (f32x2){UP / LOG2E, UP / LOG2E}, (f32x2){1e-6f, 1e-6f}); SB; \
              _Pragma("unroll") for (int j_ = 0; j_ < 4; ++j_) pm[j_] += pe[j_]; SB
#define PHI_B(o) PHI_S; const u32x4 o = {pk2(pm[0].x, pm[0].y), pk2(pm[1].x, pm[1].y), pk2(pm[2].x, pm[2].y), pk2(pm[3].x, pm[3].y)}
DI unsigned relu2(unsigned v) {
  typedef short s16x2 __attribute__((ext_vector_type(2)));
  return __builtin_bit_cast(unsigned, __builtin_elementwise_max(__builtin_bit_cast(s16x2, v), (s16x2){0, 0}));
}
DI float mul1(float a, float b) { float r; asm("v_mul_f32_e32 %0, %1, %2" : "=v"(r) : "v"(a), "v"(b)); return r; }
DI float sum8(float v) {
  v += __uint_as_float(__builtin_amdgcn_update_dpp(0u, __float_as_uint(v), 0xB1, 0xf, 0xf, true));
  v += __uint_as_float(__builtin_amdgcn_update_dpp(0u, __float_as_uint(v), 0x4E, 0xf, 0xf, true));
  v += __uint_as_float(__builtin_amdgcn_update_dpp(0u, __float_as_uint(v), 0x141, 0xf, 0xf, true));
  return v;
}
DI void lds_put(unsigned a, unsigned v) { FENCE; *(volatile LDS unsigned*)a = v; FENCE; }
DI void lds_wait(unsigned a, int n, unsigned need, int lane) {
  const unsigned ad = a + (lane & (n - 1)) * 4;
#pragma unroll 1
  for (unsigned sp = 0; sp < SPIN_CAP; ++sp) {
    FENCE; const unsigned v = *(volatile LDS unsigned*)ad; FENCE;
    if (__all(v >= need)) break;
    __builtin_amdgcn_s_sleep(LSLEEP);
  }
}
DI u64 tag_wait(unsigned voff, u32x4 rs, unsigned soff, u64 mask, u32x2 tag) {
  u64 bl = 0;
#pragma unroll 1
  for (unsigned sp = 0; sp < SPIN_CAP; ++sp) {
    u32x2 v;
    asm volatile("buffer_load_dwordx2 %0, %1, %2, %3 offen sc1\n\ts_waitcnt vmcnt(0)" : "=&v"(v) : "v"(voff), "s"(rs), "s"(soff) : "memory");
    bl = __ballot(v.x == tag.x && v.y == tag.y);
    if ((bl & mask) == mask) break;
    __builtin_amdgcn_s_sleep(TSLEEP);
  }
  return bl;
}

__global__ __launch_bounds__(512, 2) void fused(const u16* __restrict__ x, const u16* __restrict__ wqkv, const u16* __restrict__ w1, char* ws,
                                                const u16* __restrict__ wo, const u16* __restrict__ b1, const u16* __restrict__ w2,
                                                const u16* __restrict__ b2, u16* __restrict__ out) {
  TS_TOP;
  const int bid = blockIdx.x, tid = threadIdx.x, lane = tid & 63;
  const int w = __builtin_amdgcn_readfirstlane(tid >> 6);
  const int xc = bid & 7, ix = bid >> 3, sl = ix & 7, rl = ix >> 3;
  const int r = (xc >> 1) * 4 + rl, s0 = r >= 14 ? 0 : r >> 1;
  const u32x4 rx = mk_rsrc(x, S * D * 2);
  const unsigned vx0 = (unsigned)(r * 128 + (w & 1) * 64 + (lane >> 3)) * 896u + (unsigned)(((lane & 7) ^ (lane >> 4)) << 4), vx1 = (vx0 ^ 64u) + 7168u;
#define XGROUP(n) do { dma16o<(n) * 2048>(lx, vx0, rx, sg * 128 + (n) * 14336); dma16o<(n) * 2048 + 1024>(lx, vx1, rx, sg * 128 + (n) * 14336); } while (0)
  unsigned cpf;
  u64 pc; asm volatile("s_getpc_b64 %0" : "=s"(pc));
  if (w < 2) { const unsigned lx = w * 8192; const int sg = s0; XGROUP(0); XGROUP(1); XGROUP(2); XGROUP(3); }
  if (w == 4) LD1(cpf, lane < 9 ? (unsigned)(ix + lane * 32) * 128u : 0x80000000u, mk_rsrc((const void*)(pc & ~255ull), CODE_BYTES), 0u, "");
  asm volatile("" :: "s"(wqkv), "s"(w1), "s"(ws));
  const int c = (int)(((xc & 1) ? 0xEFB98765u : 0xDCA43210u) >> (4 * sl)) & 15;
  const bool kvt = sl >= 6, pure = sl == 7;
  const int rt = xc * 4 + rl, ct = sl + 2 * rl - (sl + 2 * rl >= 7 ? 7 : 0), rot2 = 7 * rl + xc - (7 * rl + xc >= 28 ? 28 : 0);
  const int l31 = lane & 31, hh = lane >> 5, q = (lane >> 2) & 3, p = lane & 3, blk = (lane >> 4) & 1;
  const unsigned slot_b = kvt ? 49152u : 40960u;
  const u32x4 rws = mk_rsrc(ws, WS_END);
  const unsigned v16 = lane * 16, v8 = lane * 8;
  TS_INIT;
  u32x2 tag;
  const unsigned swz = (unsigned)(((lane & 7) ^ ((lane >> 2) & 4)) << 4);
  f32x16 z16;
#pragma unroll
  for (int e = 0; e < 16; ++e) z16[e] = 0.f;

  u32x4 xr, br; unsigned sg_;
  const unsigned fxo = (unsigned)(rt * 64 + (tid >> 3)) * 896u + ct * 128 + (tid & 7) * 16;
#define FIN_LD do { LD4(xr, fxo, rx, 0u, ""); LD4(br, (tid & 7) * 16u, mk_rsrc(b2, D * 2), ct * 128u, ""); LD1(sg_, (unsigned)(tid & ~7), rws, WS_ST + rt * 512u, "sc1"); } while (0)
#define PUT_TILES(base, t) do { _Pragma("unroll") for (int n_ = 0; n_ < 8; ++n_) *(LDS f32x4*)((base) + n_ * 1024 + (v16 ^ (n_ * 32))) = \
      (f32x4){t[n_ >> 2][4 * (n_ & 3)], t[n_ >> 2][4 * (n_ & 3) + 1], t[n_ >> 2][4 * (n_ & 3) + 2], t[n_ >> 2][4 * (n_ & 3) + 3]}; } while (0)
  if (__builtin_expect(w < 4, 1)) {
    const int h = w, hp = h >> 1, hs_ = h & 1;
    const u32x4 rw2 = mk_rsrc(w2, F * D * 2), rwo = mk_rsrc(wo, D * D * 2);
    const unsigned vwo = (unsigned)(lane >> 3) * 896u + ct * 128 + swz;
    const unsigned vw2 = vwo + (unsigned)((h >> 1) * 896 + (h & 1) * 16) * 896u;
    const int hg = (h + xc + rl) & 3;
    u32x4 R[18];
#define ROT(u) ((u) + rot2 >= 28 ? (u) + rot2 - 28 : (u) + rot2)
#define PFLD(n) LD4(R[n], vw2, rw2, ROT(19 + ((n) >> 1)) * 28672u + ((n) & 1) * 7168u, "")
    auto fill = [&](int u, int g, int cnt) {
#pragma unroll 1
      for (int e = u + cnt; u < e; ++u, ++g) {
        const unsigned k = ROT(u) * 28672u;
        dma16(g * 8192 + h * 2048, vw2, rw2, k); dma16(g * 8192 + h * 2048 + 1024, vw2, rw2, k + 7168u);
      }
    };
    {
      __builtin_amdgcn_s_setprio(PRIO_H);
      const bool a1 = c < 10, b1_ = c < 9;
      const u32x4 r0 = mk_rsrc(a1 ? w1 : wqkv, a1 ? D * F * 2 : D * 3 * D * 2), r1 = mk_rsrc(b1_ ? w1 : wqkv, b1_ ? D * F * 2 : D * 3 * D * 2);
      const unsigned p0 = a1 ? 3584u : 2688u, p1 = b1_ ? 3584u : 2688u;
      unsigned V0, V1, V2, V3;
      {
        const unsigned kl = 32 * hs_ + (lane >> 3);
        const unsigned c0 = (a1 ? c : c - 10) * 384, c1 = c == 9 ? 512 : c0;
        const unsigned vk = (32 * hs_ + (lane >> 4)) * 2688 + ((224 * c - 1792) & ~63) + ((q ^ (lane >> 4)) << 6) + p * 16;
        V0 = kvt ? vk : kl * p0 + c0 + swz;
        V1 = kvt ? vk + 4 * 2688 : kl * p1 + c1 + swz + 128;
        V2 = kvt ? V0 + 896 : V1 + 128;
        V3 = V1 + 896;
      }
      int sg = (S0MODE ? s0 : 0) + hp; sg = sg == 7 ? 0 : sg;
      unsigned so = hp * slot_b;
#define GROUP(n) do { const unsigned kk = sg * 64 + 8 * (n), sa = kk * p0, sb_ = kk * p1; \
          if (i || hp) XGROUP(n); \
          dma16o<16384 + (n) * 1024>(lw, V0, r0, sa); \
          dma16o<24576 + (n) * 1024>(lw, V1, r1, sb_); \
          dma16o<32768 + (n) * 1024>(lw, V2, r1, sb_); \
          if (kvt) dma16o<40960 + (n) * 1024>(lw, V3, r1, sb_); } while (0)
      if (hp) BARRIER;
      unsigned t2;
#pragma unroll
      for (int i = 0; i < 4; ++i) {
        const int t = hp + 2 * i;
        if (i == 3 && hp) break;
        if (i >= 2 || (i == 1 && hp)) lds_wait(SY_DONE, 4, t - 2, lane);
        TSW(32 + t, 2 * hp);
        const unsigned lx = so + hs_ * 8192, lw = so + hs_ * 4096;
        GROUP(0); GROUP(1); GROUP(2); GROUP(3);
        TSW(40 + t, 2 * hp);
        if (i == 0 && !hp) BARRIER;
        if (i == 2 && hp) break;
        if (i == 3) lds_put(SY_GO, 1);
        if (i == 1) {
          LD1(t2, lane < (hp ? 0 : hs_ ? 13 : 49) ? lane * 128u : 0x80000000u, hs_ ? rwo : rw2, bid * (hs_ ? 1664u : 6272u), "");
          WAIT_VM(1);
        } else WAIT_VM(0);
        if (i == 2) asm volatile("" :: "v"(t2));
        lds_put(SY_FULL + h * 4, t + 2);
        TSW(48 + t, 2 * hp);
        if (!pure && ((i == 1 && hp) || i == 2)) {
#pragma unroll
          for (int n = 0; n < (i == 2 ? PFE : PFA); ++n) PFLD(n);
        }
        so = so == 0 ? 2 * slot_b : so - slot_b;
        sg = sg >= 5 ? sg - 5 : sg + 2;
      }
      tag = *(volatile LDS u32x2*)SY_TAG;
      if (hp) {
        u32x4 xf[8];
        unsigned bi;
        const bool chk = !hs_ || kvt || lane < 32;
        const u32x4 rb1 = mk_rsrc(b1, c < 10 ? F * 2 : 0);
#pragma unroll 1
        for (unsigned sp = 0; sp < SPIN_CAP; ++sp) {
#pragma unroll
          for (int k = 0; k < 7; ++k) LD4(xf[k], v16, rws, WS_CS + c * 14336u + hs_ * 1024u + k * 2048u, "sc1");
          LD4(xf[7], v16, rws, WS_ST + r * 1024u, "sc1");
          LD1(bi, lane * 4u, rb1, c * 384u + hs_ * 256u, "");
          WAIT_VM(9); lds_put(SY_FULL + h * 4, 7);
          TSW(53, 2);
          WAIT_VM(0);
          PIN(xf[0]); PIN(xf[7]); PIN(bi);
          if (kvt && l31 == 30) bi = 0x3f80u;
          unsigned bad = (xf[0].y ^ tag.x) | (xf[0].w ^ tag.x);
          float ax_ = __uint_as_float(xf[0].x), ay_ = __uint_as_float(xf[0].z);
#pragma unroll
          for (int k = 1; k < 7; ++k) {
            PIN(xf[k]);
            ax_ += __uint_as_float(xf[k].x); ay_ += __uint_as_float(xf[k].z); PIN(ax_); PIN(ay_);
            bad |= (xf[k].y ^ tag.x) | (xf[k].w ^ tag.x); PIN(bad);
          }
          const unsigned hi = pk2(ax_, ay_); ax_ -= bflo(hi); PIN(ax_); ay_ -= bfhi(hi);
          const unsigned mi = pk2(ax_, ay_); ax_ -= bflo(mi); PIN(ax_); ay_ -= bfhi(mi);
          const unsigned lo = pk2(ax_, ay_);
          *(LDS u32x4*)(L_CS + hs_ * 1024 + v16) = (u32x4){(hi & 0xffffu) | (mi << 16), (lo & 0xffffu) | (bi << 16),
                                                           (hi >> 16) | (mi & 0xffff0000u), (lo >> 16) | (bi & 0xffff0000u)};
          if (hs_) *(LDS u32x2*)(L_ST + v8) = (u32x2){xf[7].x, xf[7].z};
          else { float r0_ = __builtin_amdgcn_rcpf(bfhi(xf[7].x)) * LOG2E; PIN(r0_); *(LDS f32x2*)(L_RS + v8) = (f32x2){r0_, __builtin_amdgcn_rcpf(bfhi(xf[7].z)) * LOG2E}; }
          if (__builtin_expect(__all((bad == 0 || !chk) && xf[7].y == tag.x && xf[7].w == tag.x), 1)) break;
          __builtin_amdgcn_s_sleep(8);
        }
        lds_put(SY_FULL + h * 4, 8);
        TSW(3, 2);
        if (!pure) {
#pragma unroll
          for (int n = PFA; n < 18; ++n) PFLD(n);
        }
        lds_wait(SY_GO, 1, 1, lane);
        asm volatile("" :: "v"(t2));
      }
      __builtin_amdgcn_s_setprio(PRIO_P);
    }
    if (!pure) {
#pragma unroll
    for (int n = PFE; n < 18; ++n) { if (h < 2) PFLD(n); }
    const unsigned wb = 49152 + (h ^ 2) * 14336;
    lds_wait(SY_DONE, 4, h < 2 ? 6 : 5, lane);
#pragma unroll 1
    for (int n = 0; n < 14; ++n) dma16(wb + n * 1024, vwo, rwo, hg * 100352u + n * 7168u);
    lds_wait(SY_DONE, 4, 6, lane);
    fill(0, 16, 3);
    TSW(23, 0);
    WAIT_VM(6);
    bf16x8 WO[4][2][2];
    {
      const unsigned a0 = wb + (4 * hh + q) * 128 + (((q >> 1) & 1) << 6) + blk * 32 + p * 8, a1_ = a0 ^ 64u;
#pragma unroll
      for (int n = 0; n < 4; ++n)
#pragma unroll
        for (int sg = 0; sg < 2; ++sg) {
          const unsigned o = n * 3584 + sg * 2048;
          WO[n][sg][0] = tr_frag(a0 + o, a0 + o + 1024); WO[n][sg][1] = tr_frag(a1_ + o, a1_ + o + 1024);
        }
    }
#pragma unroll
    for (int n = 0; n < 8; ++n) {
      u32x4 t = __builtin_bit_cast(u32x4, WO[n >> 1][1][n & 1]);
      if (hh) { t.z = 0; t.w = 0; }
      WO[n >> 1][1][n & 1] = asbf(t);
    }
#pragma unroll
    for (int n = 0; n < 16; ++n) PIN(WO[n >> 2][(n >> 1) & 1][n & 1]);
    lds_put(SY_WOK + h * 4, 1);
    TSW(29, 0);
    if (EARLY) fill(16, 13, 3);
    lds_wait(SY_DONE, 4, c < 10 ? FILLAT : 7, lane);
    fill(3, 0, 6);
    lds_wait(SY_WOK, 4, 1, lane);
    fill(9, 6, 3);
    WAIT_VM(6); lds_put(SY_FULL2 + h * 4, 1);
    TSW(31, 0);
    fill(12, 9, EARLY ? 4 : 7);
    TSW(20, 0);
    lds_wait(SY_DONE, 4, 9, lane);
#pragma unroll
    for (int n = 0; n < 18; ++n) { PIN(R[n]); *(LDS u32x4*)((n < 6 ? 131072 : -24576) + h * 2048 + v16 + (n >> 1) * 8192 + (n & 1) * 1024) = R[n]; }
    WAIT_VM(0); lds_put(SY_RDY + h * 4, 1);
    TSW(4, 0);
    u32x4 PQ[2][7], KV[8];
    __builtin_amdgcn_s_setprio(PRIO_S);
    lds_wait(SY_HOK, 1, 1, lane);
    if (*(volatile LDS unsigned*)SY_HOK < 2u) tag_wait(min(lane, 4) * FS, rws, WS_FL + (rt * 32 + 19) * FS, ~0ull, tag);
#pragma unroll
    for (int n = 0; n < 14; ++n) LD4(PQ[n / 7][n % 7], v16, rws, WS_PQ + ((rt * 2 + n / 7) * 28 + hg * 7 + n % 7) * 1024u, "sc1");
    FIN_LD;
    TSW(19, 0);
    __builtin_amdgcn_s_setprio(PRIO_K);
    tag_wait(min(lane, 7) * FS, rws, WS_KF + (xc * 32 + hg * 8) * FS, ~0ull, tag);
#pragma unroll
    for (int n = 0; n < 8; ++n) LD4(KV[n], v16, rws, WS_KV + hg * 8192u + n * 1024u, "sc1");
    __builtin_amdgcn_s_setprio(PRIO_M);
    TSW(5, 0);
    u32x4 PB[2][4];
#pragma unroll
    for (int n = 0; n < 14; ++n) PIN(PQ[n / 7][n % 7]);
#pragma unroll
    for (int u = 0; u < 8; ++u) {
      const u32x4* P = PQ[u >> 2]; const int n = u & 3, lo = n < 3 ? 2 * n + 1 : 5, hi = n ? 2 * n - 1 : 1;
      PB[u >> 2][n] = (u32x4){P[lo].x, P[lo].y, P[hi].z, P[hi].w}; PIN(PB[u >> 2][n]);
    }
    WAIT_VM(4);
    TSW(16, 0);
#pragma unroll
    for (int n = 0; n < 4; ++n) PIN(KV[n]);
    f32x16 nm[3], as[2][2] = {{z16, z16}, {z16, z16}};
    u32x4 O[2][2]; float rd[2];
#define NUM0(u) nm[(u) % 3] = mfma(asbf(KV[2 * ((u) & 3)]), asbf(PQ[(u) >> 2][2 * ((u) & 3)]), z16)
#define NUM1(u) nm[(u) % 3] = mfma(asbf(KV[2 * ((u) & 3) + 1]), asbf(PB[(u) >> 2][(u) & 3]), nm[(u) % 3])
#define DEN(u) __uint_as_float(__builtin_amdgcn_permlane32_swap(__float_as_uint(nm[(u) % 3][12]), __float_as_uint(nm[(u) % 3][12]), false, false)[1])
#define APP(u, i) as[(u) >> 2][(i) & 1] = mfma(WO[(u) & 3][(i) >> 1][(i) & 1], asbf(O[(u) & 1][(i) >> 1]), as[(u) >> 2][(i) & 1])
#define MULS(a, b) _Pragma("unroll") for (int e = (a); e < (b); ++e) o[e] = mul1(nm[u % 3][e], rd[u & 1])
#define CVTS(a, b) _Pragma("unroll") for (int e = (a); e < (b); ++e) O[u & 1][e >> 2][e & 3] = pk2(o[2 * e], o[2 * e + 1])
    NUM0(0); NUM1(0); NUM0(1); NUM1(1);
    rd[0] = __builtin_amdgcn_rcpf(DEN(0) + 1e-6f);
    asm volatile("s_waitcnt vmcnt(0)" : "+v"(rd[0]), "+v"(KV[4]), "+v"(KV[5]), "+v"(KV[6]), "+v"(KV[7]) :: "memory");
    SB;
    unsigned d2 = 0; bool put0 = false;
#pragma unroll
    for (int u = 0; u < 8; ++u) {
      float o[16], dn;
      if (u) APP(u - 1, 0); SB; MULS(0, 6); SB;
      if (u) APP(u - 1, 1); SB; MULS(6, 10); CVTS(0, 2); SB;
      if (u) APP(u - 1, 2); SB; MULS(10, 13); CVTS(2, 4); SB;
      if (u) APP(u - 1, 3); SB; MULS(13, 16); CVTS(4, 6); SB;
      if (u < 6) NUM0(u + 2); SB; if (u < 7) dn = DEN(u + 1) + 1e-6f; SB;
      if (u < 6) NUM1(u + 2); SB; if (u < 7) rd[(u + 1) & 1] = __builtin_amdgcn_rcpf(dn); SB; CVTS(6, 8);
      if (u == 4) d2 = *(volatile LDS unsigned*)(SY_DONE2 + p * 4);
      if (u == 5) { put0 = __all(d2 >= 1u); if (put0) PUT_TILES(49152 + h * 16384, as[0]); }
      SB;
    }
    APP(7, 0); APP(7, 2); APP(7, 1); APP(7, 3);
    TSW(21, 0);
    if (!put0) { lds_wait(SY_DONE2, 4, 1, lane); PUT_TILES(49152 + h * 16384, as[0]); }
    PUT_TILES(57344 + h * 16384, as[1]);
    TSW(17, 0);
    }
  } else {
  const int cw = w - 4, wl = cw & 1, wh = cw >> 1;
  u32x4 xs[7];
  if (cw == 3) {
    const unsigned va = (unsigned)(r * 128 + c * 8 + (lane >> 3)) * 896u + (lane & 7) * 16;
#pragma unroll
    for (int k = 0; k < 7; ++k) LD4(xs[k], va, rx, k * 128u, "");
  }
  *(LDS unsigned*)(L_SY + lane * 4) = (lane >> 1) == 1; BARRIER;
  __builtin_amdgcn_s_setprio(PRIO_C);
  bf16x8 ones = asbf((u32x4){0x3f803f80u, 0x3f803f80u, 0x3f803f80u, 0x3f803f80u});
  PIN(ones);
  {
    {
      u64 ep;
      asm volatile("s_load_dwordx2 %0, %1, 0x0\n\ts_waitcnt lgkmcnt(0) vmcnt(0)" : "=&s"(ep) : "s"(ws) : "memory");
      tag.x = (unsigned)ep + 1u; tag.y = (unsigned)(ep >> 32) + 1u;
    }
    if (cw == 0) { asm volatile("" :: "v"(cpf)); *(volatile LDS u32x2*)SY_TAG = tag; }
    if (cw == 3) {
      float sq[2] = {0.f, 0.f}, sm[2] = {0.f, 0.f};
      const bf16x2 one2 = __builtin_bit_cast(bf16x2, 0x3f803f80u);
#pragma unroll
      for (int k = 0; k < 7; ++k) {
        PIN(xs[k]);
#pragma unroll
        for (int e = 0; e < 4; ++e) {
          const unsigned u = xs[k][e];
          const bf16x2 v = __builtin_bit_cast(bf16x2, u);
          sq[e & 1] = __builtin_amdgcn_fdot2_f32_bf16(v, v, sq[e & 1], false); sm[e & 1] = __builtin_amdgcn_fdot2_f32_bf16(v, one2, sm[e & 1], false);
        }
      }
      const float m = sum8(sm[0] + sm[1]) * (1.f / D);
      const float var = fmaxf(sum8(sq[0] + sq[1]) * (1.f / D) - m * m, 0.f);
      const u32x2 gr = {pk2(-m, __builtin_amdgcn_sqrtf(rbf(rbf(var) + 1e-5f))), tag.x};
      if ((lane & 7) == 0) ST2(gr, (unsigned)lane, rws, WS_ST + (r * 128 + c * 8) * 8, "sc1");
    }
  }
  const unsigned fl_off = WS_FL + ((r * 2 + wl) * 32 + 2 * c + wh) * FS;
  const unsigned fa = SY_FULL + p * 4;
#define CHECK_FULL(v, need) do { if (__builtin_expect(__any((v) < (need)), 0)) lds_wait(SY_FULL, 4, need, lane); FENCE; } while (0)
#define COLSUM(a_, t_, tile) do { const unsigned a = (a_); bf16x8 f[4]; \
    _Pragma("unroll") for (int ks = 0; ks < 4; ++ks) f[ks] = tr_frag(a + ks * 2048, a + ks * 2048 + (t_)); \
    SB; f32x16 cs = z16; \
    _Pragma("unroll") for (int ks = 0; ks < 4; ++ks) cs = mfma(ones, f[ks], cs); \
    const u32x2 gr = {__float_as_uint(cs[0]), tag.x}; \
    if (lane < 32) ST2(gr, v8, rws, WS_CS + (c * 7 + s0) * 2048 + (tile) * 256, "sc1"); } while (0)
  unsigned da = SY_DONE + cw * 4;
  PIN(da);
  const unsigned xm = hh ? 0u : ~0u;
  if (!kvt) {
    const unsigned xa = (wl * 64 + l31) * 128 + ((hh ^ ((l31 >> 1) & 7)) << 4);
    const unsigned wr = 16384 + (8 * hh + q) * 128 + blk * 32 + p * 8;
    const unsigned wa0 = wr + wh * 8192 + ((wh ^ (q >> 1)) << 6), wa1 = wr + wh * 16384 + ((wh ^ 1 ^ (q >> 1)) << 6);
    f32x16 acc[2][3] = {{z16, z16, z16}, {z16, z16, z16}};
    bf16x8 X[4][2], W[4][3];
#define RDA(n, ks) do { \
      if ((n) == 0 || (n) == 2) X[ks][(n) >> 1] = ld_frag(ax[ks] + ((n) >> 1) * 4096); \
      else { const unsigned a_ = ((n) == 3 ? a1_ : a0_) + ((n) == 4 ? 8192 : 0) + (ks) * 2048; W[ks][(n) == 1 ? 0 : (n) - 2] = tr_frag(a_, a_ + 512); } } while (0)
#define MMA(ks, m) acc[(m) & 1][(m) >> 1] = mfma(W[ks][(m) >> 1], X[ks][(m) & 1], acc[(m) & 1][(m) >> 1])
    unsigned ax[4] = {xa, xa ^ 32u, xa ^ 64u, xa ^ 96u}, a0_ = wa0, a1_ = wa1;
#pragma unroll
    for (int n = 0; n < 6; ++n) PIN(acc[n & 1][n >> 1]);
    lds_wait(SY_FULL, 4, 1, lane);
    TSW(6, 4); TSC(24, 4);
    if (r < 14 && !((r & 1) && wl)) {
      const int b = (r & 1) ? 2 : wl;
      COLSUM(b == 1 ? wa1 : wa0 + b * 4096, 512, 3 * wh + b);
    }
#pragma unroll
    for (int n = 0; n < 10; ++n) { RDA(n % 5, n / 5); SB; }
    unsigned tv = 1; int ts = 0;
    PIN(tv);
#pragma unroll 1
    for (;;) {
      unsigned fv; int dl;
#pragma unroll
      for (int m = 0; m < 12; ++m) {
        MMA(m / 6, m % 6); SB;
        if (m % 6 < 5) RDA(m % 6, 2 + m / 6);
        if (m == 0) { dl = ((0x24 >> ts) & 1) ? -81920 : 40960; ++ts; asm volatile("" : "+s"(dl), "+s"(ts)); }
        if (m == 2) { ax[0] += dl; ax[1] += dl; PIN(ax[0]); PIN(ax[1]); }
        if (m == 5) fv = *(volatile LDS unsigned*)fa;
        if (m == 10) *(volatile LDS unsigned*)da = tv;
        if (m == 11) { a0_ += dl; a1_ += dl; ++tv; CHECK_FULL(fv, tv); TSW(55 + ts, STW); }
        SB;
      }
      if (__builtin_expect(ts == 7, 0)) break;
#pragma unroll
      for (int m = 0; m < 12; ++m) {
        MMA(2 + m / 6, m % 6); SB;
        if (m % 6 < 5) RDA(m % 6, m / 6);
        if (m == 5) { ax[2] += dl; ax[3] += dl; }
        SB;
      }
    }
    TSW(7, 4); TSC(25, 4);
    unsigned st[2]; u32x2 cv[3];
#pragma unroll
    for (int a = 0; a < 2; ++a) st[a] = *(LDS const unsigned*)(L_ST + (wl * 64 + a * 32 + l31) * 4);
#pragma unroll
    for (int b = 0; b < 3; ++b) cv[b] = *(LDS const u32x2*)(L_CS + ((3 * wh + b) * 32 + l31) * 8);
    SB;
    MMA(2, 0); MMA(2, 1); MMA(3, 0); MMA(3, 1);
    SB;
    bf16x8 xe[2];
#pragma unroll
    for (int a = 0; a < 2; ++a) xe[a] = asbf((u32x4){((st[a] & 0xffffu) | (st[a] << 16)) & xm, st[a] & xm, 0u, 0u});
#define MME(m) acc[(m) & 1][(m) >> 1] = mfma(asbf((u32x4){cv[(m) >> 1].x, cv[(m) >> 1].y, cv[(m) >> 1].x, cv[(m) >> 1].y}), xe[(m) & 1], acc[(m) & 1][(m) >> 1])
    MME(0); MME(1);
    SB;
    TSW(18, 4);
    const int R0 = r * 4 + wl * 2;
    const unsigned sk = c == 9 ? 0x40000000u : 0u;
#define MM6(b, j) do { if ((j) < 4) MMA(2 + ((j) >> 1), 2 * (b) + ((j) & 1)); else MME(2 * (b) + ((j) & 1)); } while (0)
    if (2 * c + wh < 19) {
      const unsigned ob = WS_H + (R0 * 112 + 12 * c + 6 * wh) * 1024u;
#pragma unroll
      for (int b = 0; b < 3; ++b)
#pragma unroll
        for (int n = 0; n < 4; ++n) {
          const int a = n >> 1, e = 8 * (n & 1);
          float t[8]; u32x4 o;
          if (b < 2) MM6(b + 1, n < 2 ? 2 * n : n + 2);
          SB;
          if (n == 0) asm volatile("s_nop 10" : "+v"(acc[0][b]), "+v"(acc[1][b]));
          asm volatile("v_mul_f32_e64 %0, %8, |%8|\n\tv_mul_f32_e64 %1, %9, |%9|\n\tv_mul_f32_e64 %2, %10, |%10|\n\tv_mul_f32_e64 %3, %11, |%11|\n\t"
                       "v_mul_f32_e64 %4, %12, |%12|\n\tv_mul_f32_e64 %5, %13, |%13|\n\tv_mul_f32_e64 %6, %14, |%14|\n\tv_mul_f32_e64 %7, %15, |%15|"
                       : "=&v"(t[0]), "=&v"(t[1]), "=&v"(t[2]), "=&v"(t[3]), "=&v"(t[4]), "=&v"(t[5]), "=&v"(t[6]), "=&v"(t[7])
                       : "v"(acc[a][b][e]), "v"(acc[a][b][e + 1]), "v"(acc[a][b][e + 2]), "v"(acc[a][b][e + 3]), "v"(acc[a][b][e + 4]), "v"(acc[a][b][e + 5]),
                         "v"(acc[a][b][e + 6]), "v"(acc[a][b][e + 7]));
          SB;
          if (b < 2 && n < 2) MM6(b + 1, 2 * n + 1);
          SB;
#pragma unroll
          for (int j = 0; j < 4; ++j) o[j] = pk2(t[2 * j], t[2 * j + 1]);
#pragma unroll
          for (int j = 0; j < 4; ++j) o[j] = relu2(o[j]);
          ST4(o, v16, rws, ob + a * (112 * 1024u) + (2 * b + (n & 1)) * 1024u + (b == 2 ? sk : 0u), HMOD);
          SB;
        }
    } else {
#pragma unroll
      for (int j = 0; j < 12; ++j) MM6(1 + j / 6, j % 6);
      const unsigned ob = WS_PQ + (R0 * 28 + (c == 9 ? 22 : (c - 10) * 12 + wh * 6)) * 1024u;
      SB;
#pragma unroll
      for (int a = 0; a < 2; ++a) {
        const float rl = __builtin_amdgcn_rcpf(bfhi(st[a])) * LOG2E;
#pragma unroll
        for (int n = 0; n < 6; ++n) {
          f32x2 u[4];
#pragma unroll
          for (int j = 0; j < 4; ++j) u[j] = (f32x2){acc[a][n >> 1][8 * (n & 1) + 2 * j], acc[a][n >> 1][8 * (n & 1) + 2 * j + 1]} * rl;
          PHI_A(u); PHI_E(0); PHI_E(2); PHI_B(o);
          ST4(o, v16, rws, ob + a * (28 * 1024u) + n * 1024u + (n < 2 ? sk : 0u), HMOD);
          SB;
        }
      }
    }
  } else {
    const unsigned xa = l31 * 128 + ((hh ^ ((l31 >> 1) & 7)) << 4);
    const int ok_ = (c & 1) * 32 + 56 * cw + 8 * ((4 * blk + p + cw) & 7), ov_ = (c & 1) * 32 + 56 * cw + 32 * blk + 8 * p;
    const unsigned ka = 16384 + hh * 1024 + q * 256 + ((((ok_ >> 6) & 3) ^ q) << 6) + (ok_ & 63);
    const unsigned va = 32768 + hh * 1024 + q * 256 + ((((ov_ >> 6) & 3) ^ q) << 6) + (ov_ & 63) + ((lane & 19) == 19 ? 0x100000 : 0);
    f32x16 acc[4][2] = {{z16, z16}, {z16, z16}, {z16, z16}, {z16, z16}};
    bf16x8 X[4][4], W[4][2];
#define RDK(n, ks) do { \
      if ((n) == 1 || (n) == 5) { const unsigned a_ = ((n) == 1 ? a0_ : a1_) + (ks) * 2048; W[ks][(n) >> 2] = tr_frag(a_, a_ + 8192); } \
      else X[ks][(n) ? (n) - 1 : 0] = ld_frag(ax[ks] + ((n) ? (n) - 1 : 0) * 4096); } while (0)
#define MMK(ks, m) acc[(m) & 3][(m) >> 2] = mfma(X[ks][(m) & 3], W[ks][(m) >> 2], acc[(m) & 3][(m) >> 2])
    unsigned ax[4] = {xa, xa ^ 32u, xa ^ 64u, xa ^ 96u}, a0_ = ka, a1_ = va;
#pragma unroll
    for (int n = 0; n < 8; ++n) PIN(acc[n & 3][n >> 2]);
    lds_wait(SY_FULL, 4, 1, lane);
    if (r < 14) COLSUM((r & 1) ? va : ka, 8192, 2 * cw + (r & 1));
#pragma unroll
    for (int n = 0; n < 12; ++n) { RDK(n % 6, n / 6); SB; }
    unsigned tv = 1; int ts = 0;
    PIN(tv);
#pragma unroll 1
    for (;;) {
      unsigned fv; int dl;
#pragma unroll
      for (int m = 0; m < 16; ++m) {
        MMK(m >> 3, m & 7); SB;
        if ((m & 7) >= 1 && (m & 7) < 7) RDK((m & 7) - 1, 2 + (m >> 3));
        if (m == 0) { dl = ((0x24 >> ts) & 1) ? -98304 : 49152; ++ts; asm volatile("" : "+s"(dl), "+s"(ts)); }
        if (m == 7) { ax[0] += dl; ax[1] += dl; PIN(ax[0]); PIN(ax[1]); }
        if (m == 8) fv = *(volatile LDS unsigned*)fa;
        if (m == 14) *(volatile LDS unsigned*)da = tv;
        if (m == 15) { a0_ += dl; a1_ += dl; ++tv; CHECK_FULL(fv, tv); TSW(55 + ts, STW); }
        SB;
      }
      if (__builtin_expect(ts == 7, 0)) break;
#pragma unroll
      for (int m = 0; m < 16; ++m) {
        MMK(2 + (m >> 3), m & 7); SB;
        if ((m & 7) >= 1 && (m & 7) < 7) RDK((m & 7) - 1, m >> 3);
        if (m == 7) { ax[2] += dl; ax[3] += dl; }
        SB;
      }
    }
    TSW(8, 4);
    unsigned st[4]; u32x2 cv[2]; f32x4 rsv[2][4];
#pragma unroll
    for (int a = 0; a < 4; ++a) st[a] = *(LDS const unsigned*)(L_ST + (a * 32 + l31) * 4);
#pragma unroll
    for (int b = 0; b < 2; ++b) cv[b] = *(LDS const u32x2*)(L_CS + ((2 * cw + b) * 32 + l31) * 8);
    unsigned rsa = L_RS + hh * 16;
    PIN(rsa);
#define MK6(a, j) do { if ((j) < 4) MMK(2 + ((j) >> 1), (a) + 4 * ((j) & 1)); else acc[a][(j) & 1] = mfma(asbf((u32x4){((st[a] & 0xffffu) | (st[a] << 16)) & xm, st[a] & xm, 0u, 0u}), \
        asbf((u32x4){cv[(j) & 1].x, cv[(j) & 1].y, cv[(j) & 1].x, cv[(j) & 1].y}), acc[a][(j) & 1]); } while (0)
#pragma unroll
    for (int n = 0; n < 4; ++n) rsv[0][n] = *(LDS const f32x4*)(rsa + n * 32);
    SB;
#pragma unroll
    for (int j = 0; j < 6; ++j) MK6(0, j);
    SB;
    f32x16 kv = z16;
    u32x4 fk, fv;
#pragma unroll
    for (int gi = 0; gi < 8; ++gi) {
      const int a = gi >> 1, sg = gi & 1, e = 8 * sg;
      if (a < 3 && sg == 0) {
#pragma unroll
        for (int n = 0; n < 4; ++n) rsv[(a + 1) & 1][n] = *(LDS const f32x4*)(rsa + (a + 1) * 128 + n * 32);
      }
      const f32x4 ra_ = rsv[a & 1][2 * sg], rb_ = rsv[a & 1][2 * sg + 1];
      const f32x2 rl[4] = {{ra_[0], ra_[1]}, {ra_[2], ra_[3]}, {rb_[0], rb_[1]}, {rb_[2], rb_[3]}};
      f32x2 u[4], vv[4];
#pragma unroll
      for (int j = 0; j < 4; ++j) u[j] = (f32x2){acc[a][0][e + 2 * j], acc[a][0][e + 2 * j + 1]} * rl[j];
#pragma unroll
      for (int j = 0; j < 4; ++j) vv[j] = (f32x2){acc[a][1][e + 2 * j], acc[a][1][e + 2 * j + 1]} * rl[j];
      PHI_A(u);
      if (gi) kv = mfma(asbf(fk), asbf(fv), kv);
      SB;
      PHI_E(0);
      if (a < 3) MK6(a + 1, 3 * sg);
      SB;
      PHI_E(2);
      if (a < 3) MK6(a + 1, 3 * sg + 1);
      SB;
      fv = (u32x4){pk2(vv[0].x, vv[0].y), pk2(vv[1].x, vv[1].y), pk2(vv[2].x, vv[2].y), pk2(vv[3].x, vv[3].y)};
      SB;
      if (a < 3) MK6(a + 1, 3 * sg + 2);
      SB;
      float po[8];
#pragma unroll
      for (int j = 0; j < 8; ++j) { po[j] = pe[j >> 1][j & 1] + 1e-6f; PIN(po[j]); }
#pragma unroll
      for (int j = 0; j < 4; ++j) pm[j] = __builtin_elementwise_fma(pm[j], (f32x2){UP / LOG2E, UP / LOG2E}, (f32x2){po[2 * j], po[2 * j + 1]});
      fk = (u32x4){pk2(pm[0].x, pm[0].y), pk2(pm[1].x, pm[1].y), pk2(pm[2].x, pm[2].y), pk2(pm[3].x, pm[3].y)};
      SB;
    }
    kv = mfma(asbf(fk), asbf(fv), kv);
    kv = kv * (l31 == 28 ? 1.f / 1.4453125f : 1.f / LOG2E);
    const unsigned ko = WS_KVP + (((c - 12) * 4 + cw) * 16 + r) * 4096u;
#pragma unroll
    for (int e = 0; e < 4; ++e) {
      const u32x4 o = {pk2(kv[4 * e], kv[4 * e + 1]), tag.x, pk2(kv[4 * e + 2], kv[4 * e + 3]), tag.x};
      ST4(o, v16, rws, ko + e * 1024u, "sc1");
    }
  }
  lds_put(da, 8);
  TSW(22, 4);
  bf16x8 WH[9][4];
  unsigned ra[3][2];
#pragma unroll
  for (int cb = 0; cb < 2; ++cb) {
    ra[0][cb] = wh * 4096 + (4 * hh + q) * 128 + ((cb ^ (q >> 1)) << 6) + blk * 32 + p * 8; ra[1][cb] = ra[0][cb] + 65536; ra[2][cb] = ra[0][cb] + 131072;
    PIN(ra[0][cb]); PIN(ra[1][cb]); PIN(ra[2][cb]);
  }
#define RDW(dst, n, reg) do { const unsigned a_ = ra[(reg) >> 3][(n) & 1] + ((reg) & 7) * 8192 + ((n) >> 1) * 2048; dst[n] = tr_frag(a_, a_ + 1024); } while (0)
  if (!kvt) WAIT_VM(0);
  if (lane == 0) ST2(tag, 0u + lane, rws, fl_off, "sc1");
  TSW(9, 4);
  if (!pure) {
    lds_wait(SY_FULL2, 4, 1, lane);
#pragma unroll
    for (int n = 0; n < 36; ++n) RDW(WH[n >> 2], n & 3, (16 + (n >> 2)) % 19);
    lds_put(da, 9);
  }
  TSW(30, 4);

  if (!pure) {
    const int kh = wh, R = rt * 2 + wl;
    if (cw == 0) {
      const u64 bl = tag_wait(min(lane, 23) * FS, rws, WS_FL + rt * 32 * FS, 0x7FFFFull, tag);
      lds_put(SY_HOK, (bl & 0xF80000ull) == 0xF80000ull ? 2 : 1);
    } else lds_wait(SY_HOK, 1, 1, lane);
    WAIT_VM(0);
    TSW(12, 4);
    const unsigned hs = WS_H + (R * 112 + kh * 56) * 1024u;
    constexpr int NH = NHW, E = 28 - NH;
    u32x4 hf[NH][2];
#define HLD(u) do { const unsigned o_ = hs + ROT(u) * 2048u; \
      LD4(hf[(u) % NH][0], v16, rws, o_, "sc1"); LD4(hf[(u) % NH][1], v16, rws, o_, "offset:1024 sc1"); } while (0)
#pragma unroll
    for (int u = 0; u < NH; ++u) HLD(u);
    f32x16 af[2] = {z16, z16};
    bf16x8 W[LA + 1][4];
    TSW(13, 4); TSC(26, 4);
#pragma unroll
    for (int u = 0; u < 28; ++u) {
      constexpr auto total = [](int u_) { return 2 * NH + 2 * (u_ < E ? u_ : E) + (u_ > E ? 3 : 0); };
      asm volatile("s_waitcnt vmcnt(%2)" : "+v"(hf[u % NH][0]), "+v"(hf[u % NH][1]) : "n"(total(u) - 2 - 2 * u) : "memory");
      if (u == 9 - LA) { lds_wait(SY_RDY, 4, 1, lane); TSW(28, 4); }
#pragma unroll
      for (int n = 0; n < 4; ++n) {
        af[n & 1] = mfma(u < 9 ? WH[u < 9 ? u : 0][n] : W[u % (LA + 1)][n], asbf(hf[u % NH][n >> 1]), af[n & 1]); SB;
        if (u + LA >= 9 && u + LA < 28) RDW(W[(u + LA) % (LA + 1)], n, (u + LA - 3) % 19);
        SB;
      }
      if (u == 16 - LA) lds_put(SY_DONE2 + cw * 4, 1);
      if (u == 20 - LA) lds_put(SY_DONE2 + cw * 4, 2);
      if (u < E) HLD(u + NH);
      else if (u == E) FIN_LD;
    }
    TSW(14, 4); TSC(27, 4);
    __builtin_amdgcn_s_setprio(0);
    lds_wait(SY_DONE2, 4, 2, lane);
    PUT_TILES(114688 + cw * 8192, af);
    WAIT_VM(0);
  }
  }
  if (pure) {
    const int id = xc * 4 + (ix >> 3), hd = id >> 1;
    lds_wait(SY_DONE, 4, 8, lane);
    f32x4 a, b;
#pragma unroll 1
    for (unsigned sp = 0; sp < SPIN_CAP; ++sp) {
      u32x4 pr[4];
#pragma unroll
      for (int n = 0; n < 4; ++n) LD4(pr[n], v16, rws, WS_KVP + (hd * 16 + 2 * w + (n >> 1)) * 4096u + (id & 1) * 2048u + (n & 1) * 1024u, "sc1");
      WAIT_VM(0);
      unsigned bad = 0;
#pragma unroll
      for (int n = 0; n < 4; ++n) { PIN(pr[n]); bad |= (pr[n].y ^ tag.x) | (pr[n].w ^ tag.x); }
#define F4(n) (f32x4){bflo(pr[n].x), bfhi(pr[n].x), bflo(pr[n].z), bfhi(pr[n].z)}
      a = F4(0) + F4(2); b = F4(1) + F4(3);
      if (__all(bad == 0)) break;
      __builtin_amdgcn_s_sleep(RSLEEP);
    }
    TSW(19, 4);
    *(LDS f32x4*)(w * 2048 + v16) = a; *(LDS f32x4*)(w * 2048 + 1024 + v16) = b;
    BARRIER;
    TSW(20, 4);
    if (w == 4) {
      a = *(LDS const f32x4*)v16; b = *(LDS const f32x4*)(1024 + v16);
#pragma unroll
      for (int n = 1; n < 8; ++n) { a += *(LDS const f32x4*)(n * 2048 + v16); b += *(LDS const f32x4*)(n * 2048 + 1024 + v16); }
      u32x4 o = {pk2(a[0], a[1]), pk2(a[2], a[3]), pk2(b[0], b[1]), pk2(b[2], b[3])};
      if ((id & 1) && hh != (hd & 1)) { if (hd & 2) { o.x = 0; o.y = 0; } else { o.z = 0; o.w = 0; } }
      ST4(o, v16, rws, WS_KV + id * 1024u, "sc1");
      TSW(21, 4);
      WAIT_VM(0);
      if (lane < 8) ST2(tag, lane * 32 * FS, rws, WS_KF + id * FS, "sc1");
      TSW(10, 4); TS_DUMP;
    } else if (w == 5 && bid == 255) {
#pragma unroll 1
      for (int n = 0; n < 4; ++n) tag_wait((unsigned)((lane >> 4) * 64 + (lane & 15) * 2) * FS, rws, WS_FL + n * 256 * FS, ~0ull, tag);
      if (lane == 0) ST2(tag, 0u + lane, rws, WS_EP, "sc1");
    }
    return;
  }
  BARRIER;
  TSW(15, 4);
  {
    const unsigned a0 = 49152 + (tid >> 8) * 8192 + (tid & 7) * 1024 + ((((tid >> 3) & 31) ^ ((tid & 7) * 2)) << 4);
    f32x4 f[2][2], sp[4][2];
#pragma unroll
    for (int n = 0; n < 2; ++n) { f[n][0] = *(LDS const f32x4*)(a0 + 65536 + n * 16384); f[n][1] = *(LDS const f32x4*)(a0 + 65536 + n * 16384 + 512); }
#pragma unroll
    for (int n = 0; n < 4; ++n) { sp[n][0] = *(LDS const f32x4*)(a0 + n * 16384); sp[n][1] = *(LDS const f32x4*)(a0 + n * 16384 + 512); }
    SB;
    PIN(xr); PIN(br); PIN(sg_);
    const float rs = __builtin_amdgcn_rcpf(bfhi(sg_)), rs2 = rs * rs;
    u32x4 ov;
#pragma unroll
    for (int e = 0; e < 4; ++e) {
      const int hf_ = e >> 1, j = (e & 1) * 2;
      const float t0 = fmaf(f[0][hf_][j] + f[1][hf_][j], rs2, bflo(br[e])), t1 = fmaf(f[0][hf_][j + 1] + f[1][hf_][j + 1], rs2, bfhi(br[e]));
      const float g0 = t0 * __builtin_amdgcn_rcpf(1.f + __expf(-t0)), g1 = t1 * __builtin_amdgcn_rcpf(1.f + __expf(-t1));
      const float s0_ = (sp[0][hf_][j] + sp[1][hf_][j]) + (sp[2][hf_][j] + sp[3][hf_][j]);
      const float s1_ = (sp[0][hf_][j + 1] + sp[1][hf_][j + 1]) + (sp[2][hf_][j + 1] + sp[3][hf_][j + 1]);
      ov[e] = pk2(fmaf(s0_, g0, bflo(xr[e])), fmaf(s1_, g1, bfhi(xr[e])));
    }
    ST4(ov, fxo, mk_rsrc(out, S * D * 2), 0u, "");
    TSW(11, 4);
    if (w == 4) TS_DUMP;
  }
}

extern "C" void kernel_launch(void* const* d_in, const int* in_sizes, int n_in, void* d_out, int out_size, void* d_ws,
                              size_t ws_size, hipStream_t stream) {
  (void)hipFuncSetAttribute((const void*)fused, hipFuncAttributeMaxDynamicSharedMemorySize, LDS_BYTES);
  fused<<<256, 512, LDS_BYTES, stream>>>((const u16*)d_in[0], (const u16*)d_in[1], (const u16*)d_in[4], (char*)d_ws, (const u16*)d_in[2],
                                         (const u16*)d_in[5], (const u16*)d_in[6], (const u16*)d_in[7], (u16*)d_out);
}
```

```cpp
#include <hip/hip_runtime.h>
#include <hip/hip_bf16.h>
#include <stdint.h>
#pragma clang diagnostic ignored "-Wint-to-pointer-cast"

typedef unsigned short u16;
typedef unsigned long long u64;
typedef __bf16 bf16x8 __attribute__((ext_vector_type(8)));
typedef __bf16 bf16x2 __attribute__((ext_vector_type(2)));
typedef short s16x4 __attribute__((ext_vector_type(4)));
typedef float f32x4 __attribute__((ext_vector_type(4)));
typedef float f32x16 __attribute__((ext_vector_type(16)));
typedef float f32x2 __attribute__((ext_vector_type(2)));
typedef unsigned u32x4 __attribute__((ext_vector_type(4)));
typedef unsigned u32x2 __attribute__((ext_vector_type(2)));
#define LDS __attribute__((address_space(3)))
#define DI __device__ __forceinline__

constexpr int S = 2048, D = 448, F = 1792;
constexpr unsigned FS = 128;
constexpr int PFA = 9, PFE = 4, NHW = 7, LA = 2;
constexpr unsigned WS_EP = 0;
constexpr unsigned WS_KF = 1024;
constexpr unsigned WS_FL = WS_KF + 256 * FS;
constexpr unsigned WS_ST = WS_FL + 1024 * FS;
constexpr unsigned WS_CS = WS_ST + 16384;
constexpr unsigned WS_KV = WS_CS + 16 * 7 * 2048;
constexpr unsigned WS_KVP = WS_KV + 32768;
constexpr unsigned WS_PQ = WS_KVP + 16 * 16 * 4096;
constexpr unsigned WS_H = WS_PQ + 64 * 28 * 1024;
constexpr unsigned WS_END = WS_H + 64 * 112 * 1024;
constexpr unsigned L_CS = 155648, L_ST = L_CS + 2048, L_RS = L_ST + 512, L_SY = L_RS + 512, L_END = L_SY + 256;
constexpr unsigned SY_FULL = L_SY, SY_DONE = L_SY + 32, SY_RDY = L_SY + 48, SY_FULL2 = L_SY + 64, SY_DONE2 = L_SY + 80,
                   SY_GO = L_SY + 112, SY_WOK = L_SY + 128, SY_HOK = L_SY + 100, SY_TAG = L_SY + 104;
constexpr unsigned CODE_BYTES = 31872;
constexpr unsigned SPIN_CAP = 1u << 19, LDS_CAP = 1u << 23;

DI u32x4 mk_rsrc(const void* p, unsigned bytes) {
  uint64_t a = (uint64_t)p;
  u32x4 r; r.x = (unsigned)a; r.y = (unsigned)(a >> 32) & 0xffffu; r.z = bytes; r.w = 0x00020000u; return r;
}
DI void dma16(unsigned lds_addr, unsigned voff, u32x4 rsrc, unsigned soff) {
  asm volatile("s_mov_b32 m0, %0\n\ts_nop 0\n\tbuffer_load_dwordx4 %1, %2, %3 offen lds"
               :: "s"(lds_addr), "v"(voff), "s"(rsrc), "s"(soff) : "memory");
}
template <unsigned OFF> DI void dma16o(unsigned lds_base, unsigned voff, u32x4 rsrc, unsigned soff) {
  asm volatile("s_add_u32 m0, %0, %4\n\ts_nop 0\n\tbuffer_load_dwordx4 %1, %2, %3 offen lds"
               :: "s"(lds_base), "v"(voff), "s"(rsrc), "s"(soff), "n"(OFF) : "memory", "scc");
}
#define SB __builtin_amdgcn_sched_barrier(0)
#define FENCE asm volatile("" ::: "memory")
#define PIN(a) asm volatile("" : "+v"(a))
#define WAIT_VM(n) asm volatile("s_waitcnt vmcnt(%0)" :: "n"(n) : "memory")
#define WAIT_LDS asm volatile("s_waitcnt lgkmcnt(0)" ::: "memory")
#define BARRIER asm volatile("s_waitcnt lgkmcnt(0)\n\ts_barrier" ::: "memory")
#define LD4(dst, voff, rsrc, soff, mod) asm volatile("buffer_load_dwordx4 %0, %1, %2, %3 offen " mod : "=v"(dst) : "v"(voff), "s"(rsrc), "s"(soff) : "memory")
#define LD2(dst, voff, rsrc, soff, mod) asm volatile("buffer_load_dwordx2 %0, %1, %2, %3 offen " mod : "=v"(dst) : "v"(voff), "s"(rsrc), "s"(soff) : "memory")
#define LD1(dst, voff, rsrc, soff, mod) asm volatile("buffer_load_dword %0, %1, %2, %3 offen " mod : "=v"(dst) : "v"(voff), "s"(rsrc), "s"(soff) : "memory")
#define ST4(src, voff, rsrc, soff, mod) asm volatile("buffer_store_dwordx4 %0, %1, %2, %3 offen " mod "\n\ts_nop 1" :: "v"(src), "v"(voff), "s"(rsrc), "s"(soff) : "memory")
#define ST2(src, voff, rsrc, soff, mod) asm volatile("buffer_store_dwordx2 %0, %1, %2, %3 offen " mod :: "v"(src), "v"(voff), "s"(rsrc), "s"(soff) : "memory")
DI f32x16 mfma(bf16x8 a, bf16x8 b, f32x16 c) { return __builtin_amdgcn_mfma_f32_32x32x16_bf16(a, b, c, 0, 0, 0); }
DI bf16x8 asbf(u32x4 v) { return __builtin_bit_cast(bf16x8, v); }
DI bf16x8 ld_frag(unsigned a) { return *(LDS const bf16x8*)a; }
DI bf16x8 tr_frag(unsigned a0, unsigned a1) {
  s16x4 lo = __builtin_amdgcn_ds_read_tr16_b64_v4i16((LDS s16x4*)a0);
  s16x4 hi = __builtin_amdgcn_ds_read_tr16_b64_v4i16((LDS s16x4*)a1);
  return __builtin_bit_cast(bf16x8, __builtin_shufflevector(lo, hi, 0, 1, 2, 3, 4, 5, 6, 7));
}
DI unsigned pk2(float a, float b) { f32x2 v = {a, b}; return __builtin_bit_cast(unsigned, __builtin_convertvector(v, bf16x2)); }
DI float bflo(unsigned u) { return __uint_as_float(u << 16); }
DI float bfhi(unsigned u) { return __uint_as_float(u & 0xffff0000u); }
DI float rbf(float f) { return bflo(pk2(f, 0.f)); }
constexpr float LOG2E = 1.44269504f, EPS2 = 1e-6f * LOG2E;
DI f32x2 phi_u(float a0, float a1, f32x2 rl) { return __builtin_elementwise_fma((f32x2){a0, a1}, rl, (f32x2){EPS2, EPS2}); }
#define PHI_E(j0) _Pragma("unroll") for (int j_ = (j0); j_ < (j0) + 2; ++j_) pe[j_] = (f32x2){__builtin_amdgcn_fmed3f(__builtin_amdgcn_exp2f(u[j_].x), 0.f, 1.f), \
                                                                                     __builtin_amdgcn_fmed3f(__builtin_amdgcn_exp2f(u[j_].y), 0.f, 1.f)}; SB
#define PHI_B(o) _Pragma("unroll") for (int j_ = 0; j_ < 4; ++j_) u[j_] = (f32x2){fmaxf(u[j_].x, EPS2), fmaxf(u[j_].y, EPS2)}; SB; \
              _Pragma("unroll") for (int j_ = 0; j_ < 4; ++j_) u[j_] = __builtin_elementwise_fma(u[j_], (f32x2){1.f / LOG2E, 1.f / LOG2E}, pe[j_]); SB; \
              o = (u32x4){pk2(u[0].x, u[0].y), pk2(u[1].x, u[1].y), pk2(u[2].x, u[2].y), pk2(u[3].x, u[3].y)}
DI unsigned relu2(unsigned v) {
  typedef short s16x2 __attribute__((ext_vector_type(2)));
  return __builtin_bit_cast(unsigned, __builtin_elementwise_max(__builtin_bit_cast(s16x2, v), (s16x2){0, 0}));
}
DI float mul1(float a, float b) { float r; asm("v_mul_f32_e32 %0, %1, %2" : "=v"(r) : "v"(a), "v"(b)); return r; }
DI float sum8(float v) {
  v += __uint_as_float(__builtin_amdgcn_update_dpp(0u, __float_as_uint(v), 0xB1, 0xf, 0xf, true));
  v += __uint_as_float(__builtin_amdgcn_update_dpp(0u, __float_as_uint(v), 0x4E, 0xf, 0xf, true));
  v += __uint_as_float(__builtin_amdgcn_update_dpp(0u, __float_as_uint(v), 0x141, 0xf, 0xf, true));
  return v;
}
DI void lds_put(unsigned a, unsigned v) { FENCE; *(volatile LDS unsigned*)a = v; FENCE; }
DI void lds_wait(unsigned a, int n, unsigned need, int lane) {
  const unsigned ad = a + (lane & (n - 1)) * 4;
#pragma unroll 1
  for (unsigned sp = 0; sp < LDS_CAP; ++sp) {
    FENCE; const unsigned v = *(volatile LDS unsigned*)ad; FENCE;
    if (__all(v >= need)) break;
    __builtin_amdgcn_s_sleep(1);
  }
}
DI u64 tag_wait(unsigned voff, u32x4 rs, unsigned soff, u64 mask, u32x2 tag) {
  u64 bl = 0;
#pragma unroll 1
  for (unsigned sp = 0; sp < SPIN_CAP; ++sp) {
    u32x2 v;
    asm volatile("buffer_load_dwordx2 %0, %1, %2, %3 offen sc1\n\ts_waitcnt vmcnt(0)" : "=&v"(v) : "v"(voff), "s"(rs), "s"(soff) : "memory");
    bl = __ballot(v.x == tag.x && v.y == tag.y);
    if ((bl & mask) == mask) break;
    __builtin_amdgcn_s_sleep(4);
  }
  return bl;
}

__global__ __launch_bounds__(512, 2) void fused(const u16* __restrict__ x, const u16* __restrict__ wqkv, const u16* __restrict__ w1, char* ws,
                                                const u16* __restrict__ wo, const u16* __restrict__ b1, const u16* __restrict__ w2,
                                                const u16* __restrict__ b2, u16* __restrict__ out) {
  const int bid = blockIdx.x, tid = threadIdx.x, lane = tid & 63;
  const int w = __builtin_amdgcn_readfirstlane(tid >> 6);
  const int xc = bid & 7, ix = bid >> 3, sl = ix & 7, rl = ix >> 3;
  const int r = (xc >> 1) * 4 + rl, s0 = r >= 14 ? 0 : r >> 1;
  const u32x4 rx = mk_rsrc(x, S * D * 2);
  const unsigned vx0 = (unsigned)(r * 128 + (w & 1) * 64 + (lane >> 3)) * 896u + (unsigned)(((lane & 7) ^ (lane >> 4)) << 4), vx1 = (vx0 ^ 64u) + 7168u;
#define XGROUP(n) do { dma16o<(n) * 2048>(lx, vx0, rx, sg * 128 + (n) * 14336); dma16o<(n) * 2048 + 1024>(lx, vx1, rx, sg * 128 + (n) * 14336); } while (0)
  unsigned cpf;
  u64 pc; asm volatile("s_getpc_b64 %0" : "=s"(pc));
  if (w < 2) { const unsigned lx = w * 8192; const int sg = s0; XGROUP(0); XGROUP(1); XGROUP(2); XGROUP(3); }
  if (w == 4) LD1(cpf, lane < 8 ? (unsigned)(ix + lane * 32) * 128u : 0x80000000u, mk_rsrc((const void*)(pc & ~255ull), CODE_BYTES), 0u, "");
  asm volatile("" :: "s"(wqkv), "s"(w1), "s"(ws));
  const int c = (int)(((xc & 1) ? 0xEFB98765u : 0xDCA43210u) >> (4 * sl)) & 15;
  const bool kvt = sl >= 6, pure = sl == 7;
  const int rt = xc * 4 + rl, ct = sl + 2 * rl - (sl + 2 * rl >= 7 ? 7 : 0), rot2 = 7 * rl + xc - (7 * rl + xc >= 28 ? 28 : 0);
  const int l31 = lane & 31, hh = lane >> 5, q = (lane >> 2) & 3, p = lane & 3, blk = (lane >> 4) & 1;
  const unsigned slot_b = kvt ? 49152u : 40960u;
  const u32x4 rws = mk_rsrc(ws, WS_END);
  const unsigned v16 = lane * 16, v8 = lane * 8;
  u32x2 tag;
  const unsigned swz = (unsigned)(((lane & 7) ^ ((lane >> 2) & 4)) << 4);
  f32x16 z16;
#pragma unroll
  for (int e = 0; e < 16; ++e) z16[e] = 0.f;

  u32x4 xr, br; unsigned sg_;
  const unsigned fxo = (unsigned)(rt * 64 + (tid >> 3)) * 896u + ct * 128 + (tid & 7) * 16;
#define FIN_LD do { LD4(xr, fxo, rx, 0u, ""); LD4(br, (tid & 7) * 16u, mk_rsrc(b2, D * 2), ct * 128u, ""); LD1(sg_, (unsigned)(tid & ~7), rws, WS_ST + rt * 512u, "sc1"); } while (0)
#define PUT_TILES(base, t) do { _Pragma("unroll") for (int n_ = 0; n_ < 8; ++n_) *(LDS f32x4*)((base) + n_ * 1024 + (v16 ^ (n_ * 32))) = \
      (f32x4){t[n_ >> 2][4 * (n_ & 3)], t[n_ >> 2][4 * (n_ & 3) + 1], t[n_ >> 2][4 * (n_ & 3) + 2], t[n_ >> 2][4 * (n_ & 3) + 3]}; } while (0)
  if (__builtin_expect(w < 4, 1)) {
    const int h = w, hp = h >> 1, hs_ = h & 1;
    const u32x4 rw2 = mk_rsrc(w2, F * D * 2), rwo = mk_rsrc(wo, D * D * 2);
    const unsigned vwo = (unsigned)(lane >> 3) * 896u + ct * 128 + swz;
    const unsigned vw2 = vwo + (unsigned)((h >> 1) * 896 + (h & 1) * 16) * 896u;
    const int hg = (h + xc + rl) & 3;
    u32x4 R[18];
#define ROT(u) ((u) + rot2 >= 28 ? (u) + rot2 - 28 : (u) + rot2)
#define PFLD(n) LD4(R[n], vw2, rw2, ROT(19 + ((n) >> 1)) * 28672u + ((n) & 1) * 7168u, "")
    auto fill = [&](int u, int g, int cnt) {
#pragma unroll 1
      for (int e = u + cnt; u < e; ++u, ++g) {
        const unsigned k = ROT(u) * 28672u;
        dma16(g * 8192 + h * 2048, vw2, rw2, k); dma16(g * 8192 + h * 2048 + 1024, vw2, rw2, k + 7168u);
      }
    };
    {
      __builtin_amdgcn_s_setprio(3);
      const bool a1 = c < 10, b1_ = c < 9;
      const u32x4 r0 = mk_rsrc(a1 ? w1 : wqkv, a1 ? D * F * 2 : D * 3 * D * 2), r1 = mk_rsrc(b1_ ? w1 : wqkv, b1_ ? D * F * 2 : D * 3 * D * 2);
      const unsigned p0 = a1 ? 3584u : 2688u, p1 = b1_ ? 3584u : 2688u;
      unsigned V0, V1, V2, V3;
      {
        const unsigned kl = 32 * hs_ + (lane >> 3);
        const unsigned c0 = (a1 ? c : c - 10) * 384, c1 = c == 9 ? 512 : c0;
        const unsigned vk = (32 * hs_ + (lane >> 4)) * 2688 + ((224 * c - 1792) & ~63) + ((q ^ (lane >> 4)) << 6) + p * 16;
        V0 = kvt ? vk : kl * p0 + c0 + swz;
        V1 = kvt ? vk + 4 * 2688 : kl * p1 + c1 + swz + 128;
        V2 = kvt ? V0 + 896 : V1 + 128;
        V3 = V1 + 896;
      }
      int sg = s0 + hp; sg = sg == 7 ? 0 : sg;
      unsigned so = hp * slot_b;
#define GROUP(n) do { const unsigned kk = sg * 64 + 8 * (n), sa = kk * p0, sb_ = kk * p1; \
          if (i || hp) XGROUP(n); \
          dma16o<16384 + (n) * 1024>(lw, V0, r0, sa); \
          dma16o<24576 + (n) * 1024>(lw, V1, r1, sb_); \
          dma16o<32768 + (n) * 1024>(lw, V2, r1, sb_); \
          if (kvt) dma16o<40960 + (n) * 1024>(lw, V3, r1, sb_); } while (0)
      if (hp) BARRIER;
      unsigned t2;
#pragma unroll
      for (int i = 0; i < 4; ++i) {
        const int t = hp + 2 * i;
        if (i == 3 && hp) break;
        if (i >= 2 || (i == 1 && hp)) lds_wait(SY_DONE, 4, t - 2, lane);
        const unsigned lx = so + hs_ * 8192, lw = so + hs_ * 4096;
        GROUP(0); GROUP(1); GROUP(2); GROUP(3);
        if (i == 0 && !hp) BARRIER;
        if (i == 2 && hp) break;
        if (i == 3) lds_put(SY_GO, 1);
        if (i == 1) {
          LD1(t2, lane < (hp ? 0 : hs_ ? 13 : 49) ? lane * 128u : 0x80000000u, hs_ ? rwo : rw2, bid * (hs_ ? 1664u : 6272u), "");
          WAIT_VM(1);
        } else WAIT_VM(0);
        if (i == 2) asm volatile("" :: "v"(t2));
        lds_put(SY_FULL + h * 4, t + 2);
        if (!pure && ((i == 1 && hp) || i == 2)) {
#pragma unroll
          for (int n = 0; n < (i == 2 ? PFE : PFA); ++n) PFLD(n);
        }
        so = so == 0 ? 2 * slot_b : so - slot_b;
        sg = sg >= 5 ? sg - 5 : sg + 2;
      }
      tag = *(volatile LDS u32x2*)SY_TAG;
      if (hp) {
        u32x4 xf[8];
        unsigned bi;
        const bool chk = !hs_ || kvt || lane < 32;
        const u32x4 rb1 = mk_rsrc(b1, c < 10 ? F * 2 : 0);
#pragma unroll 1
        for (unsigned sp = 0; sp < SPIN_CAP; ++sp) {
#pragma unroll
          for (int k = 0; k < 7; ++k) LD4(xf[k], v16, rws, WS_CS + c * 14336u + hs_ * 1024u + k * 2048u, "sc1");
          LD4(xf[7], v16, rws, WS_ST + r * 1024u, "sc1");
          LD1(bi, lane * 4u, rb1, c * 384u + hs_ * 256u, "");
          WAIT_VM(9); lds_put(SY_FULL + h * 4, 7);
          WAIT_VM(0);
          PIN(xf[0]); PIN(xf[7]); PIN(bi);
          if (kvt && l31 == 30) bi = 0x3f80u;
          unsigned bad = (xf[0].y ^ tag.x) | (xf[0].w ^ tag.x);
          float ax_ = __uint_as_float(xf[0].x), ay_ = __uint_as_float(xf[0].z);
#pragma unroll
          for (int k = 1; k < 7; ++k) {
            PIN(xf[k]);
            ax_ += __uint_as_float(xf[k].x); ay_ += __uint_as_float(xf[k].z); PIN(ax_); PIN(ay_);
            bad |= (xf[k].y ^ tag.x) | (xf[k].w ^ tag.x); PIN(bad);
          }
          const unsigned hi = pk2(ax_, ay_); ax_ -= bflo(hi); PIN(ax_); ay_ -= bfhi(hi);
          const unsigned mi = pk2(ax_, ay_); ax_ -= bflo(mi); PIN(ax_); ay_ -= bfhi(mi);
          const unsigned lo = pk2(ax_, ay_);
          *(LDS u32x4*)(L_CS + hs_ * 1024 + v16) = (u32x4){(hi & 0xffffu) | (mi << 16), (lo & 0xffffu) | (bi << 16),
                                                           (hi >> 16) | (mi & 0xffff0000u), (lo >> 16) | (bi & 0xffff0000u)};
          if (hs_) *(LDS u32x2*)(L_ST + v8) = (u32x2){xf[7].x, xf[7].z};
          else { float r0_ = __builtin_amdgcn_rcpf(bfhi(xf[7].x)) * LOG2E; PIN(r0_); *(LDS f32x2*)(L_RS + v8) = (f32x2){r0_, __builtin_amdgcn_rcpf(bfhi(xf[7].z)) * LOG2E}; }
          if (__builtin_expect(__all((bad == 0 || !chk) && xf[7].y == tag.x && xf[7].w == tag.x), 1)) break;
          __builtin_amdgcn_s_sleep(8);
        }
        lds_put(SY_FULL + h * 4, 8);
        if (!pure) {
#pragma unroll
          for (int n = PFA; n < 18; ++n) PFLD(n);
        }
        lds_wait(SY_GO, 1, 1, lane);
        asm volatile("" :: "v"(t2));
      }
      __builtin_amdgcn_s_setprio(0);
    }
    if (!pure) {
#pragma unroll
    for (int n = PFE; n < 18; ++n) { if (h < 2) PFLD(n); }
    const unsigned wb = 49152 + (h ^ 2) * 14336;
    lds_wait(SY_DONE, 4, h < 2 ? 6 : 5, lane);
#pragma unroll 1
    for (int n = 0; n < 14; ++n) dma16(wb + n * 1024, vwo, rwo, hg * 100352u + n * 7168u);
    lds_wait(SY_DONE, 4, 6, lane);
    fill(0, 16, 3);
    WAIT_VM(6);
    bf16x8 WO[4][2][2];
    {
      const unsigned a0 = wb + (4 * hh + q) * 128 + (((q >> 1) & 1) << 6) + blk * 32 + p * 8, a1_ = a0 ^ 64u;
#pragma unroll
      for (int n = 0; n < 4; ++n)
#pragma unroll
        for (int sg = 0; sg < 2; ++sg) {
          const unsigned o = n * 3584 + sg * 2048;
          WO[n][sg][0] = tr_frag(a0 + o, a0 + o + 1024); WO[n][sg][1] = tr_frag(a1_ + o, a1_ + o + 1024);
        }
    }
#pragma unroll
    for (int n = 0; n < 8; ++n) {
      u32x4 t = __builtin_bit_cast(u32x4, WO[n >> 1][1][n & 1]);
      if (hh) { t.z = 0; t.w = 0; }
      WO[n >> 1][1][n & 1] = asbf(t);
    }
#pragma unroll
    for (int n = 0; n < 16; ++n) PIN(WO[n >> 2][(n >> 1) & 1][n & 1]);
    lds_put(SY_WOK + h * 4, 1);
    lds_wait(SY_DONE, 4, c < 10 ? 8 : 7, lane);
    fill(3, 0, 6);
    lds_wait(SY_WOK, 4, 1, lane);
    fill(9, 6, 3);
    WAIT_VM(6); lds_put(SY_FULL2 + h * 4, 1);
    fill(12, 9, 7);
    lds_wait(SY_DONE, 4, 9, lane);
#pragma unroll
    for (int n = 0; n < 18; ++n) { PIN(R[n]); *(LDS u32x4*)((n < 6 ? 131072 : -24576) + h * 2048 + v16 + (n >> 1) * 8192 + (n & 1) * 1024) = R[n]; }
    WAIT_VM(0); lds_put(SY_RDY + h * 4, 1);
    u32x4 PQ[2][7], KV[8];
    __builtin_amdgcn_s_setprio(1);
    lds_wait(SY_HOK, 1, 1, lane);
    if (*(volatile LDS unsigned*)SY_HOK < 2u) tag_wait(min(lane, 4) * FS, rws, WS_FL + (rt * 32 + 19) * FS, ~0ull, tag);
#pragma unroll
    for (int n = 0; n < 14; ++n) LD4(PQ[n / 7][n % 7], v16, rws, WS_PQ + ((rt * 2 + n / 7) * 28 + hg * 7 + n % 7) * 1024u, "sc1");
    FIN_LD;
    __builtin_amdgcn_s_setprio(3);
    tag_wait(min(lane, 7) * FS, rws, WS_KF + (xc * 32 + hg * 8) * FS, ~0ull, tag);
#pragma unroll
    for (int n = 0; n < 8; ++n) LD4(KV[n], v16, rws, WS_KV + hg * 8192u + n * 1024u, "sc1");
    __builtin_amdgcn_s_setprio(0);
    u32x4 PB[2][4];
#pragma unroll
    for (int n = 0; n < 14; ++n) PIN(PQ[n / 7][n % 7]);
#pragma unroll
    for (int u = 0; u < 8; ++u) {
      const u32x4* P = PQ[u >> 2]; const int n = u & 3, lo = n < 3 ? 2 * n + 1 : 5, hi = n ? 2 * n - 1 : 1;
      PB[u >> 2][n] = (u32x4){P[lo].x, P[lo].y, P[hi].z, P[hi].w}; PIN(PB[u >> 2][n]);
    }
    WAIT_VM(4);
#pragma unroll
    for (int n = 0; n < 4; ++n) PIN(KV[n]);
    f32x16 nm[3], as[2][2] = {{z16, z16}, {z16, z16}};
    u32x4 O[2][2]; float rd[2];
#define NUM0(u) nm[(u) % 3] = mfma(asbf(KV[2 * ((u) & 3)]), asbf(PQ[(u) >> 2][2 * ((u) & 3)]), z16)
#define NUM1(u) nm[(u) % 3] = mfma(asbf(KV[2 * ((u) & 3) + 1]), asbf(PB[(u) >> 2][(u) & 3]), nm[(u) % 3])
#define DEN(u) __uint_as_float(__builtin_amdgcn_permlane32_swap(__float_as_uint(nm[(u) % 3][12]), __float_as_uint(nm[(u) % 3][12]), false, false)[1])
#define APP(u, i) as[(u) >> 2][(i) & 1] = mfma(WO[(u) & 3][(i) >> 1][(i) & 1], asbf(O[(u) & 1][(i) >> 1]), as[(u) >> 2][(i) & 1])
#define MULS(a, b) _Pragma("unroll") for (int e = (a); e < (b); ++e) o[e] = mul1(nm[u % 3][e], rd[u & 1])
#define CVTS(a, b) _Pragma("unroll") for (int e = (a); e < (b); ++e) O[u & 1][e >> 2][e & 3] = pk2(o[2 * e], o[2 * e + 1])
    NUM0(0); NUM1(0); NUM0(1); NUM1(1);
    rd[0] = __builtin_amdgcn_rcpf(DEN(0) + 1e-6f);
    asm volatile("s_waitcnt vmcnt(0)" : "+v"(rd[0]), "+v"(KV[4]), "+v"(KV[5]), "+v"(KV[6]), "+v"(KV[7]) :: "memory");
    SB;
    unsigned d2 = 0; bool put0 = false;
#pragma unroll
    for (int u = 0; u < 8; ++u) {
      float o[16], dn;
      if (u) APP(u - 1, 0); SB; MULS(0, 6); SB;
      if (u) APP(u - 1, 1); SB; MULS(6, 10); CVTS(0, 2); SB;
      if (u) APP(u - 1, 2); SB; MULS(10, 13); CVTS(2, 4); SB;
      if (u) APP(u - 1, 3); SB; MULS(13, 16); CVTS(4, 6); SB;
      if (u < 6) NUM0(u + 2); SB; if (u < 7) dn = DEN(u + 1) + 1e-6f; SB;
      if (u < 6) NUM1(u + 2); SB; if (u < 7) rd[(u + 1) & 1] = __builtin_amdgcn_rcpf(dn); SB; CVTS(6, 8);
      if (u == 4) d2 = *(volatile LDS unsigned*)(SY_DONE2 + p * 4);
      if (u == 5) { put0 = __all(d2 >= 1u); if (put0) PUT_TILES(49152 + h * 16384, as[0]); }
      SB;
    }
    APP(7, 0); APP(7, 2); APP(7, 1); APP(7, 3);
    if (!put0) { lds_wait(SY_DONE2, 4, 1, lane); PUT_TILES(49152 + h * 16384, as[0]); }
    PUT_TILES(57344 + h * 16384, as[1]);
    }
  } else {
  const int cw = w - 4, wl = cw & 1, wh = cw >> 1;
  u32x4 xs[7];
  if (cw == 3) {
    const unsigned va = (unsigned)(r * 128 + c * 8 + (lane >> 3)) * 896u + (lane & 7) * 16;
#pragma unroll
    for (int k = 0; k < 7; ++k) LD4(xs[k], va, rx, k * 128u, "");
  }
  *(LDS unsigned*)(L_SY + lane * 4) = (lane >> 1) == 1; BARRIER;
  __builtin_amdgcn_s_setprio(2);
  bf16x8 ones = asbf((u32x4){0x3f803f80u, 0x3f803f80u, 0x3f803f80u, 0x3f803f80u});
  PIN(ones);
  {
    {
      u64 ep;
      asm volatile("s_load_dwordx2 %0, %1, 0x0\n\ts_waitcnt lgkmcnt(0) vmcnt(0)" : "=&s"(ep) : "s"(ws) : "memory");
      tag.x = (unsigned)ep + 1u; tag.y = (unsigned)(ep >> 32) + 1u;
    }
    if (cw == 0) { asm volatile("" :: "v"(cpf)); *(volatile LDS u32x2*)SY_TAG = tag; }
    if (cw == 3) {
      float sq[2] = {0.f, 0.f}, sm[2] = {0.f, 0.f};
      const bf16x2 one2 = __builtin_bit_cast(bf16x2, 0x3f803f80u);
#pragma unroll
      for (int k = 0; k < 7; ++k) {
        PIN(xs[k]);
#pragma unroll
        for (int e = 0; e < 4; ++e) {
          const unsigned u = xs[k][e];
          const bf16x2 v = __builtin_bit_cast(bf16x2, u);
          sq[e & 1] = __builtin_amdgcn_fdot2_f32_bf16(v, v, sq[e & 1], false); sm[e & 1] = __builtin_amdgcn_fdot2_f32_bf16(v, one2, sm[e & 1], false);
        }
      }
      const float m = sum8(sm[0] + sm[1]) * (1.f / D);
      const float var = fmaxf(sum8(sq[0] + sq[1]) * (1.f / D) - m * m, 0.f);
      const u32x2 gr = {pk2(-m, __builtin_amdgcn_sqrtf(rbf(rbf(var) + 1e-5f))), tag.x};
      if ((lane & 7) == 0) ST2(gr, (unsigned)lane, rws, WS_ST + (r * 128 + c * 8) * 8, "sc1");
    }
  }
  const unsigned fl_off = WS_FL + ((r * 2 + wl) * 32 + 2 * c + wh) * FS;
  const unsigned fa = SY_FULL + p * 4;
#define CHECK_FULL(v, need) do { if (__builtin_expect(__any((v) < (need)), 0)) lds_wait(SY_FULL, 4, need, lane); FENCE; } while (0)
#define COLSUM(a_, t_, tile) do { const unsigned a = (a_); bf16x8 f[4]; \
    _Pragma("unroll") for (int ks = 0; ks < 4; ++ks) f[ks] = tr_frag(a + ks * 2048, a + ks * 2048 + (t_)); \
    SB; f32x16 cs = z16; \
    _Pragma("unroll") for (int ks = 0; ks < 4; ++ks) cs = mfma(ones, f[ks], cs); \
    const u32x2 gr = {__float_as_uint(cs[0]), tag.x}; \
    if (lane < 32) ST2(gr, v8, rws, WS_CS + (c * 7 + s0) * 2048 + (tile) * 256, "sc1"); } while (0)
  unsigned da = SY_DONE + cw * 4;
  PIN(da);
  const unsigned xm = hh ? 0u : ~0u;
  if (!kvt) {
    const unsigned xa = (wl * 64 + l31) * 128 + ((hh ^ ((l31 >> 1) & 7)) << 4);
    const unsigned wr = 16384 + (8 * hh + q) * 128 + blk * 32 + p * 8;
    const unsigned wa0 = wr + wh * 8192 + ((wh ^ (q >> 1)) << 6), wa1 = wr + wh * 16384 + ((wh ^ 1 ^ (q >> 1)) << 6);
    f32x16 acc[2][3] = {{z16, z16, z16}, {z16, z16, z16}};
    bf16x8 X[4][2], W[4][3];
#define RDA(n, ks) do { \
      if ((n) == 0 || (n) == 2) X[ks][(n) >> 1] = ld_frag(ax[ks] + ((n) >> 1) * 4096); \
      else { const unsigned a_ = ((n) == 3 ? a1_ : a0_) + ((n) == 4 ? 8192 : 0) + (ks) * 2048; W[ks][(n) == 1 ? 0 : (n) - 2] = tr_frag(a_, a_ + 512); } } while (0)
#define MMA(ks, m) acc[(m) & 1][(m) >> 1] = mfma(W[ks][(m) >> 1], X[ks][(m) & 1], acc[(m) & 1][(m) >> 1])
    unsigned ax[4] = {xa, xa ^ 32u, xa ^ 64u, xa ^ 96u}, a0_ = wa0, a1_ = wa1;
#pragma unroll
    for (int n = 0; n < 6; ++n) PIN(acc[n & 1][n >> 1]);
    lds_wait(SY_FULL, 4, 1, lane);
    if (r < 14 && !((r & 1) && wl)) {
      const int b = (r & 1) ? 2 : wl;
      COLSUM(b == 1 ? wa1 : wa0 + b * 4096, 512, 3 * wh + b);
    }
#pragma unroll
    for (int n = 0; n < 10; ++n) { RDA(n % 5, n / 5); SB; }
    unsigned tv = 1; int ts = 0;
    PIN(tv);
#pragma unroll 1
    for (;;) {
      unsigned fv; int dl;
#pragma unroll
      for (int m = 0; m < 12; ++m) {
        MMA(m / 6, m % 6); SB;
        if (m % 6 < 5) RDA(m % 6, 2 + m / 6);
        if (m == 0) { dl = ((0x24 >> ts) & 1) ? -81920 : 40960; ++ts; asm volatile("" : "+s"(dl), "+s"(ts)); }
        if (m == 2) { ax[0] += dl; ax[1] += dl; PIN(ax[0]); PIN(ax[1]); }
        if (m == 5) fv = *(volatile LDS unsigned*)fa;
        if (m == 10) *(volatile LDS unsigned*)da = tv;
        if (m == 11) { a0_ += dl; a1_ += dl; ++tv; CHECK_FULL(fv, tv); }
        SB;
      }
      if (__builtin_expect(ts == 7, 0)) break;
#pragma unroll
      for (int m = 0; m < 12; ++m) {
        MMA(2 + m / 6, m % 6); SB;
        if (m % 6 < 5) RDA(m % 6, m / 6);
        if (m == 5) { ax[2] += dl; ax[3] += dl; }
        SB;
      }
    }
    unsigned st[2]; u32x2 cv[3];
#pragma unroll
    for (int a = 0; a < 2; ++a) st[a] = *(LDS const unsigned*)(L_ST + (wl * 64 + a * 32 + l31) * 4);
#pragma unroll
    for (int b = 0; b < 3; ++b) cv[b] = *(LDS const u32x2*)(L_CS + ((3 * wh + b) * 32 + l31) * 8);
    SB;
    MMA(2, 0); MMA(2, 1); MMA(3, 0); MMA(3, 1);
    SB;
    bf16x8 xe[2];
#pragma unroll
    for (int a = 0; a < 2; ++a) xe[a] = asbf((u32x4){((st[a] & 0xffffu) | (st[a] << 16)) & xm, st[a] & xm, 0u, 0u});
#define MME(m) acc[(m) & 1][(m) >> 1] = mfma(asbf((u32x4){cv[(m) >> 1].x, cv[(m) >> 1].y, cv[(m) >> 1].x, cv[(m) >> 1].y}), xe[(m) & 1], acc[(m) & 1][(m) >> 1])
    MME(0); MME(1);
    SB;
    const int R0 = r * 4 + wl * 2;
    const unsigned sk = c == 9 ? 0x40000000u : 0u;
#define MM6(b, j) do { if ((j) < 4) MMA(2 + ((j) >> 1), 2 * (b) + ((j) & 1)); else MME(2 * (b) + ((j) & 1)); } while (0)
    if (2 * c + wh < 19) {
      const unsigned ob = WS_H + (R0 * 112 + 12 * c + 6 * wh) * 1024u;
#pragma unroll
      for (int b = 0; b < 3; ++b)
#pragma unroll
        for (int n = 0; n < 4; ++n) {
          const int a = n >> 1, e = 8 * (n & 1);
          float t[8]; u32x4 o;
          if (b < 2) MM6(b + 1, n < 2 ? 2 * n : n + 2);
          SB;
          if (n == 0) asm volatile("s_nop 10" : "+v"(acc[0][b]), "+v"(acc[1][b]));
          asm volatile("v_mul_f32_e64 %0, %8, |%8|\n\tv_mul_f32_e64 %1, %9, |%9|\n\tv_mul_f32_e64 %2, %10, |%10|\n\tv_mul_f32_e64 %3, %11, |%11|\n\t"
                       "v_mul_f32_e64 %4, %12, |%12|\n\tv_mul_f32_e64 %5, %13, |%13|\n\tv_mul_f32_e64 %6, %14, |%14|\n\tv_mul_f32_e64 %7, %15, |%15|"
                       : "=&v"(t[0]), "=&v"(t[1]), "=&v"(t[2]), "=&v"(t[3]), "=&v"(t[4]), "=&v"(t[5]), "=&v"(t[6]), "=&v"(t[7])
                       : "v"(acc[a][b][e]), "v"(acc[a][b][e + 1]), "v"(acc[a][b][e + 2]), "v"(acc[a][b][e + 3]), "v"(acc[a][b][e + 4]), "v"(acc[a][b][e + 5]),
                         "v"(acc[a][b][e + 6]), "v"(acc[a][b][e + 7]));
          SB;
          if (b < 2 && n < 2) MM6(b + 1, 2 * n + 1);
          SB;
#pragma unroll
          for (int j = 0; j < 4; ++j) o[j] = pk2(t[2 * j], t[2 * j + 1]);
#pragma unroll
          for (int j = 0; j < 4; ++j) o[j] = relu2(o[j]);
          ST4(o, v16, rws, ob + a * (112 * 1024u) + (2 * b + (n & 1)) * 1024u + (b == 2 ? sk : 0u), "sc1");
          SB;
        }
    } else {
#pragma unroll
      for (int j = 0; j < 12; ++j) MM6(1 + j / 6, j % 6);
      const unsigned ob = WS_PQ + (R0 * 28 + (c == 9 ? 22 : (c - 10) * 12 + wh * 6)) * 1024u;
      SB;
#pragma unroll
      for (int a = 0; a < 2; ++a) {
        const float rl = __builtin_amdgcn_rcpf(bfhi(st[a])) * LOG2E;
#pragma unroll
        for (int n = 0; n < 6; ++n) {
          f32x2 u[4], pe[4]; u32x4 o;
#pragma unroll
          for (int j = 0; j < 4; ++j) u[j] = phi_u(acc[a][n >> 1][8 * (n & 1) + 2 * j], acc[a][n >> 1][8 * (n & 1) + 2 * j + 1], (f32x2){rl, rl});
          SB; PHI_E(0); PHI_E(2); PHI_B(o);
          ST4(o, v16, rws, ob + a * (28 * 1024u) + n * 1024u + (n < 2 ? sk : 0u), "sc1");
          SB;
        }
      }
    }
  } else {
    const unsigned xa = l31 * 128 + ((hh ^ ((l31 >> 1) & 7)) << 4);
    const int ok_ = (c & 1) * 32 + 56 * cw + 8 * ((4 * blk + p + cw) & 7), ov_ = (c & 1) * 32 + 56 * cw + 32 * blk + 8 * p;
    const unsigned ka = 16384 + hh * 1024 + q * 256 + ((((ok_ >> 6) & 3) ^ q) << 6) + (ok_ & 63);
    const unsigned va = 32768 + hh * 1024 + q * 256 + ((((ov_ >> 6) & 3) ^ q) << 6) + (ov_ & 63) + ((lane & 19) == 19 ? 0x100000 : 0);
    f32x16 acc[4][2] = {{z16, z16}, {z16, z16}, {z16, z16}, {z16, z16}};
    bf16x8 X[4][4], W[4][2];
#define RDK(n, ks) do { \
      if ((n) == 1 || (n) == 5) { const unsigned a_ = ((n) == 1 ? a0_ : a1_) + (ks) * 2048; W[ks][(n) >> 2] = tr_frag(a_, a_ + 8192); } \
      else X[ks][(n) ? (n) - 1 : 0] = ld_frag(ax[ks] + ((n) ? (n) - 1 : 0) * 4096); } while (0)
#define MMK(ks, m) acc[(m) & 3][(m) >> 2] = mfma(X[ks][(m) & 3], W[ks][(m) >> 2], acc[(m) & 3][(m) >> 2])
    unsigned ax[4] = {xa, xa ^ 32u, xa ^ 64u, xa ^ 96u}, a0_ = ka, a1_ = va;
#pragma unroll
    for (int n = 0; n < 8; ++n) PIN(acc[n & 3][n >> 2]);
    lds_wait(SY_FULL, 4, 1, lane);
    if (r < 14) COLSUM((r & 1) ? va : ka, 8192, 2 * cw + (r & 1));
#pragma unroll
    for (int n = 0; n < 12; ++n) { RDK(n % 6, n / 6); SB; }
    unsigned tv = 1; int ts = 0;
    PIN(tv);
#pragma unroll 1
    for (;;) {
      unsigned fv; int dl;
#pragma unroll
      for (int m = 0; m < 16; ++m) {
        MMK(m >> 3, m & 7); SB;
        if ((m & 7) >= 1 && (m & 7) < 7) RDK((m & 7) - 1, 2 + (m >> 3));
        if (m == 0) { dl = ((0x24 >> ts) & 1) ? -98304 : 49152; ++ts; asm volatile("" : "+s"(dl), "+s"(ts)); }
        if (m == 7) { ax[0] += dl; ax[1] += dl; PIN(ax[0]); PIN(ax[1]); }
        if (m == 8) fv = *(volatile LDS unsigned*)fa;
        if (m == 14) *(volatile LDS unsigned*)da = tv;
        if (m == 15) { a0_ += dl; a1_ += dl; ++tv; CHECK_FULL(fv, tv); }
        SB;
      }
      if (__builtin_expect(ts == 7, 0)) break;
#pragma unroll
      for (int m = 0; m < 16; ++m) {
        MMK(2 + (m >> 3), m & 7); SB;
        if ((m & 7) >= 1 && (m & 7) < 7) RDK((m & 7) - 1, m >> 3);
        if (m == 7) { ax[2] += dl; ax[3] += dl; }
        SB;
      }
    }
    unsigned st[4]; u32x2 cv[2]; f32x4 rsv[2][4];
#pragma unroll
    for (int a = 0; a < 4; ++a) st[a] = *(LDS const unsigned*)(L_ST + (a * 32 + l31) * 4);
#pragma unroll
    for (int b = 0; b < 2; ++b) cv[b] = *(LDS const u32x2*)(L_CS + ((2 * cw + b) * 32 + l31) * 8);
    unsigned rsa = L_RS + hh * 16;
    PIN(rsa);
#define MK6(a, j) do { if ((j) < 4) MMK(2 + ((j) >> 1), (a) + 4 * ((j) & 1)); else acc[a][(j) & 1] = mfma(asbf((u32x4){((st[a] & 0xffffu) | (st[a] << 16)) & xm, st[a] & xm, 0u, 0u}), \
        asbf((u32x4){cv[(j) & 1].x, cv[(j) & 1].y, cv[(j) & 1].x, cv[(j) & 1].y}), acc[a][(j) & 1]); } while (0)
#pragma unroll
    for (int n = 0; n < 4; ++n) rsv[0][n] = *(LDS const f32x4*)(rsa + n * 32);
    SB;
#pragma unroll
    for (int j = 0; j < 6; ++j) MK6(0, j);
    SB;
    f32x16 kv = z16;
    u32x4 fk, fv;
#pragma unroll
    for (int gi = 0; gi < 8; ++gi) {
      const int a = gi >> 1, sg = gi & 1, e = 8 * sg;
      if (a < 3 && sg == 0) {
#pragma unroll
        for (int n = 0; n < 4; ++n) rsv[(a + 1) & 1][n] = *(LDS const f32x4*)(rsa + (a + 1) * 128 + n * 32);
      }
      const f32x4 ra_ = rsv[a & 1][2 * sg], rb_ = rsv[a & 1][2 * sg + 1];
      const f32x2 rl[4] = {{ra_[0], ra_[1]}, {ra_[2], ra_[3]}, {rb_[0], rb_[1]}, {rb_[2], rb_[3]}};
      f32x2 u[4], vv[4], pe[4];
#pragma unroll
      for (int j = 0; j < 4; ++j) u[j] = phi_u(acc[a][0][e + 2 * j], acc[a][0][e + 2 * j + 1], rl[j]);
#pragma unroll
      for (int j = 0; j < 4; ++j) vv[j] = (f32x2){acc[a][1][e + 2 * j], acc[a][1][e + 2 * j + 1]} * rl[j];
      SB;
      if (gi) kv = mfma(asbf(fk), asbf(fv), kv);
      SB;
      PHI_E(0);
      if (a < 3) MK6(a + 1, 3 * sg);
      SB;
      PHI_E(2);
      if (a < 3) MK6(a + 1, 3 * sg + 1);
      SB;
      fv = (u32x4){pk2(vv[0].x, vv[0].y), pk2(vv[1].x, vv[1].y), pk2(vv[2].x, vv[2].y), pk2(vv[3].x, vv[3].y)};
      SB;
      if (a < 3) MK6(a + 1, 3 * sg + 2);
      SB;
      PHI_B(fk);
      SB;
    }
    kv = mfma(asbf(fk), asbf(fv), kv);
    kv = kv * (l31 == 28 ? 1.f / 1.4453125f : 1.f / LOG2E);
    const unsigned ko = WS_KVP + (((c - 12) * 4 + cw) * 16 + r) * 4096u;
#pragma unroll
    for (int e = 0; e < 4; ++e) {
      const u32x4 o = {pk2(kv[4 * e], kv[4 * e + 1]), tag.x, pk2(kv[4 * e + 2], kv[4 * e + 3]), tag.x};
      ST4(o, v16, rws, ko + e * 1024u, "sc1");
    }
  }
  lds_put(da, 8);
  bf16x8 WH[9][4];
  unsigned ra[3][2];
#pragma unroll
  for (int cb = 0; cb < 2; ++cb) {
    ra[0][cb] = wh * 4096 + (4 * hh + q) * 128 + ((cb ^ (q >> 1)) << 6) + blk * 32 + p * 8; ra[1][cb] = ra[0][cb] + 65536; ra[2][cb] = ra[0][cb] + 131072;
    PIN(ra[0][cb]); PIN(ra[1][cb]); PIN(ra[2][cb]);
  }
#define RDW(dst, n, reg) do { const unsigned a_ = ra[(reg) >> 3][(n) & 1] + ((reg) & 7) * 8192 + ((n) >> 1) * 2048; dst[n] = tr_frag(a_, a_ + 1024); } while (0)
  if (!kvt) WAIT_VM(0);
  if (lane == 0) ST2(tag, 0u + lane, rws, fl_off, "sc1");
  if (!pure) {
    lds_wait(SY_FULL2, 4, 1, lane);
#pragma unroll
    for (int n = 0; n < 36; ++n) RDW(WH[n >> 2], n & 3, (16 + (n >> 2)) % 19);
    lds_put(da, 9);
  }

  if (!pure) {
    const int kh = wh, R = rt * 2 + wl;
    if (cw == 0) {
      const u64 bl = tag_wait(min(lane, 23) * FS, rws, WS_FL + rt * 32 * FS, 0x7FFFFull, tag);
      lds_put(SY_HOK, (bl & 0xF80000ull) == 0xF80000ull ? 2 : 1);
    } else lds_wait(SY_HOK, 1, 1, lane);
    WAIT_VM(0);
    const unsigned hs = WS_H + (R * 112 + kh * 56) * 1024u;
    constexpr int NH = NHW, E = 28 - NH;
    u32x4 hf[NH][2];
#define HLD(u) do { const unsigned o_ = hs + ROT(u) * 2048u; \
      LD4(hf[(u) % NH][0], v16, rws, o_, "sc1"); LD4(hf[(u) % NH][1], v16, rws, o_, "offset:1024 sc1"); } while (0)
#pragma unroll
    for (int u = 0; u < NH; ++u) HLD(u);
    f32x16 af[2] = {z16, z16};
    bf16x8 W[LA + 1][4];
#pragma unroll
    for (int u = 0; u < 28; ++u) {
      constexpr auto total = [](int u_) { return 2 * NH + 2 * (u_ < E ? u_ : E) + (u_ > E ? 3 : 0); };
      asm volatile("s_waitcnt vmcnt(%2)" : "+v"(hf[u % NH][0]), "+v"(hf[u % NH][1]) : "n"(total(u) - 2 - 2 * u) : "memory");
      if (u == 9 - LA) { lds_wait(SY_RDY, 4, 1, lane); }
#pragma unroll
      for (int n = 0; n < 4; ++n) {
        af[n & 1] = mfma(u < 9 ? WH[u < 9 ? u : 0][n] : W[u % (LA + 1)][n], asbf(hf[u % NH][n >> 1]), af[n & 1]); SB;
        if (u + LA >= 9 && u + LA < 28) RDW(W[(u + LA) % (LA + 1)], n, (u + LA - 3) % 19);
        SB;
      }
      if (u == 16 - LA) lds_put(SY_DONE2 + cw * 4, 1);
      if (u == 20 - LA) lds_put(SY_DONE2 + cw * 4, 2);
      if (u < E) HLD(u + NH);
      else if (u == E) FIN_LD;
    }
    __builtin_amdgcn_s_setprio(0);
    lds_wait(SY_DONE2, 4, 2, lane);
    PUT_TILES(114688 + cw * 8192, af);
    WAIT_VM(0);
  }
  }
  if (pure) {
    const int id = xc * 4 + (ix >> 3), hd = id >> 1;
    lds_wait(SY_DONE, 4, 8, lane);
    f32x4 a, b;
#pragma unroll 1
    for (unsigned sp = 0; sp < SPIN_CAP; ++sp) {
      u32x4 pr[4];
#pragma unroll
      for (int n = 0; n < 4; ++n) LD4(pr[n], v16, rws, WS_KVP + (hd * 16 + 2 * w + (n >> 1)) * 4096u + (id & 1) * 2048u + (n & 1) * 1024u, "sc1");
      WAIT_VM(0);
      unsigned bad = 0;
#pragma unroll
      for (int n = 0; n < 4; ++n) { PIN(pr[n]); bad |= (pr[n].y ^ tag.x) | (pr[n].w ^ tag.x); }
#define F4(n) (f32x4){bflo(pr[n].x), bfhi(pr[n].x), bflo(pr[n].z), bfhi(pr[n].z)}
      a = F4(0) + F4(2); b = F4(1) + F4(3);
      if (__all(bad == 0)) break;
      __builtin_amdgcn_s_sleep(4);
    }
    *(LDS f32x4*)(w * 2048 + v16) = a; *(LDS f32x4*)(w * 2048 + 1024 + v16) = b;
    BARRIER;
    if (w == 4) {
      a = *(LDS const f32x4*)v16; b = *(LDS const f32x4*)(1024 + v16);
#pragma unroll
      for (int n = 1; n < 8; ++n) { a += *(LDS const f32x4*)(n * 2048 + v16); b += *(LDS const f32x4*)(n * 2048 + 1024 + v16); }
      u32x4 o = {pk2(a[0], a[1]), pk2(a[2], a[3]), pk2(b[0], b[1]), pk2(b[2], b[3])};
      if ((id & 1) && hh != (hd & 1)) { if (hd & 2) { o.x = 0; o.y = 0; } else { o.z = 0; o.w = 0; } }
      ST4(o, v16, rws, WS_KV + id * 1024u, "sc1");
      WAIT_VM(0);
      if (lane < 8) ST2(tag, lane * 32 * FS, rws, WS_KF + id * FS, "sc1");
    } else if (w == 5 && bid == 255) {
#pragma unroll 1
      for (int n = 0; n < 4; ++n) tag_wait((unsigned)((lane >> 4) * 64 + (lane & 15) * 2) * FS, rws, WS_FL + n * 256 * FS, ~0ull, tag);
      if (lane == 0) ST2(tag, 0u + lane, rws, WS_EP, "sc1");
    }
    return;
  }
  BARRIER;
  {
    const unsigned a0 = 49152 + (tid >> 8) * 8192 + (tid & 7) * 1024 + ((((tid >> 3) & 31) ^ ((tid & 7) * 2)) << 4);
    f32x4 f[2][2], sp[4][2];
#pragma unroll
    for (int n = 0; n < 2; ++n) { f[n][0] = *(LDS const f32x4*)(a0 + 65536 + n * 16384); f[n][1] = *(LDS const f32x4*)(a0 + 65536 + n * 16384 + 512); }
#pragma unroll
    for (int n = 0; n < 4; ++n) { sp[n][0] = *(LDS const f32x4*)(a0 + n * 16384); sp[n][1] = *(LDS const f32x4*)(a0 + n * 16384 + 512); }
    SB;
    PIN(xr); PIN(br); PIN(sg_);
    const float rs = __builtin_amdgcn_rcpf(bfhi(sg_)), rs2 = rs * rs;
    u32x4 ov;
#pragma unroll
    for (int e = 0; e < 4; ++e) {
      const int hf_ = e >> 1, j = (e & 1) * 2;
      const float t0 = fmaf(f[0][hf_][j] + f[1][hf_][j], rs2, bflo(br[e])), t1 = fmaf(f[0][hf_][j + 1] + f[1][hf_][j + 1], rs2, bfhi(br[e]));
      const float g0 = t0 * __builtin_amdgcn_rcpf(1.f + __expf(-t0)), g1 = t1 * __builtin_amdgcn_rcpf(1.f + __expf(-t1));
      const float s0_ = (sp[0][hf_][j] + sp[1][hf_][j]) + (sp[2][hf_][j] + sp[3][hf_][j]);
      const float s1_ = (sp[0][hf_][j + 1] + sp[1][hf_][j + 1]) + (sp[2][hf_][j + 1] + sp[3][hf_][j + 1]);
      ov[e] = pk2(fmaf(s0_, g0, bflo(xr[e])), fmaf(s1_, g1, bfhi(xr[e])));
    }
    ST4(ov, fxo, mk_rsrc(out, S * D * 2), 0u, "sc0 sc1");
  }
}

extern "C" void kernel_launch(void* const* d_in, const int* in_sizes, int n_in, void* d_out, int out_size, void* d_ws,
                              size_t ws_size, hipStream_t stream) {
  (void)hipFuncSetAttribute((const void*)fused, hipFuncAttributeMaxDynamicSharedMemorySize, L_END);
  fused<<<256, 512, L_END, stream>>>((const u16*)d_in[0], (const u16*)d_in[1], (const u16*)d_in[4], (char*)d_ws, (const u16*)d_in[2],
                                         (const u16*)d_in[5], (const u16*)d_in[6], (const u16*)d_in[7], (u16*)d_out);
}
```

```cpp
#include <hip/hip_runtime.h>
#include <hip/hip_bf16.h>
#include <stdint.h>
#pragma clang diagnostic ignored "-Wint-to-pointer-cast"

#define HMOD "sc1"
#define PRIO_H 3
#define PRIO_C 2
#define LSLEEP 1
#define STW 99
#define PF0 2
#define PFN 0
#define FILLAT 8
#define THR_N 0
#define THR_V 4
#define NHW 7
#define TSLEEP 4
#define TOUCH 0
#define S0MODE 1
#define FS 128u
#define POLL_LATE 0
#define TCH2 5
#define PRIO_S 1
#define RSLEEP 4
#define NX 9
#define PFA 9
#define PRIO_P 0
#define PRIO_W 0
#define EARLY 0
#define CTROT 1
#define ROTM 1
#define LA 2
#define NOKV 0
#define PRIO_M 0
#define PRIO_K 3
#define PFE 4
#define PD 5
#define PMASK 10
#define PD2 10
#define PMASK2 12
#define LNOP 0
typedef unsigned short u16;
typedef unsigned long long u64;
typedef __bf16 bf16x8 __attribute__((ext_vector_type(8)));
typedef __bf16 bf16x2 __attribute__((ext_vector_type(2)));
typedef short s16x4 __attribute__((ext_vector_type(4)));
typedef float f32x4 __attribute__((ext_vector_type(4)));
typedef float f32x16 __attribute__((ext_vector_type(16)));
typedef float f32x2 __attribute__((ext_vector_type(2)));
typedef unsigned u32x4 __attribute__((ext_vector_type(4)));
typedef unsigned u32x2 __attribute__((ext_vector_type(2)));
#define LDS __attribute__((address_space(3)))
#define DI __device__ __forceinline__

constexpr int S = 2048, D = 448, F = 1792;
constexpr unsigned WS_EP = 0;
constexpr unsigned WS_KF = 1024;
constexpr unsigned WS_FL = WS_KF + 256 * FS;
constexpr unsigned WS_ST = WS_FL + 1024 * FS;
constexpr unsigned WS_CS = WS_ST + 32768;
constexpr unsigned WS_KV = WS_CS + 16 * 7 * 2048;
constexpr unsigned WS_KVP = WS_KV + 32768;
constexpr unsigned WS_PQ = WS_KVP + 16 * 16 * 4096;
constexpr unsigned WS_H = WS_PQ + 64 * 28 * 1024;
constexpr unsigned WS_END = WS_H + 64 * 112 * 1024;
constexpr unsigned L_CS = 155648, L_ST = L_CS + 2048, L_RS = L_ST + 512, L_SY = L_RS + 512, L_END = L_SY + 256;
constexpr unsigned SY_FULL = L_SY, SY_AUX = L_SY + 16, SY_DONE = L_SY + 32, SY_RDY = L_SY + 48, SY_FULL2 = L_SY + 64, SY_DONE2 = L_SY + 80,
                   SY_GO = L_SY + 112, SY_WOK = L_SY + 128, SY_KOK = L_SY + 96, SY_HOK = L_SY + 100, SY_TAG = L_SY + 104;
constexpr unsigned CODE_BYTES = 33024;
constexpr unsigned SPIN_CAP = 1u << 17;

#define LDS_BYTES L_END
#define TSW(n, wv)
#define TSC(n, wv)
#define TS_TOP
#define TS_INIT
#define TS_DUMP

DI u32x4 mk_rsrc(const void* p, unsigned bytes) {
  uint64_t a = (uint64_t)p;
  u32x4 r; r.x = (unsigned)a; r.y = (unsigned)(a >> 32) & 0xffffu; r.z = bytes; r.w = 0x00020000u; return r;
}
DI void dma16(unsigned lds_addr, unsigned voff, u32x4 rsrc, unsigned soff) {
  asm volatile("s_mov_b32 m0, %0\n\ts_nop 0\n\tbuffer_load_dwordx4 %1, %2, %3 offen lds"
               :: "s"(lds_addr), "v"(voff), "s"(rsrc), "s"(soff) : "memory");
}
template <unsigned OFF> DI void dma16o(unsigned lds_base, unsigned voff, u32x4 rsrc, unsigned soff) {
  asm volatile("s_add_u32 m0, %0, %4\n\ts_nop 0\n\tbuffer_load_dwordx4 %1, %2, %3 offen lds"
               :: "s"(lds_base), "v"(voff), "s"(rsrc), "s"(soff), "n"(OFF) : "memory", "scc");
}
#define SB __builtin_amdgcn_sched_barrier(0)
#define FENCE asm volatile("" ::: "memory")
#define PIN(a) asm volatile("" : "+v"(a))
#define WAIT_VM(n) asm volatile("s_waitcnt vmcnt(%0)" :: "n"(n) : "memory")
#define WAIT_LDS asm volatile("s_waitcnt lgkmcnt(0)" ::: "memory")
#define BARRIER asm volatile("s_waitcnt lgkmcnt(0)\n\ts_barrier" ::: "memory")
#define LD4(dst, voff, rsrc, soff, mod) asm volatile("buffer_load_dwordx4 %0, %1, %2, %3 offen " mod : "=v"(dst) : "v"(voff), "s"(rsrc), "s"(soff) : "memory")
#define LD2(dst, voff, rsrc, soff, mod) asm volatile("buffer_load_dwordx2 %0, %1, %2, %3 offen " mod : "=v"(dst) : "v"(voff), "s"(rsrc), "s"(soff) : "memory")
#define LD1(dst, voff, rsrc, soff, mod) asm volatile("buffer_load_dword %0, %1, %2, %3 offen " mod : "=v"(dst) : "v"(voff), "s"(rsrc), "s"(soff) : "memory")
#define ST4(src, voff, rsrc, soff, mod) asm volatile("buffer_store_dwordx4 %0, %1, %2, %3 offen " mod "\n\ts_nop 1" :: "v"(src), "v"(voff), "s"(rsrc), "s"(soff) : "memory")
#define ST2(src, voff, rsrc, soff, mod) asm volatile("buffer_store_dwordx2 %0, %1, %2, %3 offen " mod :: "v"(src), "v"(voff), "s"(rsrc), "s"(soff) : "memory")
DI f32x4 mfma16(bf16x8 a, bf16x8 b, f32x4 c) { return __builtin_amdgcn_mfma_f32_16x16x32_bf16(a, b, c, 0, 0, 0); }
DI f32x16 mfma(bf16x8 a, bf16x8 b, f32x16 c) { return __builtin_amdgcn_mfma_f32_32x32x16_bf16(a, b, c, 0, 0, 0); }
DI bf16x8 asbf(u32x4 v) { return __builtin_bit_cast(bf16x8, v); }
DI bf16x8 ld_frag(unsigned a) { return *(LDS const bf16x8*)a; }
DI bf16x8 tr_frag(unsigned a0, unsigned a1) {
  s16x4 lo = __builtin_amdgcn_ds_read_tr16_b64_v4i16((LDS s16x4*)a0);
  s16x4 hi = __builtin_amdgcn_ds_read_tr16_b64_v4i16((LDS s16x4*)a1);
  return __builtin_bit_cast(bf16x8, __builtin_shufflevector(lo, hi, 0, 1, 2, 3, 4, 5, 6, 7));
}
DI unsigned pk2(float a, float b) { f32x2 v = {a, b}; return __builtin_bit_cast(unsigned, __builtin_convertvector(v, bf16x2)); }
DI float bflo(unsigned u) { return __uint_as_float(u << 16); }
DI float bfhi(unsigned u) { return __uint_as_float(u & 0xffff0000u); }
DI float rbf(float f) { return bflo(pk2(f, 0.f)); }
DI f32x2 mul_clamp(f32x2 a, f32x2 b) { f32x2 r; asm("v_pk_mul_f32 %0, %1, %2 clamp" : "=v"(r) : "v"(a), "v"(b)); return r; }
constexpr float DN = 0x1p-40f, UP = 0x1p40f, LOG2E = 1.44269504f;
#define PHI_A(u) f32x2 pm[4], pe[4]; _Pragma("unroll") for (int j_ = 0; j_ < 4; ++j_) pm[j_] = mul_clamp(u[j_], (f32x2){DN, DN}); SB
#define PHI_E(j0) _Pragma("unroll") for (int j_ = (j0); j_ < (j0) + 2; ++j_) pe[j_] = (f32x2){__builtin_amdgcn_fmed3f(__builtin_amdgcn_exp2f(u[j_].x), 0.f, 1.f), \
                                                                                     __builtin_amdgcn_fmed3f(__builtin_amdgcn_exp2f(u[j_].y), 0.f, 1.f)}; SB
#define PHI_S _Pragma("unroll") for (int j_ = 0; j_ < 4; ++j_) pm[j_] = __builtin_elementwise_fma(pm[j_], (f32x2){UP / LOG2E, UP / LOG2E}, (f32x2){1e-6f, 1e-6f}); SB; \
              _Pragma("unroll") for (int j_ = 0; j_ < 4; ++j_) pm[j_] += pe[j_]; SB
#define PHI_B(o) PHI_S; const u32x4 o = {pk2(pm[0].x, pm[0].y), pk2(pm[1].x, pm[1].y), pk2(pm[2].x, pm[2].y), pk2(pm[3].x, pm[3].y)}
DI unsigned relu2(unsigned v) {
  typedef short s16x2 __attribute__((ext_vector_type(2)));
  return __builtin_bit_cast(unsigned, __builtin_elementwise_max(__builtin_bit_cast(s16x2, v), (s16x2){0, 0}));
}
DI float mul1(float a, float b) { float r; asm("v_mul_f32_e32 %0, %1, %2" : "=v"(r) : "v"(a), "v"(b)); return r; }
DI float sum8(float v) {
  v += __uint_as_float(__builtin_amdgcn_update_dpp(0u, __float_as_uint(v), 0xB1, 0xf, 0xf, true));
  v += __uint_as_float(__builtin_amdgcn_update_dpp(0u, __float_as_uint(v), 0x4E, 0xf, 0xf, true));
  v += __uint_as_float(__builtin_amdgcn_update_dpp(0u, __float_as_uint(v), 0x141, 0xf, 0xf, true));
  return v;
}
DI void lds_put(unsigned a, unsigned v) { FENCE; *(volatile LDS unsigned*)a = v; FENCE; }
DI void lds_wait(unsigned a, int n, unsigned need, int lane) {
  const unsigned ad = a + (lane & (n - 1)) * 4;
#pragma unroll 1
  for (unsigned sp = 0; sp < SPIN_CAP; ++sp) {
    FENCE; const unsigned v = *(volatile LDS unsigned*)ad; FENCE;
    if (__all(v >= need)) break;
    __builtin_amdgcn_s_sleep(LSLEEP);
  }
}
DI u64 tag_wait(unsigned voff, u32x4 rs, unsigned soff, u64 mask, u32x2 tag) {
  u64 bl = 0;
#pragma unroll 1
  for (unsigned sp = 0; sp < SPIN_CAP; ++sp) {
    u32x2 v;
    asm volatile("buffer_load_dwordx2 %0, %1, %2, %3 offen sc1\n\ts_waitcnt vmcnt(0)" : "=&v"(v) : "v"(voff), "s"(rs), "s"(soff) : "memory");
    bl = __ballot(v.x == tag.x && v.y == tag.y);
    if ((bl & mask) == mask) break;
    __builtin_amdgcn_s_sleep(TSLEEP);
  }
  return bl;
}

__global__ __launch_bounds__(512, 2) void fused(const u16* __restrict__ x, const u16* __restrict__ wqkv, const u16* __restrict__ w1, char* ws,
                                                const u16* __restrict__ wo, const u16* __restrict__ b1, const u16* __restrict__ w2,
                                                const u16* __restrict__ b2, u16* __restrict__ out) {
  TS_TOP;
  const int bid = blockIdx.x, tid = threadIdx.x, lane = tid & 63;
  const int w = __builtin_amdgcn_readfirstlane(tid >> 6);
  const int xc = bid & 7, ix = bid >> 3, sl = ix & 7, rl = ix >> 3;
  const int r = (xc >> 1) * 4 + rl, s0 = r >= 14 ? 0 : r >> 1;
  const u32x4 rx = mk_rsrc(x, S * D * 2);
  const unsigned vx0 = (unsigned)(r * 128 + (w & 1) * 64 + (lane >> 3)) * 896u + (unsigned)(((lane & 7) ^ (lane >> 4)) << 4), vx1 = (vx0 ^ 64u) + 7168u;
#define XGROUP(n) do { dma16o<(n) * 2048>(lx, vx0, rx, sg * 128 + (n) * 14336); dma16o<(n) * 2048 + 1024>(lx, vx1, rx, sg * 128 + (n) * 14336); } while (0)
  unsigned cpf;
  u64 pc; asm volatile("s_getpc_b64 %0" : "=s"(pc));
  if (w < 2) { const unsigned lx = w * 8192; const int sg = s0; XGROUP(0); XGROUP(1); XGROUP(2); XGROUP(3); }
  if (w == 4) LD1(cpf, lane < 9 ? (unsigned)(ix + lane * 32) * 128u : 0x80000000u, mk_rsrc((const void*)(pc & ~255ull), CODE_BYTES), 0u, "");
  asm volatile("" :: "s"(wqkv), "s"(w1), "s"(ws));
  const int c = (int)(((xc & 1) ? 0xEFB98765u : 0xDCA43210u) >> (4 * sl)) & 15;
  const bool kvt = sl >= 6, pure = sl == 7;
  const int rt = xc * 4 + rl, ct = sl + 2 * rl - (sl + 2 * rl >= 7 ? 7 : 0), rot2 = 7 * rl + xc - (7 * rl + xc >= 28 ? 28 : 0);
  const int l31 = lane & 31, hh = lane >> 5, q = (lane >> 2) & 3, p = lane & 3, blk = (lane >> 4) & 1;
  const unsigned slot_b = kvt ? 49152u : 40960u;
  const u32x4 rws = mk_rsrc(ws, WS_END);
  const unsigned v16 = lane * 16, v8 = lane * 8;
  TS_INIT;
  u32x2 tag;
  const unsigned swz = (unsigned)(((lane & 7) ^ ((lane >> 2) & 4)) << 4);
  f32x16 z16;
#pragma unroll
  for (int e = 0; e < 16; ++e) z16[e] = 0.f;

  u32x4 xr, br; unsigned sg_;
  const unsigned fxo = (unsigned)(rt * 64 + (tid >> 3)) * 896u + ct * 128 + (tid & 7) * 16;
#define FIN_LD do { LD4(xr, fxo, rx, 0u, ""); LD4(br, (tid & 7) * 16u, mk_rsrc(b2, D * 2), ct * 128u, ""); LD1(sg_, (unsigned)(tid & ~7) * 2u, rws, WS_ST + rt * 1024u, "sc1"); } while (0)
#define PUT_TILES(base, t) do { _Pragma("unroll") for (int n_ = 0; n_ < 8; ++n_) *(LDS f32x4*)((base) + n_ * 1024 + (v16 ^ (n_ * 32))) = \
      (f32x4){t[n_ >> 2][4 * (n_ & 3)], t[n_ >> 2][4 * (n_ & 3) + 1], t[n_ >> 2][4 * (n_ & 3) + 2], t[n_ >> 2][4 * (n_ & 3) + 3]}; } while (0)
  if (__builtin_expect(w < 4, 1)) {
    const int h = w, hp = h >> 1, hs_ = h & 1;
    const u32x4 rw2 = mk_rsrc(w2, F * D * 2), rwo = mk_rsrc(wo, D * D * 2);
    const unsigned vwo = (unsigned)(lane >> 3) * 896u + ct * 128 + swz;
    const unsigned vw2 = vwo + (unsigned)((h >> 1) * 896 + (h & 1) * 16) * 896u;
    const int hg = (h + xc + rl) & 3;
    u32x4 R[18];
#define ROT(u) ((u) + rot2 >= 28 ? (u) + rot2 - 28 : (u) + rot2)
#define PFLD(n) LD4(R[n], vw2, rw2, ROT(19 + ((n) >> 1)) * 28672u + ((n) & 1) * 7168u, "")
    auto fill = [&](int u, int g, int cnt) {
#pragma unroll 1
      for (int e = u + cnt; u < e; ++u, ++g) {
        const unsigned k = ROT(u) * 28672u;
        dma16(g * 8192 + h * 2048, vw2, rw2, k); dma16(g * 8192 + h * 2048 + 1024, vw2, rw2, k + 7168u);
      }
    };
    {
      __builtin_amdgcn_s_setprio(PRIO_H);
      const bool a1 = c < 10, b1_ = c < 9;
      const u32x4 r0 = mk_rsrc(a1 ? w1 : wqkv, a1 ? D * F * 2 : D * 3 * D * 2), r1 = mk_rsrc(b1_ ? w1 : wqkv, b1_ ? D * F * 2 : D * 3 * D * 2);
      const unsigned p0 = a1 ? 3584u : 2688u, p1 = b1_ ? 3584u : 2688u;
      unsigned V0, V1, V2, V3;
      {
        const unsigned kl = 32 * hs_ + (lane >> 3);
        const unsigned c0 = (a1 ? c : c - 10) * 384, c1 = c == 9 ? 512 : c0;
        const unsigned vk = (32 * hs_ + (lane >> 4)) * 2688 + ((224 * c - 1792) & ~63) + ((q ^ (lane >> 4)) << 6) + p * 16;
        V0 = kvt ? vk : kl * p0 + c0 + swz;
        V1 = kvt ? vk + 4 * 2688 : kl * p1 + c1 + swz + 128;
        V2 = kvt ? V0 + 896 : V1 + 128;
        V3 = V1 + 896;
      }
      int sg = (S0MODE ? s0 : 0) + hp; sg = sg == 7 ? 0 : sg;
      unsigned so = hp * slot_b;
#define GROUP(n) do { const unsigned kk = sg * 64 + 8 * (n), sa = kk * p0, sb_ = kk * p1; \
          if (i || hp) XGROUP(n); \
          dma16o<16384 + (n) * 1024>(lw, V0, r0, sa); \
          dma16o<24576 + (n) * 1024>(lw, V1, r1, sb_); \
          dma16o<32768 + (n) * 1024>(lw, V2, r1, sb_); \
          if (kvt) dma16o<40960 + (n) * 1024>(lw, V3, r1, sb_); } while (0)
      if (hp) BARRIER;
      unsigned t2;
#pragma unroll
      for (int i = 0; i < 4; ++i) {
        const int t = hp + 2 * i;
        if (i == 3 && hp) break;
        if (i >= 2 || (i == 1 && hp)) lds_wait(SY_DONE, 4, t - 2, lane);
        TSW(32 + t, 2 * hp);
        const unsigned lx = so + hs_ * 8192, lw = so + hs_ * 4096;
        GROUP(0); GROUP(1); GROUP(2); GROUP(3);
        TSW(40 + t, 2 * hp);
        if (i == 0 && !hp) BARRIER;
        if (i == 2 && hp) break;
        if (i == 3) lds_put(SY_GO, 1);
        if (i == 1) {
          LD1(t2, lane < (hp ? 0 : hs_ ? 13 : 49) ? lane * 128u : 0x80000000u, hs_ ? rwo : rw2, bid * (hs_ ? 1664u : 6272u), "");
          WAIT_VM(1);
        } else WAIT_VM(0);
        if (i == 2) asm volatile("" :: "v"(t2));
        lds_put(SY_FULL + h * 4, t + 2);
        TSW(48 + t, 2 * hp);
        if (!pure && ((i == 1 && hp) || i == 2)) {
#pragma unroll
          for (int n = 0; n < (i == 2 ? PFE : PFA); ++n) PFLD(n);
        }
        so = so == 0 ? 2 * slot_b : so - slot_b;
        sg = sg >= 5 ? sg - 5 : sg + 2;
      }
      tag = *(volatile LDS u32x2*)SY_TAG;
      if (hp) {
        u32x4 xf[8];
        unsigned bi;
        const bool chk = !hs_ || kvt || lane < 32;
        const u32x4 rb1 = mk_rsrc(b1, c < 10 ? F * 2 : 0);
#pragma unroll 1
        for (unsigned sp = 0; sp < SPIN_CAP; ++sp) {
#pragma unroll
          for (int k = 0; k < 7; ++k) LD4(xf[k], v16, rws, WS_CS + c * 14336u + hs_ * 1024u + k * 2048u, "sc1");
          LD4(xf[7], v16, rws, WS_ST + r * 2048u + hs_ * 1024u, "sc1");
          LD1(bi, lane * 4u, rb1, c * 384u + hs_ * 256u, "");
          WAIT_VM(9); lds_put(SY_FULL + h * 4, 7);
          TSW(53, 2);
          WAIT_VM(0);
          PIN(xf[0]); PIN(xf[7]); PIN(bi);
          if (kvt && l31 == 30) bi = 0x3f80u;
          unsigned bad = (xf[0].y ^ tag.x) | (xf[0].w ^ tag.x);
          float ax_ = __uint_as_float(xf[0].x), ay_ = __uint_as_float(xf[0].z);
#pragma unroll
          for (int k = 1; k < 7; ++k) {
            PIN(xf[k]);
            ax_ += __uint_as_float(xf[k].x); ay_ += __uint_as_float(xf[k].z); PIN(ax_); PIN(ay_);
            bad |= (xf[k].y ^ tag.x) | (xf[k].w ^ tag.x); PIN(bad);
          }
          const unsigned hi = pk2(ax_, ay_); ax_ -= bflo(hi); PIN(ax_); ay_ -= bfhi(hi);
          const unsigned mi = pk2(ax_, ay_); ax_ -= bflo(mi); PIN(ax_); ay_ -= bfhi(mi);
          const unsigned lo = pk2(ax_, ay_);
          *(LDS u32x4*)(L_CS + hs_ * 1024 + v16) = (u32x4){(hi & 0xffffu) | (mi << 16), (lo & 0xffffu) | (bi << 16),
                                                           (hi >> 16) | (mi & 0xffff0000u), (lo >> 16) | (bi & 0xffff0000u)};
          *(LDS unsigned*)(L_ST + hs_ * 256 + lane * 4) = xf[7].x;
          *(LDS float*)(L_RS + hs_ * 256 + lane * 4) = __builtin_amdgcn_rcpf(bfhi(xf[7].x)) * LOG2E;
          if (__builtin_expect(__all((bad == 0 || !chk) && xf[7].y == tag.x), 1)) break;
          __builtin_amdgcn_s_sleep(8);
        }
        lds_put(SY_FULL + h * 4, 8);
        TSW(3, 2);
        if (!pure) {
#pragma unroll
          for (int n = PFA; n < 18; ++n) PFLD(n);
        }
        lds_wait(SY_GO, 1, 1, lane);
        asm volatile("" :: "v"(t2));
      }
      __builtin_amdgcn_s_setprio(PRIO_P);
    }
    if (!pure) {
#pragma unroll
    for (int n = PFE; n < 18; ++n) { if (h < 2) PFLD(n); }
    const unsigned wb = 49152 + (h ^ 2) * 14336;
    lds_wait(SY_DONE, 4, h < 2 ? 6 : 5, lane);
#pragma unroll 1
    for (int n = 0; n < 14; ++n) dma16(wb + n * 1024, vwo, rwo, hg * 100352u + n * 7168u);
    lds_wait(SY_DONE, 4, 6, lane);
    fill(0, 16, 3);
    TSW(23, 0);
    WAIT_VM(6);
    bf16x8 WO[4][2][2];
    {
      const unsigned a0 = wb + (4 * hh + q) * 128 + (((q >> 1) & 1) << 6) + blk * 32 + p * 8, a1_ = a0 ^ 64u;
#pragma unroll
      for (int n = 0; n < 4; ++n)
#pragma unroll
        for (int sg = 0; sg < 2; ++sg) {
          const unsigned o = n * 3584 + sg * 2048;
          WO[n][sg][0] = tr_frag(a0 + o, a0 + o + 1024); WO[n][sg][1] = tr_frag(a1_ + o, a1_ + o + 1024);
        }
    }
#pragma unroll
    for (int n = 0; n < 8; ++n) {
      u32x4 t = __builtin_bit_cast(u32x4, WO[n >> 1][1][n & 1]);
      if (hh) { t.z = 0; t.w = 0; }
      WO[n >> 1][1][n & 1] = asbf(t);
    }
#pragma unroll
    for (int n = 0; n < 16; ++n) PIN(WO[n >> 2][(n >> 1) & 1][n & 1]);
    lds_put(SY_WOK + h * 4, 1);
    TSW(29, 0);
    if (EARLY) fill(16, 13, 3);
    lds_wait(SY_DONE, 4, c < 10 ? FILLAT : 7, lane);
    fill(3, 0, 6);
    lds_wait(SY_WOK, 4, 1, lane);
    fill(9, 6, 3);
    WAIT_VM(6); lds_put(SY_FULL2 + h * 4, 1);
    TSW(31, 0);
    fill(12, 9, EARLY ? 4 : 7);
    TSW(20, 0);
    lds_wait(SY_DONE, 4, 9, lane);
#pragma unroll
    for (int n = 0; n < 18; ++n) { PIN(R[n]); *(LDS u32x4*)((n < 6 ? 131072 : -24576) + h * 2048 + v16 + (n >> 1) * 8192 + (n & 1) * 1024) = R[n]; }
    WAIT_VM(0); lds_put(SY_RDY + h * 4, 1);
    TSW(4, 0);
    u32x4 PQ[2][7], KV[8];
    __builtin_amdgcn_s_setprio(PRIO_S);
    lds_wait(SY_HOK, 1, 1, lane);
    if (*(volatile LDS unsigned*)SY_HOK < 2u) tag_wait(min(lane, 4) * FS, rws, WS_FL + (rt * 32 + 19) * FS, ~0ull, tag);
#pragma unroll
    for (int n = 0; n < 14; ++n) LD4(PQ[n / 7][n % 7], v16, rws, WS_PQ + ((rt * 2 + n / 7) * 28 + hg * 7 + n % 7) * 1024u, "sc1");
    FIN_LD;
    TSW(19, 0);
    __builtin_amdgcn_s_setprio(PRIO_K);
    tag_wait(min(lane, 7) * FS, rws, WS_KF + (xc * 32 + hg * 8) * FS, ~0ull, tag);
#pragma unroll
    for (int n = 0; n < 8; ++n) LD4(KV[n], v16, rws, WS_KV + hg * 8192u + n * 1024u, "sc1");
    __builtin_amdgcn_s_setprio(PRIO_M);
    TSW(5, 0);
    u32x4 PB[2][4];
#pragma unroll
    for (int n = 0; n < 14; ++n) PIN(PQ[n / 7][n % 7]);
#pragma unroll
    for (int u = 0; u < 8; ++u) {
      const u32x4* P = PQ[u >> 2]; const int n = u & 3, lo = n < 3 ? 2 * n + 1 : 5, hi = n ? 2 * n - 1 : 1;
      PB[u >> 2][n] = (u32x4){P[lo].x, P[lo].y, P[hi].z, P[hi].w}; PIN(PB[u >> 2][n]);
    }
    WAIT_VM(4);
    TSW(16, 0);
#pragma unroll
    for (int n = 0; n < 4; ++n) PIN(KV[n]);
    f32x16 nm[3], as[2][2] = {{z16, z16}, {z16, z16}};
    u32x4 O[2][2]; float rd[2];
#define NUM0(u) nm[(u) % 3] = mfma(asbf(KV[2 * ((u) & 3)]), asbf(PQ[(u) >> 2][2 * ((u) & 3)]), z16)
#define NUM1(u) nm[(u) % 3] = mfma(asbf(KV[2 * ((u) & 3) + 1]), asbf(PB[(u) >> 2][(u) & 3]), nm[(u) % 3])
#define DEN(u) __uint_as_float(__builtin_amdgcn_permlane32_swap(__float_as_uint(nm[(u) % 3][12]), __float_as_uint(nm[(u) % 3][12]), false, false)[1])
#define APP(u, i) as[(u) >> 2][(i) & 1] = mfma(WO[(u) & 3][(i) >> 1][(i) & 1], asbf(O[(u) & 1][(i) >> 1]), as[(u) >> 2][(i) & 1])
#define MULS(a, b) _Pragma("unroll") for (int e = (a); e < (b); ++e) o[e] = mul1(nm[u % 3][e], rd[u & 1])
#define CVTS(a, b) _Pragma("unroll") for (int e = (a); e < (b); ++e) O[u & 1][e >> 2][e & 3] = pk2(o[2 * e], o[2 * e + 1])
    NUM0(0); NUM1(0); NUM0(1); NUM1(1);
    rd[0] = __builtin_amdgcn_rcpf(DEN(0) + 1e-6f);
    asm volatile("s_waitcnt vmcnt(0)" : "+v"(rd[0]), "+v"(KV[4]), "+v"(KV[5]), "+v"(KV[6]), "+v"(KV[7]) :: "memory");
    SB;
    unsigned d2 = 0; bool put0 = false;
#pragma unroll
    for (int u = 0; u < 8; ++u) {
      float o[16], dn;
      if (u) APP(u - 1, 0); SB; MULS(0, 6); SB;
      if (u) APP(u - 1, 1); SB; MULS(6, 10); CVTS(0, 2); SB;
      if (u) APP(u - 1, 2); SB; MULS(10, 13); CVTS(2, 4); SB;
      if (u) APP(u - 1, 3); SB; MULS(13, 16); CVTS(4, 6); SB;
      if (u < 6) NUM0(u + 2); SB; if (u < 7) dn = DEN(u + 1) + 1e-6f; SB;
      if (u < 6) NUM1(u + 2); SB; if (u < 7) rd[(u + 1) & 1] = __builtin_amdgcn_rcpf(dn); SB; CVTS(6, 8);
      if (u == 4) d2 = *(volatile LDS unsigned*)(SY_DONE2 + p * 4);
      if (u == 5) { put0 = __all(d2 >= 1u); if (put0) PUT_TILES(49152 + h * 16384, as[0]); }
      SB;
    }
    APP(7, 0); APP(7, 2); APP(7, 1); APP(7, 3);
    TSW(21, 0);
    if (!put0) { lds_wait(SY_DONE2, 4, 1, lane); PUT_TILES(49152 + h * 16384, as[0]); }
    PUT_TILES(57344 + h * 16384, as[1]);
    TSW(17, 0);
    }
  } else {
  const int cw = w - 4, wl = cw & 1, wh = cw >> 1;
  u32x4 xs[7];
  if (cw == 3) {
    const unsigned va = (unsigned)(r * 128 + c * 8 + (lane >> 3)) * 896u + (lane & 7) * 16;
#pragma unroll
    for (int k = 0; k < 7; ++k) LD4(xs[k], va, rx, k * 128u, "");
  }
  *(LDS unsigned*)(L_SY + lane * 4) = (lane >> 1) == 1; BARRIER;
  __builtin_amdgcn_s_setprio(PRIO_C);
  bf16x8 ones = asbf((u32x4){0x3f803f80u, 0x3f803f80u, 0x3f803f80u, 0x3f803f80u});
  PIN(ones);
  {
    {
      u64 ep;
      asm volatile("s_load_dwordx2 %0, %1, 0x0\n\ts_waitcnt lgkmcnt(0) vmcnt(0)" : "=&s"(ep) : "s"(ws) : "memory");
      tag.x = (unsigned)ep + 1u; tag.y = (unsigned)(ep >> 32) + 1u;
    }
    if (cw == 0) { asm volatile("" :: "v"(cpf)); *(volatile LDS u32x2*)SY_TAG = tag; }
    if (cw == 3) {
      float sq[2] = {0.f, 0.f}, sm[2] = {0.f, 0.f};
      const bf16x2 one2 = __builtin_bit_cast(bf16x2, 0x3f803f80u);
#pragma unroll
      for (int k = 0; k < 7; ++k) {
        PIN(xs[k]);
#pragma unroll
        for (int e = 0; e < 4; ++e) {
          const unsigned u = xs[k][e];
          const bf16x2 v = __builtin_bit_cast(bf16x2, u);
          sq[e & 1] = __builtin_amdgcn_fdot2_f32_bf16(v, v, sq[e & 1], false); sm[e & 1] = __builtin_amdgcn_fdot2_f32_bf16(v, one2, sm[e & 1], false);
        }
      }
      const float m = sum8(sm[0] + sm[1]) * (1.f / D);
      const float var = fmaxf(sum8(sq[0] + sq[1]) * (1.f / D) - m * m, 0.f);
      const unsigned sv = __builtin_amdgcn_ds_bpermute(lane * 32, pk2(-m, __builtin_amdgcn_sqrtf(rbf(rbf(var) + 1e-5f))));
      const u32x4 gr = {sv, tag.x, sv, tag.x};
      if (lane < 8) ST4(gr, v16, rws, WS_ST + (r * 128 + c * 8) * 16, "sc1");
    }
  }
  const unsigned fl_off = WS_FL + ((r * 2 + wl) * 32 + 2 * c + wh) * FS;
  const unsigned fa = SY_FULL + p * 4;
#define CHECK_FULL(v, need) do { if (__builtin_expect(__any((v) < (need)), 0)) lds_wait(SY_FULL, 4, need, lane); FENCE; } while (0)
#define RAMP do { if (PD && ((PMASK >> cw) & 1)) __builtin_amdgcn_s_sleep(PD); if (PD2 && ((PMASK2 >> cw) & 1)) __builtin_amdgcn_s_sleep(PD2); } while (0)
#define COLSUM(a_, t_, tile) do { const unsigned a = (a_); bf16x8 f[4]; \
    _Pragma("unroll") for (int ks = 0; ks < 4; ++ks) f[ks] = tr_frag(a + ks * 2048, a + ks * 2048 + (t_)); \
    SB; f32x16 cs = z16; \
    _Pragma("unroll") for (int ks = 0; ks < 4; ++ks) cs = mfma(ones, f[ks], cs); \
    const u32x4 gr = {(unsigned)__builtin_amdgcn_ds_bpermute(lane * 8, __float_as_int(cs[0])), tag.x, (unsigned)__builtin_amdgcn_ds_bpermute(lane * 8 + 4, __float_as_int(cs[0])), tag.x}; \
    if (lane < 16) ST4(gr, v16, rws, WS_CS + (c * 7 + s0) * 2048 + (tile) * 256, "sc1"); } while (0)
  unsigned da = SY_DONE + cw * 4;
  PIN(da);
  const unsigned xm = hh ? 0u : ~0u;
  if (!kvt) {
    const unsigned xa = (wl * 64 + l31) * 128 + ((hh ^ ((l31 >> 1) & 7)) << 4);
    const unsigned wr = 16384 + (8 * hh + q) * 128 + blk * 32 + p * 8;
    const unsigned wa0 = wr + wh * 8192 + ((wh ^ (q >> 1)) << 6), wa1 = wr + wh * 16384 + ((wh ^ 1 ^ (q >> 1)) << 6);
    f32x16 acc[2][3] = {{z16, z16, z16}, {z16, z16, z16}};
    bf16x8 X[4][2], W[4][3];
#define RDA(n, ks) do { \
      if ((n) == 0 || (n) == 2) X[ks][(n) >> 1] = ld_frag(ax[ks] + ((n) >> 1) * 4096); \
      else { const unsigned a_ = ((n) == 3 ? a1_ : a0_) + ((n) == 4 ? 8192 : 0) + (ks) * 2048; W[ks][(n) == 1 ? 0 : (n) - 2] = tr_frag(a_, a_ + 512); } } while (0)
#define MMA(ks, m) acc[(m) & 1][(m) >> 1] = mfma(W[ks][(m) >> 1], X[ks][(m) & 1], acc[(m) & 1][(m) >> 1])
    unsigned ax[4] = {xa, xa ^ 32u, xa ^ 64u, xa ^ 96u}, a0_ = wa0, a1_ = wa1;
#pragma unroll
    for (int n = 0; n < 6; ++n) PIN(acc[n & 1][n >> 1]);
    lds_wait(SY_FULL, 4, 1, lane);
    RAMP;
    TSW(6, 4); TSC(24, 4);
    if (r < 14 && !((r & 1) && wl)) {
      const int b = (r & 1) ? 2 : wl;
      COLSUM(b == 1 ? wa1 : wa0 + b * 4096, 512, 3 * wh + b);
    }
#pragma unroll
    for (int n = 0; n < 10; ++n) { RDA(n % 5, n / 5); SB; }
    unsigned tv = 1; int ts = 0;
    PIN(tv);
#pragma unroll 1
    for (;;) {
      unsigned fv; int dl;
#pragma unroll
      for (int m = 0; m < 12; ++m) {
        MMA(m / 6, m % 6); SB;
        if (m % 6 < 5) RDA(m % 6, 2 + m / 6);
        if (m == 0) { dl = ((0x24 >> ts) & 1) ? -81920 : 40960; ++ts; asm volatile("" : "+s"(dl), "+s"(ts)); }
        if (m == 2) { ax[0] += dl; ax[1] += dl; PIN(ax[0]); PIN(ax[1]); }
        if (m == 5) fv = *(volatile LDS unsigned*)fa;
        if (m == 10) *(volatile LDS unsigned*)da = tv;
        if (m == 11) { a0_ += dl; a1_ += dl; ++tv; CHECK_FULL(fv, tv); TSW(55 + ts, STW); }
        SB;
      }
      if (__builtin_expect(ts == 7, 0)) break;
#pragma unroll
      for (int m = 0; m < 12; ++m) {
        MMA(2 + m / 6, m % 6); SB;
        if (m % 6 < 5) RDA(m % 6, m / 6);
        if (m == 5) { ax[2] += dl; ax[3] += dl; if (LNOP) __builtin_amdgcn_s_sleep(LNOP); }
        SB;
      }
    }
    TSW(7, 4); TSC(25, 4);
    unsigned st[2]; u32x2 cv[3];
#pragma unroll
    for (int a = 0; a < 2; ++a) st[a] = *(LDS const unsigned*)(L_ST + (wl * 64 + a * 32 + l31) * 4);
#pragma unroll
    for (int b = 0; b < 3; ++b) cv[b] = *(LDS const u32x2*)(L_CS + ((3 * wh + b) * 32 + l31) * 8);
    SB;
    MMA(2, 0); MMA(2, 1); MMA(3, 0); MMA(3, 1);
    SB;
    bf16x8 xe[2];
#pragma unroll
    for (int a = 0; a < 2; ++a) xe[a] = asbf((u32x4){((st[a] & 0xffffu) | (st[a] << 16)) & xm, st[a] & xm, 0u, 0u});
#define MME(m) acc[(m) & 1][(m) >> 1] = mfma(asbf((u32x4){cv[(m) >> 1].x, cv[(m) >> 1].y, cv[(m) >> 1].x, cv[(m) >> 1].y}), xe[(m) & 1], acc[(m) & 1][(m) >> 1])
    MME(0); MME(1);
    SB;
    TSW(18, 4);
    const int R0 = r * 4 + wl * 2;
    const unsigned sk = c == 9 ? 0x40000000u : 0u;
#define MM6(b, j) do { if ((j) < 4) MMA(2 + ((j) >> 1), 2 * (b) + ((j) & 1)); else MME(2 * (b) + ((j) & 1)); } while (0)
    if (2 * c + wh < 19) {
      const unsigned ob = WS_H + (R0 * 112 + 12 * c + 6 * wh) * 1024u;
#pragma unroll
      for (int b = 0; b < 3; ++b)
#pragma unroll
        for (int n = 0; n < 4; ++n) {
          const int a = n >> 1, e = 8 * (n & 1);
          float t[8]; u32x4 o;
          if (b < 2) MM6(b + 1, n < 2 ? 2 * n : n + 2);
          SB;
          if (n == 0) asm volatile("s_nop 10" : "+v"(acc[0][b]), "+v"(acc[1][b]));
          asm volatile("v_mul_f32_e64 %0, %8, |%8|\n\tv_mul_f32_e64 %1, %9, |%9|\n\tv_mul_f32_e64 %2, %10, |%10|\n\tv_mul_f32_e64 %3, %11, |%11|\n\t"
                       "v_mul_f32_e64 %4, %12, |%12|\n\tv_mul_f32_e64 %5, %13, |%13|\n\tv_mul_f32_e64 %6, %14, |%14|\n\tv_mul_f32_e64 %7, %15, |%15|"
                       : "=&v"(t[0]), "=&v"(t[1]), "=&v"(t[2]), "=&v"(t[3]), "=&v"(t[4]), "=&v"(t[5]), "=&v"(t[6]), "=&v"(t[7])
                       : "v"(acc[a][b][e]), "v"(acc[a][b][e + 1]), "v"(acc[a][b][e + 2]), "v"(acc[a][b][e + 3]), "v"(acc[a][b][e + 4]), "v"(acc[a][b][e + 5]),
                         "v"(acc[a][b][e + 6]), "v"(acc[a][b][e + 7]));
          SB;
          if (b < 2 && n < 2) MM6(b + 1, 2 * n + 1);
          SB;
#pragma unroll
          for (int j = 0; j < 4; ++j) o[j] = pk2(t[2 * j], t[2 * j + 1]);
#pragma unroll
          for (int j = 0; j < 4; ++j) o[j] = relu2(o[j]);
          ST4(o, v16, rws, ob + a * (112 * 1024u) + (2 * b + (n & 1)) * 1024u + (b == 2 ? sk : 0u), HMOD);
          SB;
        }
    } else {
#pragma unroll
      for (int j = 0; j < 12; ++j) MM6(1 + j / 6, j % 6);
      const unsigned ob = WS_PQ + (R0 * 28 + (c == 9 ? 22 : (c - 10) * 12 + wh * 6)) * 1024u;
      SB;
#pragma unroll
      for (int a = 0; a < 2; ++a) {
        const float rl = __builtin_amdgcn_rcpf(bfhi(st[a])) * LOG2E;
#pragma unroll
        for (int n = 0; n < 6; ++n) {
          f32x2 u[4];
#pragma unroll
          for (int j = 0; j < 4; ++j) u[j] = (f32x2){acc[a][n >> 1][8 * (n & 1) + 2 * j], acc[a][n >> 1][8 * (n & 1) + 2 * j + 1]} * rl;
          PHI_A(u); PHI_E(0); PHI_E(2); PHI_B(o);
          ST4(o, v16, rws, ob + a * (28 * 1024u) + n * 1024u + (n < 2 ? sk : 0u), HMOD);
          SB;
        }
      }
    }
  } else {
    const unsigned xa = l31 * 128 + ((hh ^ ((l31 >> 1) & 7)) << 4);
    const int ok_ = (c & 1) * 32 + 56 * cw + 8 * ((4 * blk + p + cw) & 7), ov_ = (c & 1) * 32 + 56 * cw + 32 * blk + 8 * p;
    const unsigned ka = 16384 + hh * 1024 + q * 256 + ((((ok_ >> 6) & 3) ^ q) << 6) + (ok_ & 63);
    const unsigned va = 32768 + hh * 1024 + q * 256 + ((((ov_ >> 6) & 3) ^ q) << 6) + (ov_ & 63) + ((lane & 19) == 19 ? 0x100000 : 0);
    f32x16 acc[4][2] = {{z16, z16}, {z16, z16}, {z16, z16}, {z16, z16}};
    bf16x8 X[4][4], W[4][2];
#define RDK(n, ks) do { \
      if ((n) == 1 || (n) == 5) { const unsigned a_ = ((n) == 1 ? a0_ : a1_) + (ks) * 2048; W[ks][(n) >> 2] = tr_frag(a_, a_ + 8192); } \
      else X[ks][(n) ? (n) - 1 : 0] = ld_frag(ax[ks] + ((n) ? (n) - 1 : 0) * 4096); } while (0)
#define MMK(ks, m) acc[(m) & 3][(m) >> 2] = mfma(X[ks][(m) & 3], W[ks][(m) >> 2], acc[(m) & 3][(m) >> 2])
    unsigned ax[4] = {xa, xa ^ 32u, xa ^ 64u, xa ^ 96u}, a0_ = ka, a1_ = va;
#pragma unroll
    for (int n = 0; n < 8; ++n) PIN(acc[n & 3][n >> 2]);
    lds_wait(SY_FULL, 4, 1, lane);
    if (r < 14) COLSUM((r & 1) ? va : ka, 8192, 2 * cw + (r & 1));
#pragma unroll
    for (int n = 0; n < 12; ++n) { RDK(n % 6, n / 6); SB; }
    unsigned tv = 1; int ts = 0;
    PIN(tv);
#pragma unroll 1
    for (;;) {
      unsigned fv; int dl;
#pragma unroll
      for (int m = 0; m < 16; ++m) {
        MMK(m >> 3, m & 7); SB;
        if ((m & 7) >= 1 && (m & 7) < 7) RDK((m & 7) - 1, 2 + (m >> 3));
        if (m == 0) { dl = ((0x24 >> ts) & 1) ? -98304 : 49152; ++ts; asm volatile("" : "+s"(dl), "+s"(ts)); }
        if (m == 7) { ax[0] += dl; ax[1] += dl; PIN(ax[0]); PIN(ax[1]); }
        if (m == 8) fv = *(volatile LDS unsigned*)fa;
        if (m == 14) *(volatile LDS unsigned*)da = tv;
        if (m == 15) { a0_ += dl; a1_ += dl; ++tv; CHECK_FULL(fv, tv); TSW(55 + ts, STW); }
        SB;
      }
      if (__builtin_expect(ts == 7, 0)) break;
#pragma unroll
      for (int m = 0; m < 16; ++m) {
        MMK(2 + (m >> 3), m & 7); SB;
        if ((m & 7) >= 1 && (m & 7) < 7) RDK((m & 7) - 1, m >> 3);
        if (m == 7) { ax[2] += dl; ax[3] += dl; }
        SB;
      }
    }
    TSW(8, 4);
    unsigned st[4]; u32x2 cv[2]; f32x4 rsv[2][4];
#pragma unroll
    for (int a = 0; a < 4; ++a) st[a] = *(LDS const unsigned*)(L_ST + (a * 32 + l31) * 4);
#pragma unroll
    for (int b = 0; b < 2; ++b) cv[b] = *(LDS const u32x2*)(L_CS + ((2 * cw + b) * 32 + l31) * 8);
    unsigned rsa = L_RS + hh * 16;
    PIN(rsa);
#define MK6(a, j) do { if ((j) < 4) MMK(2 + ((j) >> 1), (a) + 4 * ((j) & 1)); else acc[a][(j) & 1] = mfma(asbf((u32x4){((st[a] & 0xffffu) | (st[a] << 16)) & xm, st[a] & xm, 0u, 0u}), \
        asbf((u32x4){cv[(j) & 1].x, cv[(j) & 1].y, cv[(j) & 1].x, cv[(j) & 1].y}), acc[a][(j) & 1]); } while (0)
#pragma unroll
    for (int n = 0; n < 4; ++n) rsv[0][n] = *(LDS const f32x4*)(rsa + n * 32);
    SB;
#pragma unroll
    for (int j = 0; j < 6; ++j) MK6(0, j);
    SB;
    f32x16 kv = z16;
    u32x4 fk, fv;
#pragma unroll
    for (int gi = 0; gi < 8; ++gi) {
      const int a = gi >> 1, sg = gi & 1, e = 8 * sg;
      if (a < 3 && sg == 0) {
#pragma unroll
        for (int n = 0; n < 4; ++n) rsv[(a + 1) & 1][n] = *(LDS const f32x4*)(rsa + (a + 1) * 128 + n * 32);
      }
      const f32x4 ra_ = rsv[a & 1][2 * sg], rb_ = rsv[a & 1][2 * sg + 1];
      const f32x2 rl[4] = {{ra_[0], ra_[1]}, {ra_[2], ra_[3]}, {rb_[0], rb_[1]}, {rb_[2], rb_[3]}};
      f32x2 u[4], vv[4];
#pragma unroll
      for (int j = 0; j < 4; ++j) u[j] = (f32x2){acc[a][0][e + 2 * j], acc[a][0][e + 2 * j + 1]} * rl[j];
#pragma unroll
      for (int j = 0; j < 4; ++j) vv[j] = (f32x2){acc[a][1][e + 2 * j], acc[a][1][e + 2 * j + 1]} * rl[j];
      PHI_A(u);
      if (gi) kv = mfma(asbf(fk), asbf(fv), kv);
      SB;
      PHI_E(0);
      if (a < 3) MK6(a + 1, 3 * sg);
      SB;
      PHI_E(2);
      if (a < 3) MK6(a + 1, 3 * sg + 1);
      SB;
      fv = (u32x4){pk2(vv[0].x, vv[0].y), pk2(vv[1].x, vv[1].y), pk2(vv[2].x, vv[2].y), pk2(vv[3].x, vv[3].y)};
      SB;
      if (a < 3) MK6(a + 1, 3 * sg + 2);
      SB;
      float po[8];
#pragma unroll
      for (int j = 0; j < 8; ++j) { po[j] = pe[j >> 1][j & 1] + 1e-6f; PIN(po[j]); }
#pragma unroll
      for (int j = 0; j < 4; ++j) pm[j] = __builtin_elementwise_fma(pm[j], (f32x2){UP / LOG2E, UP / LOG2E}, (f32x2){po[2 * j], po[2 * j + 1]});
      fk = (u32x4){pk2(pm[0].x, pm[0].y), pk2(pm[1].x, pm[1].y), pk2(pm[2].x, pm[2].y), pk2(pm[3].x, pm[3].y)};
      SB;
    }
    kv = mfma(asbf(fk), asbf(fv), kv);
    kv = kv * (l31 == 28 ? 1.f / 1.4453125f : 1.f / LOG2E);
    const unsigned ko = WS_KVP + (((c - 12) * 4 + cw) * 16 + r) * 4096u;
#pragma unroll
    for (int e = 0; e < 4; ++e) {
      const u32x4 o = {pk2(kv[4 * e], kv[4 * e + 1]), tag.x, pk2(kv[4 * e + 2], kv[4 * e + 3]), tag.x};
      ST4(o, v16, rws, ko + e * 1024u, "sc1");
    }
  }
  lds_put(da, 8);
  TSW(22, 4);
  bf16x8 WH[9][4];
  unsigned ra[3][2];
#pragma unroll
  for (int cb = 0; cb < 2; ++cb) {
    ra[0][cb] = wh * 4096 + (4 * hh + q) * 128 + ((cb ^ (q >> 1)) << 6) + blk * 32 + p * 8; ra[1][cb] = ra[0][cb] + 65536; ra[2][cb] = ra[0][cb] + 131072;
    PIN(ra[0][cb]); PIN(ra[1][cb]); PIN(ra[2][cb]);
  }
#define RDW(dst, n, reg) do { const unsigned a_ = ra[(reg) >> 3][(n) & 1] + ((reg) & 7) * 8192 + ((n) >> 1) * 2048; dst[n] = tr_frag(a_, a_ + 1024); } while (0)
  if (!kvt) WAIT_VM(0);
  if (lane < 8) ST4(((u32x4){tag.x, tag.y, tag.x, tag.y}), v16, rws, fl_off, "sc1");
  TSW(9, 4);
  if (!pure) {
    lds_wait(SY_FULL2, 4, 1, lane);
#pragma unroll
    for (int n = 0; n < 36; ++n) RDW(WH[n >> 2], n & 3, (16 + (n >> 2)) % 19);
    lds_put(da, 9);
  }
  TSW(30, 4);

  if (!pure) {
    const int kh = wh, R = rt * 2 + wl;
    if (cw == 0) {
      const u64 bl = tag_wait(min(lane, 23) * FS, rws, WS_FL + rt * 32 * FS, 0x7FFFFull, tag);
      lds_put(SY_HOK, (bl & 0xF80000ull) == 0xF80000ull ? 2 : 1);
    } else lds_wait(SY_HOK, 1, 1, lane);
    WAIT_VM(0);
    TSW(12, 4);
    const unsigned hs = WS_H + (R * 112 + kh * 56) * 1024u;
    constexpr int NH = NHW, E = 28 - NH;
    u32x4 hf[NH][2];
#define HLD(u) do { const unsigned o_ = hs + ROT(u) * 2048u; \
      LD4(hf[(u) % NH][0], v16, rws, o_, "sc1"); LD4(hf[(u) % NH][1], v16, rws, o_, "offset:1024 sc1"); } while (0)
#pragma unroll
    for (int u = 0; u < NH; ++u) HLD(u);
    f32x16 af[2] = {z16, z16};
    bf16x8 W[LA + 1][4];
    TSW(13, 4); TSC(26, 4);
#pragma unroll
    for (int u = 0; u < 28; ++u) {
      constexpr auto total = [](int u_) { return 2 * NH + 2 * (u_ < E ? u_ : E) + (u_ > E ? 3 : 0); };
      asm volatile("s_waitcnt vmcnt(%2)" : "+v"(hf[u % NH][0]), "+v"(hf[u % NH][1]) : "n"(total(u) - 2 - 2 * u) : "memory");
      if (u == 9 - LA) { lds_wait(SY_RDY, 4, 1, lane); TSW(28, 4); }
#pragma unroll
      for (int n = 0; n < 4; ++n) {
        af[n & 1] = mfma(u < 9 ? WH[u < 9 ? u : 0][n] : W[u % (LA + 1)][n], asbf(hf[u % NH][n >> 1]), af[n & 1]); SB;
        if (u + LA >= 9 && u + LA < 28) RDW(W[(u + LA) % (LA + 1)], n, (u + LA - 3) % 19);
        SB;
      }
      if (u == 16 - LA) lds_put(SY_DONE2 + cw * 4, 1);
      if (u == 20 - LA) lds_put(SY_DONE2 + cw * 4, 2);
      if (u < E) HLD(u + NH);
      else if (u == E) FIN_LD;
    }
    TSW(14, 4); TSC(27, 4);
    __builtin_amdgcn_s_setprio(0);
    lds_wait(SY_DONE2, 4, 2, lane);
    PUT_TILES(114688 + cw * 8192, af);
    WAIT_VM(0);
  }
  }
  if (pure) {
    const int id = xc * 4 + (ix >> 3), hd = id >> 1;
    lds_wait(SY_DONE, 4, 8, lane);
    f32x4 a, b;
#pragma unroll 1
    for (unsigned sp = 0; sp < SPIN_CAP; ++sp) {
      u32x4 pr[4];
#pragma unroll
      for (int n = 0; n < 4; ++n) LD4(pr[n], v16, rws, WS_KVP + (hd * 16 + 2 * w + (n >> 1)) * 4096u + (id & 1) * 2048u + (n & 1) * 1024u, "sc1");
      WAIT_VM(0);
      unsigned bad = 0;
#pragma unroll
      for (int n = 0; n < 4; ++n) { PIN(pr[n]); bad |= (pr[n].y ^ tag.x) | (pr[n].w ^ tag.x); }
#define F4(n) (f32x4){bflo(pr[n].x), bfhi(pr[n].x), bflo(pr[n].z), bfhi(pr[n].z)}
      a = F4(0) + F4(2); b = F4(1) + F4(3);
      if (__all(bad == 0)) break;
      __builtin_amdgcn_s_sleep(RSLEEP);
    }
    TSW(19, 4);
    *(LDS f32x4*)(w * 2048 + v16) = a; *(LDS f32x4*)(w * 2048 + 1024 + v16) = b;
    BARRIER;
    TSW(20, 4);
    if (w == 4) {
      a = *(LDS const f32x4*)v16; b = *(LDS const f32x4*)(1024 + v16);
#pragma unroll
      for (int n = 1; n < 8; ++n) { a += *(LDS const f32x4*)(n * 2048 + v16); b += *(LDS const f32x4*)(n * 2048 + 1024 + v16); }
      u32x4 o = {pk2(a[0], a[1]), pk2(a[2], a[3]), pk2(b[0], b[1]), pk2(b[2], b[3])};
      if ((id & 1) && hh != (hd & 1)) { if (hd & 2) { o.x = 0; o.y = 0; } else { o.z = 0; o.w = 0; } }
      ST4(o, v16, rws, WS_KV + id * 1024u, "sc1");
      TSW(21, 4);
      WAIT_VM(0);
      ST4(((u32x4){tag.x, tag.y, tag.x, tag.y}), (lane >> 3) * 32 * FS + (lane & 7) * 16, rws, WS_KF + id * FS, "sc1");
      TSW(10, 4); TS_DUMP;
    } else if (w == 5 && bid == 255) {
#pragma unroll 1
      for (int n = 0; n < 4; ++n) tag_wait((unsigned)((lane >> 4) * 64 + (lane & 15) * 2) * FS, rws, WS_FL + n * 256 * FS, ~0ull, tag);
      if (lane == 0) ST2(tag, 0u + lane, rws, WS_EP, "sc1");
    }
    return;
  }
  BARRIER;
  TSW(15, 4);
  {
    const unsigned a0 = 49152 + (tid >> 8) * 8192 + (tid & 7) * 1024 + ((((tid >> 3) & 31) ^ ((tid & 7) * 2)) << 4);
    f32x4 f[2][2], sp[4][2];
#pragma unroll
    for (int n = 0; n < 2; ++n) { f[n][0] = *(LDS const f32x4*)(a0 + 65536 + n * 16384); f[n][1] = *(LDS const f32x4*)(a0 + 65536 + n * 16384 + 512); }
#pragma unroll
    for (int n = 0; n < 4; ++n) { sp[n][0] = *(LDS const f32x4*)(a0 + n * 16384); sp[n][1] = *(LDS const f32x4*)(a0 + n * 16384 + 512); }
    SB;
    PIN(xr); PIN(br); PIN(sg_);
    const float rs = __builtin_amdgcn_rcpf(bfhi(sg_)), rs2 = rs * rs;
    u32x4 ov;
#pragma unroll
    for (int e = 0; e < 4; ++e) {
      const int hf_ = e >> 1, j = (e & 1) * 2;
      const float t0 = fmaf(f[0][hf_][j] + f[1][hf_][j], rs2, bflo(br[e])), t1 = fmaf(f[0][hf_][j + 1] + f[1][hf_][j + 1], rs2, bfhi(br[e]));
      const float g0 = t0 * __builtin_amdgcn_rcpf(1.f + __expf(-t0)), g1 = t1 * __builtin_amdgcn_rcpf(1.f + __expf(-t1));
      const float s0_ = (sp[0][hf_][j] + sp[1][hf_][j]) + (sp[2][hf_][j] + sp[3][hf_][j]);
      const float s1_ = (sp[0][hf_][j + 1] + sp[1][hf_][j + 1]) + (sp[2][hf_][j + 1] + sp[3][hf_][j + 1]);
      ov[e] = pk2(fmaf(s0_, g0, bflo(xr[e])), fmaf(s1_, g1, bfhi(xr[e])));
    }
    ST4(ov, fxo, mk_rsrc(out, S * D * 2), 0u, "sc0 sc1");
    TSW(11, 4);
    if (w == 4) TS_DUMP;
  }
}

extern "C" void kernel_launch(void* const* d_in, const int* in_sizes, int n_in, void* d_out, int out_size, void* d_ws,
                              size_t ws_size, hipStream_t stream) {
  (void)hipFuncSetAttribute((const void*)fused, hipFuncAttributeMaxDynamicSharedMemorySize, LDS_BYTES);
  fused<<<256, 512, LDS_BYTES, stream>>>((const u16*)d_in[0], (const u16*)d_in[1], (const u16*)d_in[4], (char*)d_ws, (const u16*)d_in[2],
                                         (const u16*)d_in[5], (const u16*)d_in[6], (const u16*)d_in[7], (u16*)d_out);
}
```

```cpp
#include <hip/hip_runtime.h>
#include <hip/hip_bf16.h>
#include <stdint.h>
#pragma clang diagnostic ignored "-Wint-to-pointer-cast"

#define HMOD "sc1"
#define PRIO_H 3
#define PRIO_C 2
#define LSLEEP 1
#define STW 99
#define PF0 2
#define PFN 0
#define FILLAT 8
#define THR_N 0
#define THR_V 4
#define NHW 7
#define TSLEEP 4
#define TOUCH 0
#define S0MODE 1
#define FS 128u
#define POLL_LATE 0
#define TCH2 5
#define PRIO_S 1
#define RSLEEP 4
#define NX 9
#define PFA 9
#define PRIO_P 0
#define PRIO_W 0
#define EARLY 0
#define CTROT 1
#define ROTM 1
#define LA 2
#define NOKV 0
#define PRIO_M 0
#define PRIO_K 3
#define PFE 4
#define PD 0
#define PMASK 2
#define PD2 0
#define PMASK2 12
#define LNOP 4
#define WD 0
#define LFROM 0
#define LTO 4
#define LNA 4
#define LTA 4
#define KNA 0
typedef unsigned short u16;
typedef unsigned long long u64;
typedef __bf16 bf16x8 __attribute__((ext_vector_type(8)));
typedef __bf16 bf16x2 __attribute__((ext_vector_type(2)));
typedef short s16x4 __attribute__((ext_vector_type(4)));
typedef float f32x4 __attribute__((ext_vector_type(4)));
typedef float f32x16 __attribute__((ext_vector_type(16)));
typedef float f32x2 __attribute__((ext_vector_type(2)));
typedef unsigned u32x4 __attribute__((ext_vector_type(4)));
typedef unsigned u32x2 __attribute__((ext_vector_type(2)));
#define LDS __attribute__((address_space(3)))
#define DI __device__ __forceinline__

constexpr int S = 2048, D = 448, F = 1792;
constexpr unsigned WS_EP = 0;
constexpr unsigned WS_KF = 1024;
constexpr unsigned WS_FL = WS_KF + 256 * FS;
constexpr unsigned WS_ST = WS_FL + 1024 * FS;
constexpr unsigned WS_CS = WS_ST + 32768;
constexpr unsigned WS_KV = WS_CS + 16 * 7 * 2048;
constexpr unsigned WS_KVP = WS_KV + 32768;
constexpr unsigned WS_PQ = WS_KVP + 16 * 16 * 4096;
constexpr unsigned WS_H = WS_PQ + 64 * 28 * 1024;
constexpr unsigned WS_END = WS_H + 64 * 112 * 1024;
constexpr unsigned L_CS = 155648, L_ST = L_CS + 2048, L_RS = L_ST + 512, L_SY = L_RS + 512, L_END = L_SY + 256;
constexpr unsigned SY_FULL = L_SY, SY_AUX = L_SY + 16, SY_DONE = L_SY + 32, SY_RDY = L_SY + 48, SY_FULL2 = L_SY + 64, SY_DONE2 = L_SY + 80,
                   SY_GO = L_SY + 112, SY_WOK = L_SY + 128, SY_KOK = L_SY + 96, SY_HOK = L_SY + 100, SY_TAG = L_SY + 104;
constexpr unsigned CODE_BYTES = 33024;
constexpr unsigned SPIN_CAP = 1u << 17;

#define LDS_BYTES L_END
#define TSW(n, wv)
#define TSC(n, wv)
#define TS_TOP
#define TS_INIT
#define TS_DUMP

DI u32x4 mk_rsrc(const void* p, unsigned bytes) {
  uint64_t a = (uint64_t)p;
  u32x4 r; r.x = (unsigned)a; r.y = (unsigned)(a >> 32) & 0xffffu; r.z = bytes; r.w = 0x00020000u; return r;
}
DI void dma16(unsigned lds_addr, unsigned voff, u32x4 rsrc, unsigned soff) {
  asm volatile("s_mov_b32 m0, %0\n\ts_nop 0\n\tbuffer_load_dwordx4 %1, %2, %3 offen lds"
               :: "s"(lds_addr), "v"(voff), "s"(rsrc), "s"(soff) : "memory");
}
template <unsigned OFF> DI void dma16o(unsigned lds_base, unsigned voff, u32x4 rsrc, unsigned soff) {
  asm volatile("s_add_u32 m0, %0, %4\n\ts_nop 0\n\tbuffer_load_dwordx4 %1, %2, %3 offen lds"
               :: "s"(lds_base), "v"(voff), "s"(rsrc), "s"(soff), "n"(OFF) : "memory", "scc");
}
#define SB __builtin_amdgcn_sched_barrier(0)
#define FENCE asm volatile("" ::: "memory")
#define PIN(a) asm volatile("" : "+v"(a))
#define WAIT_VM(n) asm volatile("s_waitcnt vmcnt(%0)" :: "n"(n) : "memory")
#define WAIT_LDS asm volatile("s_waitcnt lgkmcnt(0)" ::: "memory")
#define BARRIER asm volatile("s_waitcnt lgkmcnt(0)\n\ts_barrier" ::: "memory")
#define LD4(dst, voff, rsrc, soff, mod) asm volatile("buffer_load_dwordx4 %0, %1, %2, %3 offen " mod : "=v"(dst) : "v"(voff), "s"(rsrc), "s"(soff) : "memory")
#define LD2(dst, voff, rsrc, soff, mod) asm volatile("buffer_load_dwordx2 %0, %1, %2, %3 offen " mod : "=v"(dst) : "v"(voff), "s"(rsrc), "s"(soff) : "memory")
#define LD1(dst, voff, rsrc, soff, mod) asm volatile("buffer_load_dword %0, %1, %2, %3 offen " mod : "=v"(dst) : "v"(voff), "s"(rsrc), "s"(soff) : "memory")
#define ST4(src, voff, rsrc, soff, mod) asm volatile("buffer_store_dwordx4 %0, %1, %2, %3 offen " mod "\n\ts_nop 1" :: "v"(src), "v"(voff), "s"(rsrc), "s"(soff) : "memory")
#define ST2(src, voff, rsrc, soff, mod) asm volatile("buffer_store_dwordx2 %0, %1, %2, %3 offen " mod :: "v"(src), "v"(voff), "s"(rsrc), "s"(soff) : "memory")
DI f32x4 mfma16(bf16x8 a, bf16x8 b, f32x4 c) { return __builtin_amdgcn_mfma_f32_16x16x32_bf16(a, b, c, 0, 0, 0); }
DI f32x16 mfma(bf16x8 a, bf16x8 b, f32x16 c) { return __builtin_amdgcn_mfma_f32_32x32x16_bf16(a, b, c, 0, 0, 0); }
DI bf16x8 asbf(u32x4 v) { return __builtin_bit_cast(bf16x8, v); }
DI bf16x8 ld_frag(unsigned a) { return *(LDS const bf16x8*)a; }
DI bf16x8 tr_frag(unsigned a0, unsigned a1) {
  s16x4 lo = __builtin_amdgcn_ds_read_tr16_b64_v4i16((LDS s16x4*)a0);
  s16x4 hi = __builtin_amdgcn_ds_read_tr16_b64_v4i16((LDS s16x4*)a1);
  return __builtin_bit_cast(bf16x8, __builtin_shufflevector(lo, hi, 0, 1, 2, 3, 4, 5, 6, 7));
}
DI unsigned pk2(float a, float b) { f32x2 v = {a, b}; return __builtin_bit_cast(unsigned, __builtin_convertvector(v, bf16x2)); }
DI float bflo(unsigned u) { return __uint_as_float(u << 16); }
DI float bfhi(unsigned u) { return __uint_as_float(u & 0xffff0000u); }
DI float rbf(float f) { return bflo(pk2(f, 0.f)); }
DI f32x2 mul_clamp(f32x2 a, f32x2 b) { f32x2 r; asm("v_pk_mul_f32 %0, %1, %2 clamp" : "=v"(r) : "v"(a), "v"(b)); return r; }
constexpr float DN = 0x1p-40f, UP = 0x1p40f, LOG2E = 1.44269504f;
#define PHI_A(u) f32x2 pm[4], pe[4]; _Pragma("unroll") for (int j_ = 0; j_ < 4; ++j_) pm[j_] = mul_clamp(u[j_], (f32x2){DN, DN}); SB
#define PHI_E(j0) _Pragma("unroll") for (int j_ = (j0); j_ < (j0) + 2; ++j_) pe[j_] = (f32x2){__builtin_amdgcn_fmed3f(__builtin_amdgcn_exp2f(u[j_].x), 0.f, 1.f), \
                                                                                     __builtin_amdgcn_fmed3f(__builtin_amdgcn_exp2f(u[j_].y), 0.f, 1.f)}; SB
#define PHI_S _Pragma("unroll") for (int j_ = 0; j_ < 4; ++j_) pm[j_] = __builtin_elementwise_fma(pm[j_], (f32x2){UP / LOG2E, UP / LOG2E}, (f32x2){1e-6f, 1e-6f}); SB; \
              _Pragma("unroll") for (int j_ = 0; j_ < 4; ++j_) pm[j_] += pe[j_]; SB
#define PHI_B(o) PHI_S; const u32x4 o = {pk2(pm[0].x, pm[0].y), pk2(pm[1].x, pm[1].y), pk2(pm[2].x, pm[2].y), pk2(pm[3].x, pm[3].y)}
DI unsigned relu2(unsigned v) {
  typedef short s16x2 __attribute__((ext_vector_type(2)));
  return __builtin_bit_cast(unsigned, __builtin_elementwise_max(__builtin_bit_cast(s16x2, v), (s16x2){0, 0}));
}
DI float mul1(float a, float b) { float r; asm("v_mul_f32_e32 %0, %1, %2" : "=v"(r) : "v"(a), "v"(b)); return r; }
DI float sum8(float v) {
  v += __uint_as_float(__builtin_amdgcn_update_dpp(0u, __float_as_uint(v), 0xB1, 0xf, 0xf, true));
  v += __uint_as_float(__builtin_amdgcn_update_dpp(0u, __float_as_uint(v), 0x4E, 0xf, 0xf, true));
  v += __uint_as_float(__builtin_amdgcn_update_dpp(0u, __float_as_uint(v), 0x141, 0xf, 0xf, true));
  return v;
}
DI void lds_put(unsigned a, unsigned v) { FENCE; *(volatile LDS unsigned*)a = v; FENCE; }
DI void lds_wait(unsigned a, int n, unsigned need, int lane) {
  const unsigned ad = a + (lane & (n - 1)) * 4;
#pragma unroll 1
  for (unsigned sp = 0; sp < SPIN_CAP; ++sp) {
    FENCE; const unsigned v = *(volatile LDS unsigned*)ad; FENCE;
    if (__all(v >= need)) break;
    __builtin_amdgcn_s_sleep(LSLEEP);
  }
}
DI u64 tag_wait(unsigned voff, u32x4 rs, unsigned soff, u64 mask, u32x2 tag) {
  u64 bl = 0;
#pragma unroll 1
  for (unsigned sp = 0; sp < SPIN_CAP; ++sp) {
    u32x2 v;
    asm volatile("buffer_load_dwordx2 %0, %1, %2, %3 offen sc1\n\ts_waitcnt vmcnt(0)" : "=&v"(v) : "v"(voff), "s"(rs), "s"(soff) : "memory");
    bl = __ballot(v.x == tag.x && v.y == tag.y);
    if ((bl & mask) == mask) break;
    __builtin_amdgcn_s_sleep(TSLEEP);
  }
  return bl;
}

__global__ __launch_bounds__(512, 2) void fused(const u16* __restrict__ x, const u16* __restrict__ wqkv, const u16* __restrict__ w1, char* ws,
                                                const u16* __restrict__ wo, const u16* __restrict__ b1, const u16* __restrict__ w2,
                                                const u16* __restrict__ b2, u16* __restrict__ out) {
  TS_TOP;
  const int bid = blockIdx.x, tid = threadIdx.x, lane = tid & 63;
  const int w = __builtin_amdgcn_readfirstlane(tid >> 6);
  const int xc = bid & 7, ix = bid >> 3, sl = ix & 7, rl = ix >> 3;
  const int r = (xc >> 1) * 4 + rl, s0 = r >= 14 ? 0 : r >> 1;
  const u32x4 rx = mk_rsrc(x, S * D * 2);
  const unsigned vx0 = (unsigned)(r * 128 + (w & 1) * 64 + (lane >> 3)) * 896u + (unsigned)(((lane & 7) ^ (lane >> 4)) << 4), vx1 = (vx0 ^ 64u) + 7168u;
#define XGROUP(n) do { dma16o<(n) * 2048>(lx, vx0, rx, sg * 128 + (n) * 14336); dma16o<(n) * 2048 + 1024>(lx, vx1, rx, sg * 128 + (n) * 14336); } while (0)
  unsigned cpf;
  u64 pc; asm volatile("s_getpc_b64 %0" : "=s"(pc));
  if (w < 2) { const unsigned lx = w * 8192; const int sg = s0; XGROUP(0); XGROUP(1); XGROUP(2); XGROUP(3); }
  if (w == 4) LD1(cpf, lane < 9 ? (unsigned)(ix + lane * 32) * 128u : 0x80000000u, mk_rsrc((const void*)(pc & ~255ull), CODE_BYTES), 0u, "");
  asm volatile("" :: "s"(wqkv), "s"(w1), "s"(ws));
  const int c = (int)(((xc & 1) ? 0xEFB98765u : 0xDCA43210u) >> (4 * sl)) & 15;
  const bool kvt = sl >= 6, pure = sl == 7;
  const int rt = xc * 4 + rl, ct = sl + 2 * rl - (sl + 2 * rl >= 7 ? 7 : 0), rot2 = 7 * rl + xc - (7 * rl + xc >= 28 ? 28 : 0);
  const int l31 = lane & 31, hh = lane >> 5, q = (lane >> 2) & 3, p = lane & 3, blk = (lane >> 4) & 1;
  const unsigned slot_b = kvt ? 49152u : 40960u;
  const u32x4 rws = mk_rsrc(ws, WS_END);
  const unsigned v16 = lane * 16, v8 = lane * 8;
  TS_INIT;
  u32x2 tag;
  const unsigned swz = (unsigned)(((lane & 7) ^ ((lane >> 2) & 4)) << 4);
  f32x16 z16;
#pragma unroll
  for (int e = 0; e < 16; ++e) z16[e] = 0.f;

  u32x4 xr, br; unsigned sg_;
  const unsigned fxo = (unsigned)(rt * 64 + (tid >> 3)) * 896u + ct * 128 + (tid & 7) * 16;
#define FIN_LD do { LD4(xr, fxo, rx, 0u, ""); LD4(br, (tid & 7) * 16u, mk_rsrc(b2, D * 2), ct * 128u, ""); LD1(sg_, (unsigned)(tid & ~7) * 2u, rws, WS_ST + rt * 1024u, "sc1"); } while (0)
#define PUT_TILES(base, t) do { _Pragma("unroll") for (int n_ = 0; n_ < 8; ++n_) *(LDS f32x4*)((base) + n_ * 1024 + (v16 ^ (n_ * 32))) = \
      (f32x4){t[n_ >> 2][4 * (n_ & 3)], t[n_ >> 2][4 * (n_ & 3) + 1], t[n_ >> 2][4 * (n_ & 3) + 2], t[n_ >> 2][4 * (n_ & 3) + 3]}; } while (0)
  if (__builtin_expect(w < 4, 1)) {
    const int h = w, hp = h >> 1, hs_ = h & 1;
    const u32x4 rw2 = mk_rsrc(w2, F * D * 2), rwo = mk_rsrc(wo, D * D * 2);
    const unsigned vwo = (unsigned)(lane >> 3) * 896u + ct * 128 + swz;
    const unsigned vw2 = vwo + (unsigned)((h >> 1) * 896 + (h & 1) * 16) * 896u;
    const int hg = (h + xc + rl) & 3;
    u32x4 R[18];
#define ROT(u) ((u) + rot2 >= 28 ? (u) + rot2 - 28 : (u) + rot2)
#define PFLD(n) LD4(R[n], vw2, rw2, ROT(19 + ((n) >> 1)) * 28672u + ((n) & 1) * 7168u, "")
    auto fill = [&](int u, int g, int cnt) {
#pragma unroll 1
      for (int e = u + cnt; u < e; ++u, ++g) {
        const unsigned k = ROT(u) * 28672u;
        dma16(g * 8192 + h * 2048, vw2, rw2, k); dma16(g * 8192 + h * 2048 + 1024, vw2, rw2, k + 7168u);
      }
    };
    {
      __builtin_amdgcn_s_setprio(PRIO_H);
      const bool a1 = c < 10, b1_ = c < 9;
      const u32x4 r0 = mk_rsrc(a1 ? w1 : wqkv, a1 ? D * F * 2 : D * 3 * D * 2), r1 = mk_rsrc(b1_ ? w1 : wqkv, b1_ ? D * F * 2 : D * 3 * D * 2);
      const unsigned p0 = a1 ? 3584u : 2688u, p1 = b1_ ? 3584u : 2688u;
      unsigned V0, V1, V2, V3;
      {
        const unsigned kl = 32 * hs_ + (lane >> 3);
        const unsigned c0 = (a1 ? c : c - 10) * 384, c1 = c == 9 ? 512 : c0;
        const unsigned vk = (32 * hs_ + (lane >> 4)) * 2688 + ((224 * c - 1792) & ~63) + ((q ^ (lane >> 4)) << 6) + p * 16;
        V0 = kvt ? vk : kl * p0 + c0 + swz;
        V1 = kvt ? vk + 4 * 2688 : kl * p1 + c1 + swz + 128;
        V2 = kvt ? V0 + 896 : V1 + 128;
        V3 = V1 + 896;
      }
      int sg = (S0MODE ? s0 : 0) + hp; sg = sg == 7 ? 0 : sg;
      unsigned so = hp * slot_b;
#define GROUP(n) do { const unsigned kk = sg * 64 + 8 * (n), sa = kk * p0, sb_ = kk * p1; \
          if (i || hp) XGROUP(n); \
          dma16o<16384 + (n) * 1024>(lw, V0, r0, sa); \
          dma16o<24576 + (n) * 1024>(lw, V1, r1, sb_); \
          dma16o<32768 + (n) * 1024>(lw, V2, r1, sb_); \
          if (kvt) dma16o<40960 + (n) * 1024>(lw, V3, r1, sb_); } while (0)
      if (hp) BARRIER;
      unsigned t2;
#pragma unroll
      for (int i = 0; i < 4; ++i) {
        const int t = hp + 2 * i;
        if (i == 3 && hp) break;
        if (i >= 2 || (i == 1 && hp)) lds_wait(SY_DONE, 4, t - 2, lane);
        TSW(32 + t, 2 * hp);
        const unsigned lx = so + hs_ * 8192, lw = so + hs_ * 4096;
        GROUP(0); GROUP(1); GROUP(2); GROUP(3);
        TSW(40 + t, 2 * hp);
        if (i == 0 && !hp) BARRIER;
        if (i == 2 && hp) break;
        if (i == 3) lds_put(SY_GO, 1);
        if (i == 1) {
          LD1(t2, lane < (hp ? 0 : hs_ ? 13 : 49) ? lane * 128u : 0x80000000u, hs_ ? rwo : rw2, bid * (hs_ ? 1664u : 6272u), "");
          WAIT_VM(1);
        } else WAIT_VM(0);
        if (i == 2) asm volatile("" :: "v"(t2));
        lds_put(SY_FULL + h * 4, t + 2);
        TSW(48 + t, 2 * hp);
        if (!pure && ((i == 1 && hp) || i == 2)) {
#pragma unroll
          for (int n = 0; n < (i == 2 ? PFE : PFA); ++n) PFLD(n);
        }
        so = so == 0 ? 2 * slot_b : so - slot_b;
        sg = sg >= 5 ? sg - 5 : sg + 2;
      }
      tag = *(volatile LDS u32x2*)SY_TAG;
      if (hp) {
        u32x4 xf[8];
        unsigned bi;
        const bool chk = !hs_ || kvt || lane < 32;
        const u32x4 rb1 = mk_rsrc(b1, c < 10 ? F * 2 : 0);
#pragma unroll 1
        for (unsigned sp = 0; sp < SPIN_CAP; ++sp) {
#pragma unroll
          for (int k = 0; k < 7; ++k) LD4(xf[k], v16, rws, WS_CS + c * 14336u + hs_ * 1024u + k * 2048u, "sc1");
          LD4(xf[7], v16, rws, WS_ST + r * 2048u + hs_ * 1024u, "sc1");
          LD1(bi, lane * 4u, rb1, c * 384u + hs_ * 256u, "");
          WAIT_VM(9); lds_put(SY_FULL + h * 4, 7);
          TSW(53, 2);
          WAIT_VM(0);
          PIN(xf[0]); PIN(xf[7]); PIN(bi);
          if (kvt && l31 == 30) bi = 0x3f80u;
          unsigned bad = (xf[0].y ^ tag.x) | (xf[0].w ^ tag.x);
          float ax_ = __uint_as_float(xf[0].x), ay_ = __uint_as_float(xf[0].z);
#pragma unroll
          for (int k = 1; k < 7; ++k) {
            PIN(xf[k]);
            ax_ += __uint_as_float(xf[k].x); ay_ += __uint_as_float(xf[k].z); PIN(ax_); PIN(ay_);
            bad |= (xf[k].y ^ tag.x) | (xf[k].w ^ tag.x); PIN(bad);
          }
          const unsigned hi = pk2(ax_, ay_); ax_ -= bflo(hi); PIN(ax_); ay_ -= bfhi(hi);
          const unsigned mi = pk2(ax_, ay_); ax_ -= bflo(mi); PIN(ax_); ay_ -= bfhi(mi);
          const unsigned lo = pk2(ax_, ay_);
          *(LDS u32x4*)(L_CS + hs_ * 1024 + v16) = (u32x4){(hi & 0xffffu) | (mi << 16), (lo & 0xffffu) | (bi << 16),
                                                           (hi >> 16) | (mi & 0xffff0000u), (lo >> 16) | (bi & 0xffff0000u)};
          *(LDS unsigned*)(L_ST + hs_ * 256 + lane * 4) = xf[7].x;
          *(LDS float*)(L_RS + hs_ * 256 + lane * 4) = __builtin_amdgcn_rcpf(bfhi(xf[7].x)) * LOG2E;
          if (__builtin_expect(__all((bad == 0 || !chk) && xf[7].y == tag.x), 1)) break;
          __builtin_amdgcn_s_sleep(8);
        }
        lds_put(SY_FULL + h * 4, 8);
        TSW(3, 2);
        if (!pure) {
#pragma unroll
          for (int n = PFA; n < 18; ++n) PFLD(n);
        }
        lds_wait(SY_GO, 1, 1, lane);
        asm volatile("" :: "v"(t2));
      }
      __builtin_amdgcn_s_setprio(PRIO_P);
    }
    if (!pure) {
#pragma unroll
    for (int n = PFE; n < 18; ++n) { if (h < 2) PFLD(n); }
    const unsigned wb = 49152 + (h ^ 2) * 14336;
    lds_wait(SY_DONE, 4, h < 2 ? 6 : 5, lane);
#pragma unroll 1
    for (int n = 0; n < 14; ++n) dma16(wb + n * 1024, vwo, rwo, hg * 100352u + n * 7168u);
    lds_wait(SY_DONE, 4, 6, lane);
    fill(0, 16, 3);
    TSW(23, 0);
    WAIT_VM(6);
    bf16x8 WO[4][2][2];
    {
      const unsigned a0 = wb + (4 * hh + q) * 128 + (((q >> 1) & 1) << 6) + blk * 32 + p * 8, a1_ = a0 ^ 64u;
#pragma unroll
      for (int n = 0; n < 4; ++n)
#pragma unroll
        for (int sg = 0; sg < 2; ++sg) {
          const unsigned o = n * 3584 + sg * 2048;
          WO[n][sg][0] = tr_frag(a0 + o, a0 + o + 1024); WO[n][sg][1] = tr_frag(a1_ + o, a1_ + o + 1024);
        }
    }
#pragma unroll
    for (int n = 0; n < 8; ++n) {
      u32x4 t = __builtin_bit_cast(u32x4, WO[n >> 1][1][n & 1]);
      if (hh) { t.z = 0; t.w = 0; }
      WO[n >> 1][1][n & 1] = asbf(t);
    }
#pragma unroll
    for (int n = 0; n < 16; ++n) PIN(WO[n >> 2][(n >> 1) & 1][n & 1]);
    lds_put(SY_WOK + h * 4, 1);
    TSW(29, 0);
    if (EARLY) fill(16, 13, 3);
    lds_wait(SY_DONE, 4, c < 10 ? FILLAT : 7, lane);
    fill(3, 0, 6);
    lds_wait(SY_WOK, 4, 1, lane);
    fill(9, 6, 3);
    WAIT_VM(6); lds_put(SY_FULL2 + h * 4, 1);
    TSW(31, 0);
    fill(12, 9, EARLY ? 4 : 7);
    TSW(20, 0);
    lds_wait(SY_DONE, 4, 9, lane);
#pragma unroll
    for (int n = 0; n < 18; ++n) { PIN(R[n]); *(LDS u32x4*)((n < 6 ? 131072 : -24576) + h * 2048 + v16 + (n >> 1) * 8192 + (n & 1) * 1024) = R[n]; }
    WAIT_VM(0); lds_put(SY_RDY + h * 4, 1);
    TSW(4, 0);
    u32x4 PQ[2][7], KV[8];
    __builtin_amdgcn_s_setprio(PRIO_S);
    lds_wait(SY_HOK, 1, 1, lane);
    if (*(volatile LDS unsigned*)SY_HOK < 2u) tag_wait(min(lane, 4) * FS, rws, WS_FL + (rt * 32 + 19) * FS, ~0ull, tag);
#pragma unroll
    for (int n = 0; n < 14; ++n) LD4(PQ[n / 7][n % 7], v16, rws, WS_PQ + ((rt * 2 + n / 7) * 28 + hg * 7 + n % 7) * 1024u, "sc1");
    FIN_LD;
    TSW(19, 0);
    __builtin_amdgcn_s_setprio(PRIO_K);
    tag_wait(min(lane, 7) * FS, rws, WS_KF + (xc * 32 + hg * 8) * FS, ~0ull, tag);
#pragma unroll
    for (int n = 0; n < 8; ++n) LD4(KV[n], v16, rws, WS_KV + hg * 8192u + n * 1024u, "sc1");
    __builtin_amdgcn_s_setprio(PRIO_M);
    TSW(5, 0);
    u32x4 PB[2][4];
#pragma unroll
    for (int n = 0; n < 14; ++n) PIN(PQ[n / 7][n % 7]);
#pragma unroll
    for (int u = 0; u < 8; ++u) {
      const u32x4* P = PQ[u >> 2]; const int n = u & 3, lo = n < 3 ? 2 * n + 1 : 5, hi = n ? 2 * n - 1 : 1;
      PB[u >> 2][n] = (u32x4){P[lo].x, P[lo].y, P[hi].z, P[hi].w}; PIN(PB[u >> 2][n]);
    }
    WAIT_VM(4);
    TSW(16, 0);
#pragma unroll
    for (int n = 0; n < 4; ++n) PIN(KV[n]);
    f32x16 nm[3], as[2][2] = {{z16, z16}, {z16, z16}};
    u32x4 O[2][2]; float rd[2];
#define NUM0(u) nm[(u) % 3] = mfma(asbf(KV[2 * ((u) & 3)]), asbf(PQ[(u) >> 2][2 * ((u) & 3)]), z16)
#define NUM1(u) nm[(u) % 3] = mfma(asbf(KV[2 * ((u) & 3) + 1]), asbf(PB[(u) >> 2][(u) & 3]), nm[(u) % 3])
#define DEN(u) __uint_as_float(__builtin_amdgcn_permlane32_swap(__float_as_uint(nm[(u) % 3][12]), __float_as_uint(nm[(u) % 3][12]), false, false)[1])
#define APP(u, i) as[(u) >> 2][(i) & 1] = mfma(WO[(u) & 3][(i) >> 1][(i) & 1], asbf(O[(u) & 1][(i) >> 1]), as[(u) >> 2][(i) & 1])
#define MULS(a, b) _Pragma("unroll") for (int e = (a); e < (b); ++e) o[e] = mul1(nm[u % 3][e], rd[u & 1])
#define CVTS(a, b) _Pragma("unroll") for (int e = (a); e < (b); ++e) O[u & 1][e >> 2][e & 3] = pk2(o[2 * e], o[2 * e + 1])
    NUM0(0); NUM1(0); NUM0(1); NUM1(1);
    rd[0] = __builtin_amdgcn_rcpf(DEN(0) + 1e-6f);
    asm volatile("s_waitcnt vmcnt(0)" : "+v"(rd[0]), "+v"(KV[4]), "+v"(KV[5]), "+v"(KV[6]), "+v"(KV[7]) :: "memory");
    SB;
    unsigned d2 = 0; bool put0 = false;
#pragma unroll
    for (int u = 0; u < 8; ++u) {
      float o[16], dn;
      if (u) APP(u - 1, 0); SB; MULS(0, 6); SB;
      if (u) APP(u - 1, 1); SB; MULS(6, 10); CVTS(0, 2); SB;
      if (u) APP(u - 1, 2); SB; MULS(10, 13); CVTS(2, 4); SB;
      if (u) APP(u - 1, 3); SB; MULS(13, 16); CVTS(4, 6); SB;
      if (u < 6) NUM0(u + 2); SB; if (u < 7) dn = DEN(u + 1) + 1e-6f; SB;
      if (u < 6) NUM1(u + 2); SB; if (u < 7) rd[(u + 1) & 1] = __builtin_amdgcn_rcpf(dn); SB; CVTS(6, 8);
      if (u == 4) d2 = *(volatile LDS unsigned*)(SY_DONE2 + p * 4);
      if (u == 5) { put0 = __all(d2 >= 1u); if (put0) PUT_TILES(49152 + h * 16384, as[0]); }
      SB;
    }
    APP(7, 0); APP(7, 2); APP(7, 1); APP(7, 3);
    TSW(21, 0);
    if (!put0) { lds_wait(SY_DONE2, 4, 1, lane); PUT_TILES(49152 + h * 16384, as[0]); }
    PUT_TILES(57344 + h * 16384, as[1]);
    TSW(17, 0);
    }
  } else {
  const int cw = w - 4, wl = cw & 1, wh = cw >> 1;
  u32x4 xs[7];
  if (cw == 3) {
    const unsigned va = (unsigned)(r * 128 + c * 8 + (lane >> 3)) * 896u + (lane & 7) * 16;
#pragma unroll
    for (int k = 0; k < 7; ++k) LD4(xs[k], va, rx, k * 128u, "");
  }
  *(LDS unsigned*)(L_SY + lane * 4) = (lane >> 1) == 1; BARRIER;
  __builtin_amdgcn_s_setprio(PRIO_C);
  bf16x8 ones = asbf((u32x4){0x3f803f80u, 0x3f803f80u, 0x3f803f80u, 0x3f803f80u});
  PIN(ones);
  {
    {
      u64 ep;
      asm volatile("s_load_dwordx2 %0, %1, 0x0\n\ts_waitcnt lgkmcnt(0) vmcnt(0)" : "=&s"(ep) : "s"(ws) : "memory");
      tag.x = (unsigned)ep + 1u; tag.y = (unsigned)(ep >> 32) + 1u;
    }
    if (cw == 0) { asm volatile("" :: "v"(cpf)); *(volatile LDS u32x2*)SY_TAG = tag; }
    if (cw == 3) {
      float sq[2] = {0.f, 0.f}, sm[2] = {0.f, 0.f};
      const bf16x2 one2 = __builtin_bit_cast(bf16x2, 0x3f803f80u);
#pragma unroll
      for (int k = 0; k < 7; ++k) {
        PIN(xs[k]);
#pragma unroll
        for (int e = 0; e < 4; ++e) {
          const unsigned u = xs[k][e];
          const bf16x2 v = __builtin_bit_cast(bf16x2, u);
          sq[e & 1] = __builtin_amdgcn_fdot2_f32_bf16(v, v, sq[e & 1], false); sm[e & 1] = __builtin_amdgcn_fdot2_f32_bf16(v, one2, sm[e & 1], false);
        }
      }
      const float m = sum8(sm[0] + sm[1]) * (1.f / D);
      const float var = fmaxf(sum8(sq[0] + sq[1]) * (1.f / D) - m * m, 0.f);
      const unsigned sv = __builtin_amdgcn_ds_bpermute(lane * 32, pk2(-m, __builtin_amdgcn_sqrtf(rbf(rbf(var) + 1e-5f))));
      const u32x4 gr = {sv, tag.x, sv, tag.x};
      if (lane < 8) ST4(gr, v16, rws, WS_ST + (r * 128 + c * 8) * 16, "sc1");
    }
  }
  const unsigned fl_off = WS_FL + ((r * 2 + wl) * 32 + 2 * c + wh) * FS;
  const unsigned fa = SY_FULL + p * 4;
#define CHECK_FULL(v, need) do { if (__builtin_expect(__any((v) < (need)), 0)) lds_wait(SY_FULL, 4, need, lane); FENCE; } while (0)
#define RAMP do { if (WD && (sl & 1)) __builtin_amdgcn_s_sleep(WD); if (PD && ((PMASK >> cw) & 1)) __builtin_amdgcn_s_sleep(PD); if (PD2 && ((PMASK2 >> cw) & 1)) __builtin_amdgcn_s_sleep(PD2); } while (0)
#define COLSUM(a_, t_, tile) do { const unsigned a = (a_); bf16x8 f[4]; \
    _Pragma("unroll") for (int ks = 0; ks < 4; ++ks) f[ks] = tr_frag(a + ks * 2048, a + ks * 2048 + (t_)); \
    SB; f32x16 cs = z16; \
    _Pragma("unroll") for (int ks = 0; ks < 4; ++ks) cs = mfma(ones, f[ks], cs); \
    const u32x4 gr = {(unsigned)__builtin_amdgcn_ds_bpermute(lane * 8, __float_as_int(cs[0])), tag.x, (unsigned)__builtin_amdgcn_ds_bpermute(lane * 8 + 4, __float_as_int(cs[0])), tag.x}; \
    if (lane < 16) ST4(gr, v16, rws, WS_CS + (c * 7 + s0) * 2048 + (tile) * 256, "sc1"); } while (0)
  unsigned da = SY_DONE + cw * 4;
  PIN(da);
  const unsigned xm = hh ? 0u : ~0u;
  if (!kvt) {
    const unsigned xa = (wl * 64 + l31) * 128 + ((hh ^ ((l31 >> 1) & 7)) << 4);
    const unsigned wr = 16384 + (8 * hh + q) * 128 + blk * 32 + p * 8;
    const unsigned wa0 = wr + wh * 8192 + ((wh ^ (q >> 1)) << 6), wa1 = wr + wh * 16384 + ((wh ^ 1 ^ (q >> 1)) << 6);
    f32x16 acc[2][3] = {{z16, z16, z16}, {z16, z16, z16}};
    bf16x8 X[4][2], W[4][3];
#define RDA(n, ks) do { \
      if ((n) == 0 || (n) == 2) X[ks][(n) >> 1] = ld_frag(ax[ks] + ((n) >> 1) * 4096); \
      else { const unsigned a_ = ((n) == 3 ? a1_ : a0_) + ((n) == 4 ? 8192 : 0) + (ks) * 2048; W[ks][(n) == 1 ? 0 : (n) - 2] = tr_frag(a_, a_ + 512); } } while (0)
#define MMA(ks, m) acc[(m) & 1][(m) >> 1] = mfma(W[ks][(m) >> 1], X[ks][(m) & 1], acc[(m) & 1][(m) >> 1])
    unsigned ax[4] = {xa, xa ^ 32u, xa ^ 64u, xa ^ 96u}, a0_ = wa0, a1_ = wa1;
#pragma unroll
    for (int n = 0; n < 6; ++n) PIN(acc[n & 1][n >> 1]);
    lds_wait(SY_FULL, 4, 1, lane);
    RAMP;
    TSW(6, 4); TSC(24, 4);
    if (r < 14 && !((r & 1) && wl)) {
      const int b = (r & 1) ? 2 : wl;
      COLSUM(b == 1 ? wa1 : wa0 + b * 4096, 512, 3 * wh + b);
    }
#pragma unroll
    for (int n = 0; n < 10; ++n) { RDA(n % 5, n / 5); SB; }
    unsigned tv = 1; int ts = 0;
    PIN(tv);
#pragma unroll 1
    for (;;) {
      unsigned fv; int dl;
#pragma unroll
      for (int m = 0; m < 12; ++m) {
        MMA(m / 6, m % 6); SB;
        if (m % 6 < 5) RDA(m % 6, 2 + m / 6);
        if (m == 0) { dl = ((0x24 >> ts) & 1) ? -81920 : 40960; ++ts; asm volatile("" : "+s"(dl), "+s"(ts)); }
        if (m == 2) { ax[0] += dl; ax[1] += dl; PIN(ax[0]); PIN(ax[1]); }
        if (m == 5) { if (LNA && ts <= LTA) __builtin_amdgcn_s_sleep(LNA); fv = *(volatile LDS unsigned*)fa; }
        if (m == 10) *(volatile LDS unsigned*)da = tv;
        if (m == 11) { a0_ += dl; a1_ += dl; ++tv; CHECK_FULL(fv, tv); TSW(55 + ts, STW); }
        SB;
      }
      if (__builtin_expect(ts == 7, 0)) break;
#pragma unroll
      for (int m = 0; m < 12; ++m) {
        MMA(2 + m / 6, m % 6); SB;
        if (m % 6 < 5) RDA(m % 6, m / 6);
        if (m == 5) { ax[2] += dl; ax[3] += dl; if (LNOP && ts >= LFROM && ts <= LTO) __builtin_amdgcn_s_sleep(LNOP); }
        SB;
      }
    }
    TSW(7, 4); TSC(25, 4);
    unsigned st[2]; u32x2 cv[3];
#pragma unroll
    for (int a = 0; a < 2; ++a) st[a] = *(LDS const unsigned*)(L_ST + (wl * 64 + a * 32 + l31) * 4);
#pragma unroll
    for (int b = 0; b < 3; ++b) cv[b] = *(LDS const u32x2*)(L_CS + ((3 * wh + b) * 32 + l31) * 8);
    SB;
    MMA(2, 0); MMA(2, 1); MMA(3, 0); MMA(3, 1);
    SB;
    bf16x8 xe[2];
#pragma unroll
    for (int a = 0; a < 2; ++a) xe[a] = asbf((u32x4){((st[a] & 0xffffu) | (st[a] << 16)) & xm, st[a] & xm, 0u, 0u});
#define MME(m) acc[(m) & 1][(m) >> 1] = mfma(asbf((u32x4){cv[(m) >> 1].x, cv[(m) >> 1].y, cv[(m) >> 1].x, cv[(m) >> 1].y}), xe[(m) & 1], acc[(m) & 1][(m) >> 1])
    MME(0); MME(1);
    SB;
    TSW(18, 4);
    const int R0 = r * 4 + wl * 2;
    const unsigned sk = c == 9 ? 0x40000000u : 0u;
#define MM6(b, j) do { if ((j) < 4) MMA(2 + ((j) >> 1), 2 * (b) + ((j) & 1)); else MME(2 * (b) + ((j) & 1)); } while (0)
    if (2 * c + wh < 19) {
      const unsigned ob = WS_H + (R0 * 112 + 12 * c + 6 * wh) * 1024u;
#pragma unroll
      for (int b = 0; b < 3; ++b)
#pragma unroll
        for (int n = 0; n < 4; ++n) {
          const int a = n >> 1, e = 8 * (n & 1);
          float t[8]; u32x4 o;
          if (b < 2) MM6(b + 1, n < 2 ? 2 * n : n + 2);
          SB;
          if (n == 0) asm volatile("s_nop 10" : "+v"(acc[0][b]), "+v"(acc[1][b]));
          asm volatile("v_mul_f32_e64 %0, %8, |%8|\n\tv_mul_f32_e64 %1, %9, |%9|\n\tv_mul_f32_e64 %2, %10, |%10|\n\tv_mul_f32_e64 %3, %11, |%11|\n\t"
                       "v_mul_f32_e64 %4, %12, |%12|\n\tv_mul_f32_e64 %5, %13, |%13|\n\tv_mul_f32_e64 %6, %14, |%14|\n\tv_mul_f32_e64 %7, %15, |%15|"
                       : "=&v"(t[0]), "=&v"(t[1]), "=&v"(t[2]), "=&v"(t[3]), "=&v"(t[4]), "=&v"(t[5]), "=&v"(t[6]), "=&v"(t[7])
                       : "v"(acc[a][b][e]), "v"(acc[a][b][e + 1]), "v"(acc[a][b][e + 2]), "v"(acc[a][b][e + 3]), "v"(acc[a][b][e + 4]), "v"(acc[a][b][e + 5]),
                         "v"(acc[a][b][e + 6]), "v"(acc[a][b][e + 7]));
          SB;
          if (b < 2 && n < 2) MM6(b + 1, 2 * n + 1);
          SB;
#pragma unroll
          for (int j = 0; j < 4; ++j) o[j] = pk2(t[2 * j], t[2 * j + 1]);
#pragma unroll
          for (int j = 0; j < 4; ++j) o[j] = relu2(o[j]);
          ST4(o, v16, rws, ob + a * (112 * 1024u) + (2 * b + (n & 1)) * 1024u + (b == 2 ? sk : 0u), HMOD);
          SB;
        }
    } else {
#pragma unroll
      for (int j = 0; j < 12; ++j) MM6(1 + j / 6, j % 6);
      const unsigned ob = WS_PQ + (R0 * 28 + (c == 9 ? 22 : (c - 10) * 12 + wh * 6)) * 1024u;
      SB;
#pragma unroll
      for (int a = 0; a < 2; ++a) {
        const float rl = __builtin_amdgcn_rcpf(bfhi(st[a])) * LOG2E;
#pragma unroll
        for (int n = 0; n < 6; ++n) {
          f32x2 u[4];
#pragma unroll
          for (int j = 0; j < 4; ++j) u[j] = (f32x2){acc[a][n >> 1][8 * (n & 1) + 2 * j], acc[a][n >> 1][8 * (n & 1) + 2 * j + 1]} * rl;
          PHI_A(u); PHI_E(0); PHI_E(2); PHI_B(o);
          ST4(o, v16, rws, ob + a * (28 * 1024u) + n * 1024u + (n < 2 ? sk : 0u), HMOD);
          SB;
        }
      }
    }
  } else {
    const unsigned xa = l31 * 128 + ((hh ^ ((l31 >> 1) & 7)) << 4);
    const int ok_ = (c & 1) * 32 + 56 * cw + 8 * ((4 * blk + p + cw) & 7), ov_ = (c & 1) * 32 + 56 * cw + 32 * blk + 8 * p;
    const unsigned ka = 16384 + hh * 1024 + q * 256 + ((((ok_ >> 6) & 3) ^ q) << 6) + (ok_ & 63);
    const unsigned va = 32768 + hh * 1024 + q * 256 + ((((ov_ >> 6) & 3) ^ q) << 6) + (ov_ & 63) + ((lane & 19) == 19 ? 0x100000 : 0);
    f32x16 acc[4][2] = {{z16, z16}, {z16, z16}, {z16, z16}, {z16, z16}};
    bf16x8 X[4][4], W[4][2];
#define RDK(n, ks) do { \
      if ((n) == 1 || (n) == 5) { const unsigned a_ = ((n) == 1 ? a0_ : a1_) + (ks) * 2048; W[ks][(n) >> 2] = tr_frag(a_, a_ + 8192); } \
      else X[ks][(n) ? (n) - 1 : 0] = ld_frag(ax[ks] + ((n) ? (n) - 1 : 0) * 4096); } while (0)
#define MMK(ks, m) acc[(m) & 3][(m) >> 2] = mfma(X[ks][(m) & 3], W[ks][(m) >> 2], acc[(m) & 3][(m) >> 2])
    unsigned ax[4] = {xa, xa ^ 32u, xa ^ 64u, xa ^ 96u}, a0_ = ka, a1_ = va;
#pragma unroll
    for (int n = 0; n < 8; ++n) PIN(acc[n & 3][n >> 2]);
    lds_wait(SY_FULL, 4, 1, lane);
    if (r < 14) COLSUM((r & 1) ? va : ka, 8192, 2 * cw + (r & 1));
#pragma unroll
    for (int n = 0; n < 12; ++n) { RDK(n % 6, n / 6); SB; }
    unsigned tv = 1; int ts = 0;
    PIN(tv);
#pragma unroll 1
    for (;;) {
      unsigned fv; int dl;
#pragma unroll
      for (int m = 0; m < 16; ++m) {
        MMK(m >> 3, m & 7); SB;
        if ((m & 7) >= 1 && (m & 7) < 7) RDK((m & 7) - 1, 2 + (m >> 3));
        if (m == 0) { dl = ((0x24 >> ts) & 1) ? -98304 : 49152; ++ts; asm volatile("" : "+s"(dl), "+s"(ts)); }
        if (m == 7) { ax[0] += dl; ax[1] += dl; PIN(ax[0]); PIN(ax[1]); }
        if (m == 8) { if (KNA && ts <= 1) __builtin_amdgcn_s_sleep(KNA); fv = *(volatile LDS unsigned*)fa; }
        if (m == 14) *(volatile LDS unsigned*)da = tv;
        if (m == 15) { a0_ += dl; a1_ += dl; ++tv; CHECK_FULL(fv, tv); TSW(55 + ts, STW); }
        SB;
      }
      if (__builtin_expect(ts == 7, 0)) break;
#pragma unroll
      for (int m = 0; m < 16; ++m) {
        MMK(2 + (m >> 3), m & 7); SB;
        if ((m & 7) >= 1 && (m & 7) < 7) RDK((m & 7) - 1, m >> 3);
        if (m == 7) { ax[2] += dl; ax[3] += dl; }
        SB;
      }
    }
    TSW(8, 4);
    unsigned st[4]; u32x2 cv[2]; f32x4 rsv[2][4];
#pragma unroll
    for (int a = 0; a < 4; ++a) st[a] = *(LDS const unsigned*)(L_ST + (a * 32 + l31) * 4);
#pragma unroll
    for (int b = 0; b < 2; ++b) cv[b] = *(LDS const u32x2*)(L_CS + ((2 * cw + b) * 32 + l31) * 8);
    unsigned rsa = L_RS + hh * 16;
    PIN(rsa);
#define MK6(a, j) do { if ((j) < 4) MMK(2 + ((j) >> 1), (a) + 4 * ((j) & 1)); else acc[a][(j) & 1] = mfma(asbf((u32x4){((st[a] & 0xffffu) | (st[a] << 16)) & xm, st[a] & xm, 0u, 0u}), \
        asbf((u32x4){cv[(j) & 1].x, cv[(j) & 1].y, cv[(j) & 1].x, cv[(j) & 1].y}), acc[a][(j) & 1]); } while (0)
#pragma unroll
    for (int n = 0; n < 4; ++n) rsv[0][n] = *(LDS const f32x4*)(rsa + n * 32);
    SB;
#pragma unroll
    for (int j = 0; j < 6; ++j) MK6(0, j);
    SB;
    f32x16 kv = z16;
    u32x4 fk, fv;
#pragma unroll
    for (int gi = 0; gi < 8; ++gi) {
      const int a = gi >> 1, sg = gi & 1, e = 8 * sg;
      if (a < 3 && sg == 0) {
#pragma unroll
        for (int n = 0; n < 4; ++n) rsv[(a + 1) & 1][n] = *(LDS const f32x4*)(rsa + (a + 1) * 128 + n * 32);
      }
      const f32x4 ra_ = rsv[a & 1][2 * sg], rb_ = rsv[a & 1][2 * sg + 1];
      const f32x2 rl[4] = {{ra_[0], ra_[1]}, {ra_[2], ra_[3]}, {rb_[0], rb_[1]}, {rb_[2], rb_[3]}};
      f32x2 u[4], vv[4];
#pragma unroll
      for (int j = 0; j < 4; ++j) u[j] = (f32x2){acc[a][0][e + 2 * j], acc[a][0][e + 2 * j + 1]} * rl[j];
#pragma unroll
      for (int j = 0; j < 4; ++j) vv[j] = (f32x2){acc[a][1][e + 2 * j], acc[a][1][e + 2 * j + 1]} * rl[j];
      PHI_A(u);
      if (gi) kv = mfma(asbf(fk), asbf(fv), kv);
      SB;
      PHI_E(0);
      if (a < 3) MK6(a + 1, 3 * sg);
      SB;
      PHI_E(2);
      if (a < 3) MK6(a + 1, 3 * sg + 1);
      SB;
      fv = (u32x4){pk2(vv[0].x, vv[0].y), pk2(vv[1].x, vv[1].y), pk2(vv[2].x, vv[2].y), pk2(vv[3].x, vv[3].y)};
      SB;
      if (a < 3) MK6(a + 1, 3 * sg + 2);
      SB;
      float po[8];
#pragma unroll
      for (int j = 0; j < 8; ++j) { po[j] = pe[j >> 1][j & 1] + 1e-6f; PIN(po[j]); }
#pragma unroll
      for (int j = 0; j < 4; ++j) pm[j] = __builtin_elementwise_fma(pm[j], (f32x2){UP / LOG2E, UP / LOG2E}, (f32x2){po[2 * j], po[2 * j + 1]});
      fk = (u32x4){pk2(pm[0].x, pm[0].y), pk2(pm[1].x, pm[1].y), pk2(pm[2].x, pm[2].y), pk2(pm[3].x, pm[3].y)};
      SB;
    }
    kv = mfma(asbf(fk), asbf(fv), kv);
    kv = kv * (l31 == 28 ? 1.f / 1.4453125f : 1.f / LOG2E);
    const unsigned ko = WS_KVP + (((c - 12) * 4 + cw) * 16 + r) * 4096u;
#pragma unroll
    for (int e = 0; e < 4; ++e) {
      const u32x4 o = {pk2(kv[4 * e], kv[4 * e + 1]), tag.x, pk2(kv[4 * e + 2], kv[4 * e + 3]), tag.x};
      ST4(o, v16, rws, ko + e * 1024u, "sc1");
    }
  }
  lds_put(da, 8);
  TSW(22, 4);
  bf16x8 WH[9][4];
  unsigned ra[3][2];
#pragma unroll
  for (int cb = 0; cb < 2; ++cb) {
    ra[0][cb] = wh * 4096 + (4 * hh + q) * 128 + ((cb ^ (q >> 1)) << 6) + blk * 32 + p * 8; ra[1][cb] = ra[0][cb] + 65536; ra[2][cb] = ra[0][cb] + 131072;
    PIN(ra[0][cb]); PIN(ra[1][cb]); PIN(ra[2][cb]);
  }
#define RDW(dst, n, reg) do { const unsigned a_ = ra[(reg) >> 3][(n) & 1] + ((reg) & 7) * 8192 + ((n) >> 1) * 2048; dst[n] = tr_frag(a_, a_ + 1024); } while (0)
  if (!kvt) WAIT_VM(0);
  if (lane < 8) ST4(((u32x4){tag.x, tag.y, tag.x, tag.y}), v16, rws, fl_off, "sc1");
  TSW(9, 4);
  if (!pure) {
    lds_wait(SY_FULL2, 4, 1, lane);
#pragma unroll
    for (int n = 0; n < 36; ++n) RDW(WH[n >> 2], n & 3, (16 + (n >> 2)) % 19);
    lds_put(da, 9);
  }
  TSW(30, 4);

  if (!pure) {
    const int kh = wh, R = rt * 2 + wl;
    if (cw == 0) {
      const u64 bl = tag_wait(min(lane, 23) * FS, rws, WS_FL + rt * 32 * FS, 0x7FFFFull, tag);
      lds_put(SY_HOK, (bl & 0xF80000ull) == 0xF80000ull ? 2 : 1);
    } else lds_wait(SY_HOK, 1, 1, lane);
    WAIT_VM(0);
    TSW(12, 4);
    const unsigned hs = WS_H + (R * 112 + kh * 56) * 1024u;
    constexpr int NH = NHW, E = 28 - NH;
    u32x4 hf[NH][2];
#define HLD(u) do { const unsigned o_ = hs + ROT(u) * 2048u; \
      LD4(hf[(u) % NH][0], v16, rws, o_, "sc1"); LD4(hf[(u) % NH][1], v16, rws, o_, "offset:1024 sc1"); } while (0)
#pragma unroll
    for (int u = 0; u < NH; ++u) HLD(u);
    f32x16 af[2] = {z16, z16};
    bf16x8 W[LA + 1][4];
    TSW(13, 4); TSC(26, 4);
#pragma unroll
    for (int u = 0; u < 28; ++u) {
      constexpr auto total = [](int u_) { return 2 * NH + 2 * (u_ < E ? u_ : E) + (u_ > E ? 3 : 0); };
      asm volatile("s_waitcnt vmcnt(%2)" : "+v"(hf[u % NH][0]), "+v"(hf[u % NH][1]) : "n"(total(u) - 2 - 2 * u) : "memory");
      if (u == 9 - LA) { lds_wait(SY_RDY, 4, 1, lane); TSW(28, 4); }
#pragma unroll
      for (int n = 0; n < 4; ++n) {
        af[n & 1] = mfma(u < 9 ? WH[u < 9 ? u : 0][n] : W[u % (LA + 1)][n], asbf(hf[u % NH][n >> 1]), af[n & 1]); SB;
        if (u + LA >= 9 && u + LA < 28) RDW(W[(u + LA) % (LA + 1)], n, (u + LA - 3) % 19);
        SB;
      }
      if (u == 16 - LA) lds_put(SY_DONE2 + cw * 4, 1);
      if (u == 20 - LA) lds_put(SY_DONE2 + cw * 4, 2);
      if (u < E) HLD(u + NH);
      else if (u == E) FIN_LD;
    }
    TSW(14, 4); TSC(27, 4);
    __builtin_amdgcn_s_setprio(0);
    lds_wait(SY_DONE2, 4, 2, lane);
    PUT_TILES(114688 + cw * 8192, af);
    WAIT_VM(0);
  }
  }
  if (pure) {
    const int id = xc * 4 + (ix >> 3), hd = id >> 1;
    lds_wait(SY_DONE, 4, 8, lane);
    f32x4 a, b;
#pragma unroll 1
    for (unsigned sp = 0; sp < SPIN_CAP; ++sp) {
      u32x4 pr[4];
#pragma unroll
      for (int n = 0; n < 4; ++n) LD4(pr[n], v16, rws, WS_KVP + (hd * 16 + 2 * w + (n >> 1)) * 4096u + (id & 1) * 2048u + (n & 1) * 1024u, "sc1");
      WAIT_VM(0);
      unsigned bad = 0;
#pragma unroll
      for (int n = 0; n < 4; ++n) { PIN(pr[n]); bad |= (pr[n].y ^ tag.x) | (pr[n].w ^ tag.x); }
#define F4(n) (f32x4){bflo(pr[n].x), bfhi(pr[n].x), bflo(pr[n].z), bfhi(pr[n].z)}
      a = F4(0) + F4(2); b = F4(1) + F4(3);
      if (__all(bad == 0)) break;
      __builtin_amdgcn_s_sleep(RSLEEP);
    }
    TSW(19, 4);
    *(LDS f32x4*)(w * 2048 + v16) = a; *(LDS f32x4*)(w * 2048 + 1024 + v16) = b;
    BARRIER;
    TSW(20, 4);
    if (w == 4) {
      a = *(LDS const f32x4*)v16; b = *(LDS const f32x4*)(1024 + v16);
#pragma unroll
      for (int n = 1; n < 8; ++n) { a += *(LDS const f32x4*)(n * 2048 + v16); b += *(LDS const f32x4*)(n * 2048 + 1024 + v16); }
      u32x4 o = {pk2(a[0], a[1]), pk2(a[2], a[3]), pk2(b[0], b[1]), pk2(b[2], b[3])};
      if ((id & 1) && hh != (hd & 1)) { if (hd & 2) { o.x = 0; o.y = 0; } else { o.z = 0; o.w = 0; } }
      ST4(o, v16, rws, WS_KV + id * 1024u, "sc1");
      TSW(21, 4);
      WAIT_VM(0);
      ST4(((u32x4){tag.x, tag.y, tag.x, tag.y}), (lane >> 3) * 32 * FS + (lane & 7) * 16, rws, WS_KF + id * FS, "sc1");
      TSW(10, 4); TS_DUMP;
    } else if (w == 5 && bid == 255) {
#pragma unroll 1
      for (int n = 0; n < 4; ++n) tag_wait((unsigned)((lane >> 4) * 64 + (lane & 15) * 2) * FS, rws, WS_FL + n * 256 * FS, ~0ull, tag);
      if (lane == 0) ST2(tag, 0u + lane, rws, WS_EP, "sc1");
    }
    return;
  }
  BARRIER;
  TSW(15, 4);
  {
    const unsigned a0 = 49152 + (tid >> 8) * 8192 + (tid & 7) * 1024 + ((((tid >> 3) & 31) ^ ((tid & 7) * 2)) << 4);
    f32x4 f[2][2], sp[4][2];
#pragma unroll
    for (int n = 0; n < 2; ++n) { f[n][0] = *(LDS const f32x4*)(a0 + 65536 + n * 16384); f[n][1] = *(LDS const f32x4*)(a0 + 65536 + n * 16384 + 512); }
#pragma unroll
    for (int n = 0; n < 4; ++n) { sp[n][0] = *(LDS const f32x4*)(a0 + n * 16384); sp[n][1] = *(LDS const f32x4*)(a0 + n * 16384 + 512); }
    SB;
    PIN(xr); PIN(br); PIN(sg_);
    const float rs = __builtin_amdgcn_rcpf(bfhi(sg_)), rs2 = rs * rs;
    u32x4 ov;
#pragma unroll
    for (int e = 0; e < 4; ++e) {
      const int hf_ = e >> 1, j = (e & 1) * 2;
      const float t0 = fmaf(f[0][hf_][j] + f[1][hf_][j], rs2, bflo(br[e])), t1 = fmaf(f[0][hf_][j + 1] + f[1][hf_][j + 1], rs2, bfhi(br[e]));
      const float g0 = t0 * __builtin_amdgcn_rcpf(1.f + __expf(-t0)), g1 = t1 * __builtin_amdgcn_rcpf(1.f + __expf(-t1));
      const float s0_ = (sp[0][hf_][j] + sp[1][hf_][j]) + (sp[2][hf_][j] + sp[3][hf_][j]);
      const float s1_ = (sp[0][hf_][j + 1] + sp[1][hf_][j + 1]) + (sp[2][hf_][j + 1] + sp[3][hf_][j + 1]);
      ov[e] = pk2(fmaf(s0_, g0, bflo(xr[e])), fmaf(s1_, g1, bfhi(xr[e])));
    }
    ST4(ov, fxo, mk_rsrc(out, S * D * 2), 0u, "sc0 sc1");
    TSW(11, 4);
    if (w == 4) TS_DUMP;
  }
}

extern "C" void kernel_launch(void* const* d_in, const int* in_sizes, int n_in, void* d_out, int out_size, void* d_ws,
                              size_t ws_size, hipStream_t stream) {
  (void)hipFuncSetAttribute((const void*)fused, hipFuncAttributeMaxDynamicSharedMemorySize, LDS_BYTES);
  fused<<<256, 512, LDS_BYTES, stream>>>((const u16*)d_in[0], (const u16*)d_in[1], (const u16*)d_in[4], (char*)d_ws, (const u16*)d_in[2],
                                         (const u16*)d_in[5], (const u16*)d_in[6], (const u16*)d_in[7], (u16*)d_out);
}
```

```cpp
#include <hip/hip_runtime.h>
#include <hip/hip_bf16.h>
#include <stdint.h>
#pragma clang diagnostic ignored "-Wint-to-pointer-cast"

typedef unsigned short u16;
typedef unsigned long long u64;
typedef __bf16 bf16x8 __attribute__((ext_vector_type(8)));
typedef __bf16 bf16x2 __attribute__((ext_vector_type(2)));
typedef short s16x4 __attribute__((ext_vector_type(4)));
typedef float f32x4 __attribute__((ext_vector_type(4)));
typedef float f32x16 __attribute__((ext_vector_type(16)));
typedef float f32x2 __attribute__((ext_vector_type(2)));
typedef unsigned u32x4 __attribute__((ext_vector_type(4)));
typedef unsigned u32x2 __attribute__((ext_vector_type(2)));
#define LDS __attribute__((address_space(3)))
#define DI __device__ __forceinline__

constexpr int S = 2048, D = 448, F = 1792;
constexpr unsigned FS = 128;
constexpr int PFA = 9, PFE = 4, NHW = 7, LA = 2;
constexpr unsigned WS_EP = 0;
constexpr unsigned WS_KF = 1024;
constexpr unsigned WS_FL = WS_KF + 256 * FS;
constexpr unsigned WS_ST = WS_FL + 1024 * FS;
constexpr unsigned WS_CS = WS_ST + 32768;
constexpr unsigned WS_KV = WS_CS + 16 * 7 * 2048;
constexpr unsigned WS_KVP = WS_KV + 32768;
constexpr unsigned WS_PQ = WS_KVP + 16 * 16 * 4096;
constexpr unsigned WS_H = WS_PQ + 64 * 28 * 1024;
constexpr unsigned WS_END = WS_H + 64 * 112 * 1024;
constexpr unsigned L_CS = 155648, L_ST = L_CS + 2048, L_RS = L_ST + 512, L_SY = L_RS + 512, L_END = L_SY + 256;
constexpr unsigned SY_FULL = L_SY, SY_DONE = L_SY + 32, SY_RDY = L_SY + 48, SY_FULL2 = L_SY + 64, SY_DONE2 = L_SY + 80,
                   SY_GO = L_SY + 112, SY_WOK = L_SY + 128, SY_HOK = L_SY + 100, SY_TAG = L_SY + 104;
constexpr unsigned CODE_BYTES = 31872;
constexpr unsigned SPIN_CAP = 1u << 19, LDS_CAP = 1u << 23;

DI u32x4 mk_rsrc(const void* p, unsigned bytes) {
  uint64_t a = (uint64_t)p;
  u32x4 r; r.x = (unsigned)a; r.y = (unsigned)(a >> 32) & 0xffffu; r.z = bytes; r.w = 0x00020000u; return r;
}
DI void dma16(unsigned lds_addr, unsigned voff, u32x4 rsrc, unsigned soff) {
  asm volatile("s_mov_b32 m0, %0\n\ts_nop 0\n\tbuffer_load_dwordx4 %1, %2, %3 offen lds"
               :: "s"(lds_addr), "v"(voff), "s"(rsrc), "s"(soff) : "memory");
}
template <unsigned OFF> DI void dma16o(unsigned lds_base, unsigned voff, u32x4 rsrc, unsigned soff) {
  asm volatile("s_add_u32 m0, %0, %4\n\ts_nop 0\n\tbuffer_load_dwordx4 %1, %2, %3 offen lds"
               :: "s"(lds_base), "v"(voff), "s"(rsrc), "s"(soff), "n"(OFF) : "memory", "scc");
}
#define SB __builtin_amdgcn_sched_barrier(0)
#define FENCE asm volatile("" ::: "memory")
#define PIN(a) asm volatile("" : "+v"(a))
#define WAIT_VM(n) asm volatile("s_waitcnt vmcnt(%0)" :: "n"(n) : "memory")
#define BARRIER asm volatile("s_waitcnt lgkmcnt(0)\n\ts_barrier" ::: "memory")
#define LD4(dst, voff, rsrc, soff, mod) asm volatile("buffer_load_dwordx4 %0, %1, %2, %3 offen " mod : "=v"(dst) : "v"(voff), "s"(rsrc), "s"(soff) : "memory")
#define LD1(dst, voff, rsrc, soff, mod) asm volatile("buffer_load_dword %0, %1, %2, %3 offen " mod : "=v"(dst) : "v"(voff), "s"(rsrc), "s"(soff) : "memory")
#define ST4(src, voff, rsrc, soff, mod) asm volatile("buffer_store_dwordx4 %0, %1, %2, %3 offen " mod "\n\ts_nop 1" :: "v"(src), "v"(voff), "s"(rsrc), "s"(soff) : "memory")
#define ST2(src, voff, rsrc, soff, mod) asm volatile("buffer_store_dwordx2 %0, %1, %2, %3 offen " mod :: "v"(src), "v"(voff), "s"(rsrc), "s"(soff) : "memory")
DI f32x16 mfma(bf16x8 a, bf16x8 b, f32x16 c) { return __builtin_amdgcn_mfma_f32_32x32x16_bf16(a, b, c, 0, 0, 0); }
DI bf16x8 asbf(u32x4 v) { return __builtin_bit_cast(bf16x8, v); }
DI bf16x8 ld_frag(unsigned a) { return *(LDS const bf16x8*)a; }
DI bf16x8 tr_frag(unsigned a0, unsigned a1) {
  s16x4 lo = __builtin_amdgcn_ds_read_tr16_b64_v4i16((LDS s16x4*)a0);
  s16x4 hi = __builtin_amdgcn_ds_read_tr16_b64_v4i16((LDS s16x4*)a1);
  return __builtin_bit_cast(bf16x8, __builtin_shufflevector(lo, hi, 0, 1, 2, 3, 4, 5, 6, 7));
}
DI unsigned pk2(float a, float b) { f32x2 v = {a, b}; return __builtin_bit_cast(unsigned, __builtin_convertvector(v, bf16x2)); }
DI float bflo(unsigned u) { return __uint_as_float(u << 16); }
DI float bfhi(unsigned u) { return __uint_as_float(u & 0xffff0000u); }
DI float rbf(float f) { return bflo(pk2(f, 0.f)); }
constexpr float LOG2E = 1.44269504f, EPS2 = 1e-6f * LOG2E;
DI f32x2 phi_u(float a0, float a1, f32x2 rl) { return __builtin_elementwise_fma((f32x2){a0, a1}, rl, (f32x2){EPS2, EPS2}); }
#define PHI_E(j0) _Pragma("unroll") for (int j_ = (j0); j_ < (j0) + 2; ++j_) pe[j_] = (f32x2){__builtin_amdgcn_fmed3f(__builtin_amdgcn_exp2f(u[j_].x), 0.f, 1.f), \
                                                                                     __builtin_amdgcn_fmed3f(__builtin_amdgcn_exp2f(u[j_].y), 0.f, 1.f)}; SB
#define PHI_B(o) _Pragma("unroll") for (int j_ = 0; j_ < 4; ++j_) u[j_] = (f32x2){fmaxf(u[j_].x, EPS2), fmaxf(u[j_].y, EPS2)}; SB; \
              _Pragma("unroll") for (int j_ = 0; j_ < 4; ++j_) u[j_] = __builtin_elementwise_fma(u[j_], (f32x2){1.f / LOG2E, 1.f / LOG2E}, pe[j_]); SB; \
              o = (u32x4){pk2(u[0].x, u[0].y), pk2(u[1].x, u[1].y), pk2(u[2].x, u[2].y), pk2(u[3].x, u[3].y)}
DI unsigned relu2(unsigned v) {
  typedef short s16x2 __attribute__((ext_vector_type(2)));
  return __builtin_bit_cast(unsigned, __builtin_elementwise_max(__builtin_bit_cast(s16x2, v), (s16x2){0, 0}));
}
DI float mul1(float a, float b) { float r; asm("v_mul_f32_e32 %0, %1, %2" : "=v"(r) : "v"(a), "v"(b)); return r; }
DI float sum8(float v) {
  v += __uint_as_float(__builtin_amdgcn_update_dpp(0u, __float_as_uint(v), 0xB1, 0xf, 0xf, true));
  v += __uint_as_float(__builtin_amdgcn_update_dpp(0u, __float_as_uint(v), 0x4E, 0xf, 0xf, true));
  v += __uint_as_float(__builtin_amdgcn_update_dpp(0u, __float_as_uint(v), 0x141, 0xf, 0xf, true));
  return v;
}
DI void lds_put(unsigned a, unsigned v) { FENCE; *(volatile LDS unsigned*)a = v; FENCE; }
DI void lds_wait(unsigned a, int n, unsigned need, int lane) {
  const unsigned ad = a + (lane & (n - 1)) * 4;
#pragma unroll 1
  for (unsigned sp = 0; sp < LDS_CAP; ++sp) {
    FENCE; const unsigned v = *(volatile LDS unsigned*)ad; FENCE;
    if (__all(v >= need)) break;
    __builtin_amdgcn_s_sleep(1);
  }
}
DI u64 tag_wait(unsigned voff, u32x4 rs, unsigned soff, u64 mask, u32x2 tag) {
  u64 bl = 0;
#pragma unroll 1
  for (unsigned sp = 0; sp < SPIN_CAP; ++sp) {
    u32x2 v;
    asm volatile("buffer_load_dwordx2 %0, %1, %2, %3 offen sc1\n\ts_waitcnt vmcnt(0)" : "=&v"(v) : "v"(voff), "s"(rs), "s"(soff) : "memory");
    bl = __ballot(v.x == tag.x && v.y == tag.y);
    if ((bl & mask) == mask) break;
    __builtin_amdgcn_s_sleep(4);
  }
  return bl;
}

__global__ __launch_bounds__(512, 2) void fused(const u16* __restrict__ x, const u16* __restrict__ wqkv, const u16* __restrict__ w1, char* ws,
                                                const u16* __restrict__ wo, const u16* __restrict__ b1, const u16* __restrict__ w2,
                                                const u16* __restrict__ b2, u16* __restrict__ out) {
  const int bid = blockIdx.x, tid = threadIdx.x, lane = tid & 63;
  const int w = __builtin_amdgcn_readfirstlane(tid >> 6);
  const int xc = bid & 7, ix = bid >> 3, sl = ix & 7, rl = ix >> 3;
  const int r = (xc >> 1) * 4 + rl, s0 = r >= 14 ? 0 : r >> 1;
  const u32x4 rx = mk_rsrc(x, S * D * 2);
  const unsigned vx0 = (unsigned)(r * 128 + (w & 1) * 64 + (lane >> 3)) * 896u + (unsigned)(((lane & 7) ^ (lane >> 4)) << 4), vx1 = (vx0 ^ 64u) + 7168u;
#define XGROUP(n) do { dma16o<(n) * 2048>(lx, vx0, rx, sg * 128 + (n) * 14336); dma16o<(n) * 2048 + 1024>(lx, vx1, rx, sg * 128 + (n) * 14336); } while (0)
  unsigned cpf;
  u64 pc; asm volatile("s_getpc_b64 %0" : "=s"(pc));
  if (w < 2) { const unsigned lx = w * 8192; const int sg = s0; XGROUP(0); XGROUP(1); XGROUP(2); XGROUP(3); }
  if (w == 4) LD1(cpf, lane < 8 ? (unsigned)(ix + lane * 32) * 128u : 0x80000000u, mk_rsrc((const void*)(pc & ~255ull), CODE_BYTES), 0u, "");
  asm volatile("" :: "s"(wqkv), "s"(w1), "s"(ws));
  const int c = (int)(((xc & 1) ? 0xEFB98765u : 0xDCA43210u) >> (4 * sl)) & 15;
  const bool kvt = sl >= 6, pure = sl == 7;
  const int rt = xc * 4 + rl, ct = sl + 2 * rl - (sl + 2 * rl >= 7 ? 7 : 0), rot2 = 7 * rl + xc - (7 * rl + xc >= 28 ? 28 : 0);
  const int l31 = lane & 31, hh = lane >> 5, q = (lane >> 2) & 3, p = lane & 3, blk = (lane >> 4) & 1;
  const unsigned slot_b = kvt ? 49152u : 40960u;
  const u32x4 rws = mk_rsrc(ws, WS_END);
  const unsigned v16 = lane * 16;
  u32x2 tag;
  const unsigned swz = (unsigned)(((lane & 7) ^ ((lane >> 2) & 4)) << 4);
  f32x16 z16;
#pragma unroll
  for (int e = 0; e < 16; ++e) z16[e] = 0.f;

  u32x4 xr, br; unsigned sg_;
  const unsigned fxo = (unsigned)(rt * 64 + (tid >> 3)) * 896u + ct * 128 + (tid & 7) * 16;
#define FIN_LD do { LD4(xr, fxo, rx, 0u, ""); LD4(br, (tid & 7) * 16u, mk_rsrc(b2, D * 2), ct * 128u, ""); LD1(sg_, (unsigned)(tid & ~7) * 2u, rws, WS_ST + rt * 1024u, "sc1"); } while (0)
#define PUT_TILES(base, t) do { _Pragma("unroll") for (int n_ = 0; n_ < 8; ++n_) *(LDS f32x4*)((base) + n_ * 1024 + (v16 ^ (n_ * 32))) = \
      (f32x4){t[n_ >> 2][4 * (n_ & 3)], t[n_ >> 2][4 * (n_ & 3) + 1], t[n_ >> 2][4 * (n_ & 3) + 2], t[n_ >> 2][4 * (n_ & 3) + 3]}; } while (0)
  if (__builtin_expect(w < 4, 1)) {
    const int h = w, hp = h >> 1, hs_ = h & 1;
    const u32x4 rw2 = mk_rsrc(w2, F * D * 2), rwo = mk_rsrc(wo, D * D * 2);
    const unsigned vwo = (unsigned)(lane >> 3) * 896u + ct * 128 + swz;
    const unsigned vw2 = vwo + (unsigned)((h >> 1) * 896 + (h & 1) * 16) * 896u;
    const int hg = (h + xc + rl) & 3;
    u32x4 R[18];
#define ROT(u) ((u) + rot2 >= 28 ? (u) + rot2 - 28 : (u) + rot2)
#define PFLD(n) LD4(R[n], vw2, rw2, ROT(19 + ((n) >> 1)) * 28672u + ((n) & 1) * 7168u, "")
    auto fill = [&](int u, int g, int cnt) {
#pragma unroll 1
      for (int e = u + cnt; u < e; ++u, ++g) {
        const unsigned k = ROT(u) * 28672u;
        dma16(g * 8192 + h * 2048, vw2, rw2, k); dma16(g * 8192 + h * 2048 + 1024, vw2, rw2, k + 7168u);
      }
    };
    {
      __builtin_amdgcn_s_setprio(3);
      const bool a1 = c < 10, b1_ = c < 9;
      const u32x4 r0 = mk_rsrc(a1 ? w1 : wqkv, a1 ? D * F * 2 : D * 3 * D * 2), r1 = mk_rsrc(b1_ ? w1 : wqkv, b1_ ? D * F * 2 : D * 3 * D * 2);
      const unsigned p0 = a1 ? 3584u : 2688u, p1 = b1_ ? 3584u : 2688u;
      unsigned V0, V1, V2, V3;
      {
        const unsigned kl = 32 * hs_ + (lane >> 3);
        const unsigned c0 = (a1 ? c : c - 10) * 384, c1 = c == 9 ? 512 : c0;
        const unsigned vk = (32 * hs_ + (lane >> 4)) * 2688 + ((224 * c - 1792) & ~63) + ((q ^ (lane >> 4)) << 6) + p * 16;
        V0 = kvt ? vk : kl * p0 + c0 + swz;
        V1 = kvt ? vk + 4 * 2688 : kl * p1 + c1 + swz + 128;
        V2 = kvt ? V0 + 896 : V1 + 128;
        V3 = V1 + 896;
      }
      int sg = s0 + hp; sg = sg == 7 ? 0 : sg;
      unsigned so = hp * slot_b;
#define GROUP(n) do { const unsigned kk = sg * 64 + 8 * (n), sa = kk * p0, sb_ = kk * p1; \
          if (i || hp) XGROUP(n); \
          dma16o<16384 + (n) * 1024>(lw, V0, r0, sa); \
          dma16o<24576 + (n) * 1024>(lw, V1, r1, sb_); \
          dma16o<32768 + (n) * 1024>(lw, V2, r1, sb_); \
          if (kvt) dma16o<40960 + (n) * 1024>(lw, V3, r1, sb_); } while (0)
      if (hp) BARRIER;
      unsigned t2;
#pragma unroll
      for (int i = 0; i < 4; ++i) {
        const int t = hp + 2 * i;
        if (i == 3 && hp) break;
        if (i >= 2 || (i == 1 && hp)) lds_wait(SY_DONE, 4, t - 2, lane);
        const unsigned lx = so + hs_ * 8192, lw = so + hs_ * 4096;
        GROUP(0); GROUP(1); GROUP(2); GROUP(3);
        if (i == 0 && !hp) BARRIER;
        if (i == 2 && hp) break;
        if (i == 3) lds_put(SY_GO, 1);
        if (i == 1) {
          LD1(t2, lane < (hp ? 0 : hs_ ? 13 : 49) ? lane * 128u : 0x80000000u, hs_ ? rwo : rw2, bid * (hs_ ? 1664u : 6272u), "");
          WAIT_VM(1);
        } else WAIT_VM(0);
        if (i == 2) asm volatile("" :: "v"(t2));
        lds_put(SY_FULL + h * 4, t + 2);
        if (!pure && ((i == 1 && hp) || i == 2)) {
#pragma unroll
          for (int n = 0; n < (i == 2 ? PFE : PFA); ++n) PFLD(n);
        }
        so = so == 0 ? 2 * slot_b : so - slot_b;
        sg = sg >= 5 ? sg - 5 : sg + 2;
      }
      tag = *(volatile LDS u32x2*)SY_TAG;
      if (hp) {
        u32x4 xf[8];
        unsigned bi;
        const bool chk = !hs_ || kvt || lane < 32;
        const u32x4 rb1 = mk_rsrc(b1, c < 10 ? F * 2 : 0);
#pragma unroll 1
        for (unsigned sp = 0; sp < SPIN_CAP; ++sp) {
#pragma unroll
          for (int k = 0; k < 7; ++k) LD4(xf[k], v16, rws, WS_CS + c * 14336u + hs_ * 1024u + k * 2048u, "sc1");
          LD4(xf[7], v16, rws, WS_ST + r * 2048u + hs_ * 1024u, "sc1");
          LD1(bi, lane * 4u, rb1, c * 384u + hs_ * 256u, "");
          WAIT_VM(9); lds_put(SY_FULL + h * 4, 7);
          WAIT_VM(0);
          PIN(xf[0]); PIN(xf[7]); PIN(bi);
          if (kvt && l31 == 30) bi = 0x3f80u;
          unsigned bad = (xf[0].y ^ tag.x) | (xf[0].w ^ tag.x);
          float ax_ = __uint_as_float(xf[0].x), ay_ = __uint_as_float(xf[0].z);
#pragma unroll
          for (int k = 1; k < 7; ++k) {
            PIN(xf[k]);
            ax_ += __uint_as_float(xf[k].x); ay_ += __uint_as_float(xf[k].z); PIN(ax_); PIN(ay_);
            bad |= (xf[k].y ^ tag.x) | (xf[k].w ^ tag.x); PIN(bad);
          }
          const unsigned hi = pk2(ax_, ay_); ax_ -= bflo(hi); PIN(ax_); ay_ -= bfhi(hi);
          const unsigned mi = pk2(ax_, ay_); ax_ -= bflo(mi); PIN(ax_); ay_ -= bfhi(mi);
          const unsigned lo = pk2(ax_, ay_);
          *(LDS u32x4*)(L_CS + hs_ * 1024 + v16) = (u32x4){(hi & 0xffffu) | (mi << 16), (lo & 0xffffu) | (bi << 16),
                                                           (hi >> 16) | (mi & 0xffff0000u), (lo >> 16) | (bi & 0xffff0000u)};
          *(LDS unsigned*)(L_ST + hs_ * 256 + lane * 4) = xf[7].x;
          *(LDS float*)(L_RS + hs_ * 256 + lane * 4) = __builtin_amdgcn_rcpf(bfhi(xf[7].x)) * LOG2E;
          if (__builtin_expect(__all((bad == 0 || !chk) && xf[7].y == tag.x), 1)) break;
          __builtin_amdgcn_s_sleep(8);
        }
        lds_put(SY_FULL + h * 4, 8);
        if (!pure) {
#pragma unroll
          for (int n = PFA; n < 18; ++n) PFLD(n);
        }
        lds_wait(SY_GO, 1, 1, lane);
        asm volatile("" :: "v"(t2));
      }
      __builtin_amdgcn_s_setprio(0);
    }
    if (!pure) {
#pragma unroll
    for (int n = PFE; n < 18; ++n) { if (h < 2) PFLD(n); }
    const unsigned wb = 49152 + (h ^ 2) * 14336;
    lds_wait(SY_DONE, 4, h < 2 ? 6 : 5, lane);
#pragma unroll 1
    for (int n = 0; n < 14; ++n) dma16(wb + n * 1024, vwo, rwo, hg * 100352u + n * 7168u);
    lds_wait(SY_DONE, 4, 6, lane);
    fill(0, 16, 3);
    WAIT_VM(6);
    bf16x8 WO[4][2][2];
    {
      const unsigned a0 = wb + (4 * hh + q) * 128 + (((q >> 1) & 1) << 6) + blk * 32 + p * 8, a1_ = a0 ^ 64u;
#pragma unroll
      for (int n = 0; n < 4; ++n)
#pragma unroll
        for (int sg = 0; sg < 2; ++sg) {
          const unsigned o = n * 3584 + sg * 2048;
          WO[n][sg][0] = tr_frag(a0 + o, a0 + o + 1024); WO[n][sg][1] = tr_frag(a1_ + o, a1_ + o + 1024);
        }
    }
#pragma unroll
    for (int n = 0; n < 8; ++n) {
      u32x4 t = __builtin_bit_cast(u32x4, WO[n >> 1][1][n & 1]);
      if (hh) { t.z = 0; t.w = 0; }
      WO[n >> 1][1][n & 1] = asbf(t);
    }
#pragma unroll
    for (int n = 0; n < 16; ++n) PIN(WO[n >> 2][(n >> 1) & 1][n & 1]);
    lds_put(SY_WOK + h * 4, 1);
    lds_wait(SY_DONE, 4, c < 10 ? 8 : 7, lane);
    fill(3, 0, 6);
    lds_wait(SY_WOK, 4, 1, lane);
    fill(9, 6, 3);
    WAIT_VM(6); lds_put(SY_FULL2 + h * 4, 1);
    fill(12, 9, 7);
    lds_wait(SY_DONE, 4, 9, lane);
#pragma unroll
    for (int n = 0; n < 18; ++n) { PIN(R[n]); *(LDS u32x4*)((n < 6 ? 131072 : -24576) + h * 2048 + v16 + (n >> 1) * 8192 + (n & 1) * 1024) = R[n]; }
    WAIT_VM(0); lds_put(SY_RDY + h * 4, 1);
    u32x4 PQ[2][7], KV[8];
    __builtin_amdgcn_s_setprio(1);
    lds_wait(SY_HOK, 1, 1, lane);
    if (*(volatile LDS unsigned*)SY_HOK < 2u) tag_wait(min(lane, 4) * FS, rws, WS_FL + (rt * 32 + 19) * FS, ~0ull, tag);
#pragma unroll
    for (int n = 0; n < 14; ++n) LD4(PQ[n / 7][n % 7], v16, rws, WS_PQ + ((rt * 2 + n / 7) * 28 + hg * 7 + n % 7) * 1024u, "sc1");
    FIN_LD;
    __builtin_amdgcn_s_setprio(3);
    tag_wait(min(lane, 7) * FS, rws, WS_KF + (xc * 32 + hg * 8) * FS, ~0ull, tag);
#pragma unroll
    for (int n = 0; n < 8; ++n) LD4(KV[n], v16, rws, WS_KV + hg * 8192u + n * 1024u, "sc1");
    __builtin_amdgcn_s_setprio(0);
    u32x4 PB[2][4];
#pragma unroll
    for (int n = 0; n < 14; ++n) PIN(PQ[n / 7][n % 7]);
#pragma unroll
    for (int u = 0; u < 8; ++u) {
      const u32x4* P = PQ[u >> 2]; const int n = u & 3, lo = n < 3 ? 2 * n + 1 : 5, hi = n ? 2 * n - 1 : 1;
      PB[u >> 2][n] = (u32x4){P[lo].x, P[lo].y, P[hi].z, P[hi].w}; PIN(PB[u >> 2][n]);
    }
    WAIT_VM(4);
#pragma unroll
    for (int n = 0; n < 4; ++n) PIN(KV[n]);
    f32x16 nm[3], as[2][2] = {{z16, z16}, {z16, z16}};
    u32x4 O[2][2]; float rd[2];
#define NUM0(u) nm[(u) % 3] = mfma(asbf(KV[2 * ((u) & 3)]), asbf(PQ[(u) >> 2][2 * ((u) & 3)]), z16)
#define NUM1(u) nm[(u) % 3] = mfma(asbf(KV[2 * ((u) & 3) + 1]), asbf(PB[(u) >> 2][(u) & 3]), nm[(u) % 3])
#define DEN(u) __uint_as_float(__builtin_amdgcn_permlane32_swap(__float_as_uint(nm[(u) % 3][12]), __float_as_uint(nm[(u) % 3][12]), false, false)[1])
#define APP(u, i) as[(u) >> 2][(i) & 1] = mfma(WO[(u) & 3][(i) >> 1][(i) & 1], asbf(O[(u) & 1][(i) >> 1]), as[(u) >> 2][(i) & 1])
#define MULS(a, b) _Pragma("unroll") for (int e = (a); e < (b); ++e) o[e] = mul1(nm[u % 3][e], rd[u & 1])
#define CVTS(a, b) _Pragma("unroll") for (int e = (a); e < (b); ++e) O[u & 1][e >> 2][e & 3] = pk2(o[2 * e], o[2 * e + 1])
    NUM0(0); NUM1(0); NUM0(1); NUM1(1);
    rd[0] = __builtin_amdgcn_rcpf(DEN(0) + 1e-6f);
    asm volatile("s_waitcnt vmcnt(0)" : "+v"(rd[0]), "+v"(KV[4]), "+v"(KV[5]), "+v"(KV[6]), "+v"(KV[7]) :: "memory");
    SB;
    unsigned d2 = 0; bool put0 = false;
#pragma unroll
    for (int u = 0; u < 8; ++u) {
      float o[16], dn;
      if (u) APP(u - 1, 0); SB; MULS(0, 6); SB;
      if (u) APP(u - 1, 1); SB; MULS(6, 10); CVTS(0, 2); SB;
      if (u) APP(u - 1, 2); SB; MULS(10, 13); CVTS(2, 4); SB;
      if (u) APP(u - 1, 3); SB; MULS(13, 16); CVTS(4, 6); SB;
      if (u < 6) NUM0(u + 2); SB; if (u < 7) dn = DEN(u + 1) + 1e-6f; SB;
      if (u < 6) NUM1(u + 2); SB; if (u < 7) rd[(u + 1) & 1] = __builtin_amdgcn_rcpf(dn); SB; CVTS(6, 8);
      if (u == 4) d2 = *(volatile LDS unsigned*)(SY_DONE2 + p * 4);
      if (u == 5) { put0 = __all(d2 >= 1u); if (put0) PUT_TILES(49152 + h * 16384, as[0]); }
      SB;
    }
    APP(7, 0); APP(7, 2); APP(7, 1); APP(7, 3);
    if (!put0) { lds_wait(SY_DONE2, 4, 1, lane); PUT_TILES(49152 + h * 16384, as[0]); }
    PUT_TILES(57344 + h * 16384, as[1]);
    }
  } else {
  const int cw = w - 4, wl = cw & 1, wh = cw >> 1;
  u32x4 xs[7];
  if (cw == 3) {
    const unsigned va = (unsigned)(r * 128 + c * 8 + (lane >> 3)) * 896u + (lane & 7) * 16;
#pragma unroll
    for (int k = 0; k < 7; ++k) LD4(xs[k], va, rx, k * 128u, "");
  }
  *(LDS unsigned*)(L_SY + lane * 4) = (lane >> 1) == 1; BARRIER;
  __builtin_amdgcn_s_setprio(2);
  bf16x8 ones = asbf((u32x4){0x3f803f80u, 0x3f803f80u, 0x3f803f80u, 0x3f803f80u});
  PIN(ones);
  {
    {
      u64 ep;
      asm volatile("s_load_dwordx2 %0, %1, 0x0\n\ts_waitcnt lgkmcnt(0) vmcnt(0)" : "=&s"(ep) : "s"(ws) : "memory");
      tag.x = (unsigned)ep + 1u; tag.y = (unsigned)(ep >> 32) + 1u;
    }
    if (cw == 0) { asm volatile("" :: "v"(cpf)); *(volatile LDS u32x2*)SY_TAG = tag; }
    if (cw == 3) {
      float sq[2] = {0.f, 0.f}, sm[2] = {0.f, 0.f};
      const bf16x2 one2 = __builtin_bit_cast(bf16x2, 0x3f803f80u);
#pragma unroll
      for (int k = 0; k < 7; ++k) {
        PIN(xs[k]);
#pragma unroll
        for (int e = 0; e < 4; ++e) {
          const unsigned u = xs[k][e];
          const bf16x2 v = __builtin_bit_cast(bf16x2, u);
          sq[e & 1] = __builtin_amdgcn_fdot2_f32_bf16(v, v, sq[e & 1], false); sm[e & 1] = __builtin_amdgcn_fdot2_f32_bf16(v, one2, sm[e & 1], false);
        }
      }
      const float m = sum8(sm[0] + sm[1]) * (1.f / D);
      const float var = fmaxf(sum8(sq[0] + sq[1]) * (1.f / D) - m * m, 0.f);
      const unsigned sv = __builtin_amdgcn_ds_bpermute(lane * 32, pk2(-m, __builtin_amdgcn_sqrtf(rbf(rbf(var) + 1e-5f))));
      const u32x4 gr = {sv, tag.x, sv, tag.x};
      if (lane < 8) ST4(gr, v16, rws, WS_ST + (r * 128 + c * 8) * 16, "sc1");
    }
  }
  const unsigned fl_off = WS_FL + ((r * 2 + wl) * 32 + 2 * c + wh) * FS;
  const unsigned fa = SY_FULL + p * 4;
#define CHECK_FULL(v, need) do { if (__builtin_expect(__any((v) < (need)), 0)) lds_wait(SY_FULL, 4, need, lane); FENCE; } while (0)
#define COLSUM(a_, t_, tile) do { const unsigned a = (a_); bf16x8 f[4]; \
    _Pragma("unroll") for (int ks = 0; ks < 4; ++ks) f[ks] = tr_frag(a + ks * 2048, a + ks * 2048 + (t_)); \
    SB; f32x16 cs = z16; \
    _Pragma("unroll") for (int ks = 0; ks < 4; ++ks) cs = mfma(ones, f[ks], cs); \
    const u32x4 gr = {(unsigned)__builtin_amdgcn_ds_bpermute(lane * 8, __float_as_int(cs[0])), tag.x, (unsigned)__builtin_amdgcn_ds_bpermute(lane * 8 + 4, __float_as_int(cs[0])), tag.x}; \
    if (lane < 16) ST4(gr, v16, rws, WS_CS + (c * 7 + s0) * 2048 + (tile) * 256, "sc1"); } while (0)
  unsigned da = SY_DONE + cw * 4;
  PIN(da);
  const unsigned xm = hh ? 0u : ~0u;
  if (!kvt) {
    const unsigned xa = (wl * 64 + l31) * 128 + ((hh ^ ((l31 >> 1) & 7)) << 4);
    const unsigned wr = 16384 + (8 * hh + q) * 128 + blk * 32 + p * 8;
    const unsigned wa0 = wr + wh * 8192 + ((wh ^ (q >> 1)) << 6), wa1 = wr + wh * 16384 + ((wh ^ 1 ^ (q >> 1)) << 6);
    f32x16 acc[2][3] = {{z16, z16, z16}, {z16, z16, z16}};
    bf16x8 X[4][2], W[4][3];
#define RDA(n, ks) do { \
      if ((n) == 0 || (n) == 2) X[ks][(n) >> 1] = ld_frag(ax[ks] + ((n) >> 1) * 4096); \
      else { const unsigned a_ = ((n) == 3 ? a1_ : a0_) + ((n) == 4 ? 8192 : 0) + (ks) * 2048; W[ks][(n) == 1 ? 0 : (n) - 2] = tr_frag(a_, a_ + 512); } } while (0)
#define MMA(ks, m) acc[(m) & 1][(m) >> 1] = mfma(W[ks][(m) >> 1], X[ks][(m) & 1], acc[(m) & 1][(m) >> 1])
    unsigned ax[4] = {xa, xa ^ 32u, xa ^ 64u, xa ^ 96u}, a0_ = wa0, a1_ = wa1;
#pragma unroll
    for (int n = 0; n < 6; ++n) PIN(acc[n & 1][n >> 1]);
    lds_wait(SY_FULL, 4, 1, lane);
    if (r < 14 && !((r & 1) && wl)) {
      const int b = (r & 1) ? 2 : wl;
      COLSUM(b == 1 ? wa1 : wa0 + b * 4096, 512, 3 * wh + b);
    }
#pragma unroll
    for (int n = 0; n < 10; ++n) { RDA(n % 5, n / 5); SB; }
    unsigned tv = 1; int ts = 0;
    PIN(tv);
#pragma unroll 1
    for (;;) {
      unsigned fv; int dl;
#pragma unroll
      for (int m = 0; m < 12; ++m) {
        MMA(m / 6, m % 6); SB;
        if (m % 6 < 5) RDA(m % 6, 2 + m / 6);
        if (m == 0) { dl = ((0x24 >> ts) & 1) ? -81920 : 40960; ++ts; asm volatile("" : "+s"(dl), "+s"(ts)); }
        if (m == 2) { ax[0] += dl; ax[1] += dl; PIN(ax[0]); PIN(ax[1]); }
        if (m == 5) { if (ts <= 4) __builtin_amdgcn_s_sleep(4); fv = *(volatile LDS unsigned*)fa; }
        if (m == 10) *(volatile LDS unsigned*)da = tv;
        if (m == 11) { a0_ += dl; a1_ += dl; ++tv; CHECK_FULL(fv, tv); }
        SB;
      }
      if (__builtin_expect(ts == 7, 0)) break;
#pragma unroll
      for (int m = 0; m < 12; ++m) {
        MMA(2 + m / 6, m % 6); SB;
        if (m % 6 < 5) RDA(m % 6, m / 6);
        if (m == 5) { ax[2] += dl; ax[3] += dl; if (ts <= 4) __builtin_amdgcn_s_sleep(4); }
        SB;
      }
    }
    unsigned st[2]; u32x2 cv[3];
#pragma unroll
    for (int a = 0; a < 2; ++a) st[a] = *(LDS const unsigned*)(L_ST + (wl * 64 + a * 32 + l31) * 4);
#pragma unroll
    for (int b = 0; b < 3; ++b) cv[b] = *(LDS const u32x2*)(L_CS + ((3 * wh + b) * 32 + l31) * 8);
    SB;
    MMA(2, 0); MMA(2, 1); MMA(3, 0); MMA(3, 1);
    SB;
    bf16x8 xe[2];
#pragma unroll
    for (int a = 0; a < 2; ++a) xe[a] = asbf((u32x4){((st[a] & 0xffffu) | (st[a] << 16)) & xm, st[a] & xm, 0u, 0u});
#define MME(m) acc[(m) & 1][(m) >> 1] = mfma(asbf((u32x4){cv[(m) >> 1].x, cv[(m) >> 1].y, cv[(m) >> 1].x, cv[(m) >> 1].y}), xe[(m) & 1], acc[(m) & 1][(m) >> 1])
    MME(0); MME(1);
    SB;
    const int R0 = r * 4 + wl * 2;
    const unsigned sk = c == 9 ? 0x40000000u : 0u;
#define MM6(b, j) do { if ((j) < 4) MMA(2 + ((j) >> 1), 2 * (b) + ((j) & 1)); else MME(2 * (b) + ((j) & 1)); } while (0)
    if (2 * c + wh < 19) {
      const unsigned ob = WS_H + (R0 * 112 + 12 * c + 6 * wh) * 1024u;
#pragma unroll
      for (int b = 0; b < 3; ++b)
#pragma unroll
        for (int n = 0; n < 4; ++n) {
          const int a = n >> 1, e = 8 * (n & 1);
          float t[8]; u32x4 o;
          if (b < 2) MM6(b + 1, n < 2 ? 2 * n : n + 2);
          SB;
          if (n == 0) asm volatile("s_nop 10" : "+v"(acc[0][b]), "+v"(acc[1][b]));
          asm volatile("v_mul_f32_e64 %0, %8, |%8|\n\tv_mul_f32_e64 %1, %9, |%9|\n\tv_mul_f32_e64 %2, %10, |%10|\n\tv_mul_f32_e64 %3, %11, |%11|\n\t"
                       "v_mul_f32_e64 %4, %12, |%12|\n\tv_mul_f32_e64 %5, %13, |%13|\n\tv_mul_f32_e64 %6, %14, |%14|\n\tv_mul_f32_e64 %7, %15, |%15|"
                       : "=&v"(t[0]), "=&v"(t[1]), "=&v"(t[2]), "=&v"(t[3]), "=&v"(t[4]), "=&v"(t[5]), "=&v"(t[6]), "=&v"(t[7])
                       : "v"(acc[a][b][e]), "v"(acc[a][b][e + 1]), "v"(acc[a][b][e + 2]), "v"(acc[a][b][e + 3]), "v"(acc[a][b][e + 4]), "v"(acc[a][b][e + 5]),
                         "v"(acc[a][b][e + 6]), "v"(acc[a][b][e + 7]));
          SB;
          if (b < 2 && n < 2) MM6(b + 1, 2 * n + 1);
          SB;
#pragma unroll
          for (int j = 0; j < 4; ++j) o[j] = pk2(t[2 * j], t[2 * j + 1]);
#pragma unroll
          for (int j = 0; j < 4; ++j) o[j] = relu2(o[j]);
          ST4(o, v16, rws, ob + a * (112 * 1024u) + (2 * b + (n & 1)) * 1024u + (b == 2 ? sk : 0u), "sc1");
          SB;
        }
    } else {
#pragma unroll
      for (int j = 0; j < 12; ++j) MM6(1 + j / 6, j % 6);
      const unsigned ob = WS_PQ + (R0 * 28 + (c == 9 ? 22 : (c - 10) * 12 + wh * 6)) * 1024u;
      SB;
#pragma unroll
      for (int a = 0; a < 2; ++a) {
        const float rl = __builtin_amdgcn_rcpf(bfhi(st[a])) * LOG2E;
#pragma unroll
        for (int n = 0; n < 6; ++n) {
          f32x2 u[4], pe[4]; u32x4 o;
#pragma unroll
          for (int j = 0; j < 4; ++j) u[j] = phi_u(acc[a][n >> 1][8 * (n & 1) + 2 * j], acc[a][n >> 1][8 * (n & 1) + 2 * j + 1], (f32x2){rl, rl});
          SB; PHI_E(0); PHI_E(2); PHI_B(o);
          ST4(o, v16, rws, ob + a * (28 * 1024u) + n * 1024u + (n < 2 ? sk : 0u), "sc1");
          SB;
        }
      }
    }
  } else {
    const unsigned xa = l31 * 128 + ((hh ^ ((l31 >> 1) & 7)) << 4);
    const int ok_ = (c & 1) * 32 + 56 * cw + 8 * ((4 * blk + p + cw) & 7), ov_ = (c & 1) * 32 + 56 * cw + 32 * blk + 8 * p;
    const unsigned ka = 16384 + hh * 1024 + q * 256 + ((((ok_ >> 6) & 3) ^ q) << 6) + (ok_ & 63);
    const unsigned va = 32768 + hh * 1024 + q * 256 + ((((ov_ >> 6) & 3) ^ q) << 6) + (ov_ & 63) + ((lane & 19) == 19 ? 0x100000 : 0);
    f32x16 acc[4][2] = {{z16, z16}, {z16, z16}, {z16, z16}, {z16, z16}};
    bf16x8 X[4][4], W[4][2];
#define RDK(n, ks) do { \
      if ((n) == 1 || (n) == 5) { const unsigned a_ = ((n) == 1 ? a0_ : a1_) + (ks) * 2048; W[ks][(n) >> 2] = tr_frag(a_, a_ + 8192); } \
      else X[ks][(n) ? (n) - 1 : 0] = ld_frag(ax[ks] + ((n) ? (n) - 1 : 0) * 4096); } while (0)
#define MMK(ks, m) acc[(m) & 3][(m) >> 2] = mfma(X[ks][(m) & 3], W[ks][(m) >> 2], acc[(m) & 3][(m) >> 2])
    unsigned ax[4] = {xa, xa ^ 32u, xa ^ 64u, xa ^ 96u}, a0_ = ka, a1_ = va;
#pragma unroll
    for (int n = 0; n < 8; ++n) PIN(acc[n & 3][n >> 2]);
    lds_wait(SY_FULL, 4, 1, lane);
    if (r < 14) COLSUM((r & 1) ? va : ka, 8192, 2 * cw + (r & 1));
#pragma unroll
    for (int n = 0; n < 12; ++n) { RDK(n % 6, n / 6); SB; }
    unsigned tv = 1; int ts = 0;
    PIN(tv);
#pragma unroll 1
    for (;;) {
      unsigned fv; int dl;
#pragma unroll
      for (int m = 0; m < 16; ++m) {
        MMK(m >> 3, m & 7); SB;
        if ((m & 7) >= 1 && (m & 7) < 7) RDK((m & 7) - 1, 2 + (m >> 3));
        if (m == 0) { dl = ((0x24 >> ts) & 1) ? -98304 : 49152; ++ts; asm volatile("" : "+s"(dl), "+s"(ts)); }
        if (m == 7) { ax[0] += dl; ax[1] += dl; PIN(ax[0]); PIN(ax[1]); }
        if (m == 8) fv = *(volatile LDS unsigned*)fa;
        if (m == 14) *(volatile LDS unsigned*)da = tv;
        if (m == 15) { a0_ += dl; a1_ += dl; ++tv; CHECK_FULL(fv, tv); }
        SB;
      }
      if (__builtin_expect(ts == 7, 0)) break;
#pragma unroll
      for (int m = 0; m < 16; ++m) {
        MMK(2 + (m >> 3), m & 7); SB;
        if ((m & 7) >= 1 && (m & 7) < 7) RDK((m & 7) - 1, m >> 3);
        if (m == 7) { ax[2] += dl; ax[3] += dl; }
        SB;
      }
    }
    unsigned st[4]; u32x2 cv[2]; f32x4 rsv[2][4];
#pragma unroll
    for (int a = 0; a < 4; ++a) st[a] = *(LDS const unsigned*)(L_ST + (a * 32 + l31) * 4);
#pragma unroll
    for (int b = 0; b < 2; ++b) cv[b] = *(LDS const u32x2*)(L_CS + ((2 * cw + b) * 32 + l31) * 8);
    unsigned rsa = L_RS + hh * 16;
    PIN(rsa);
#define MK6(a, j) do { if ((j) < 4) MMK(2 + ((j) >> 1), (a) + 4 * ((j) & 1)); else acc[a][(j) & 1] = mfma(asbf((u32x4){((st[a] & 0xffffu) | (st[a] << 16)) & xm, st[a] & xm, 0u, 0u}), \
        asbf((u32x4){cv[(j) & 1].x, cv[(j) & 1].y, cv[(j) & 1].x, cv[(j) & 1].y}), acc[a][(j) & 1]); } while (0)
#pragma unroll
    for (int n = 0; n < 4; ++n) rsv[0][n] = *(LDS const f32x4*)(rsa + n * 32);
    SB;
#pragma unroll
    for (int j = 0; j < 6; ++j) MK6(0, j);
    SB;
    f32x16 kv = z16;
    u32x4 fk, fv;
#pragma unroll
    for (int gi = 0; gi < 8; ++gi) {
      const int a = gi >> 1, sg = gi & 1, e = 8 * sg;
      if (a < 3 && sg == 0) {
#pragma unroll
        for (int n = 0; n < 4; ++n) rsv[(a + 1) & 1][n] = *(LDS const f32x4*)(rsa + (a + 1) * 128 + n * 32);
      }
      const f32x4 ra_ = rsv[a & 1][2 * sg], rb_ = rsv[a & 1][2 * sg + 1];
      const f32x2 rl[4] = {{ra_[0], ra_[1]}, {ra_[2], ra_[3]}, {rb_[0], rb_[1]}, {rb_[2], rb_[3]}};
      f32x2 u[4], vv[4], pe[4];
#pragma unroll
      for (int j = 0; j < 4; ++j) u[j] = phi_u(acc[a][0][e + 2 * j], acc[a][0][e + 2 * j + 1], rl[j]);
#pragma unroll
      for (int j = 0; j < 4; ++j) vv[j] = (f32x2){acc[a][1][e + 2 * j], acc[a][1][e + 2 * j + 1]} * rl[j];
      SB;
      if (gi) kv = mfma(asbf(fk), asbf(fv), kv);
      SB;
      PHI_E(0);
      if (a < 3) MK6(a + 1, 3 * sg);
      SB;
      PHI_E(2);
      if (a < 3) MK6(a + 1, 3 * sg + 1);
      SB;
      fv = (u32x4){pk2(vv[0].x, vv[0].y), pk2(vv[1].x, vv[1].y), pk2(vv[2].x, vv[2].y), pk2(vv[3].x, vv[3].y)};
      SB;
      if (a < 3) MK6(a + 1, 3 * sg + 2);
      SB;
      PHI_B(fk);
      SB;
    }
    kv = mfma(asbf(fk), asbf(fv), kv);
    kv = kv * (l31 == 28 ? 1.f / 1.4453125f : 1.f / LOG2E);
    const unsigned ko = WS_KVP + (((c - 12) * 4 + cw) * 16 + r) * 4096u;
#pragma unroll
    for (int e = 0; e < 4; ++e) {
      const u32x4 o = {pk2(kv[4 * e], kv[4 * e + 1]), tag.x, pk2(kv[4 * e + 2], kv[4 * e + 3]), tag.x};
      ST4(o, v16, rws, ko + e * 1024u, "sc1");
    }
  }
  lds_put(da, 8);
  bf16x8 WH[9][4];
  unsigned ra[3][2];
#pragma unroll
  for (int cb = 0; cb < 2; ++cb) {
    ra[0][cb] = wh * 4096 + (4 * hh + q) * 128 + ((cb ^ (q >> 1)) << 6) + blk * 32 + p * 8; ra[1][cb] = ra[0][cb] + 65536; ra[2][cb] = ra[0][cb] + 131072;
    PIN(ra[0][cb]); PIN(ra[1][cb]); PIN(ra[2][cb]);
  }
#define RDW(dst, n, reg) do { const unsigned a_ = ra[(reg) >> 3][(n) & 1] + ((reg) & 7) * 8192 + ((n) >> 1) * 2048; dst[n] = tr_frag(a_, a_ + 1024); } while (0)
  if (!kvt) WAIT_VM(0);
  if (lane < 8) ST4(((u32x4){tag.x, tag.y, tag.x, tag.y}), v16, rws, fl_off, "sc1");
  if (!pure) {
    lds_wait(SY_FULL2, 4, 1, lane);
#pragma unroll
    for (int n = 0; n < 36; ++n) RDW(WH[n >> 2], n & 3, (16 + (n >> 2)) % 19);
    lds_put(da, 9);
  }

  if (!pure) {
    const int kh = wh, R = rt * 2 + wl;
    if (cw == 0) {
      const u64 bl = tag_wait(min(lane, 23) * FS, rws, WS_FL + rt * 32 * FS, 0x7FFFFull, tag);
      lds_put(SY_HOK, (bl & 0xF80000ull) == 0xF80000ull ? 2 : 1);
    } else lds_wait(SY_HOK, 1, 1, lane);
    WAIT_VM(0);
    const unsigned hs = WS_H + (R * 112 + kh * 56) * 1024u;
    constexpr int NH = NHW, E = 28 - NH;
    u32x4 hf[NH][2];
#define HLD(u) do { const unsigned o_ = hs + ROT(u) * 2048u; \
      LD4(hf[(u) % NH][0], v16, rws, o_, "sc1"); LD4(hf[(u) % NH][1], v16, rws, o_, "offset:1024 sc1"); } while (0)
#pragma unroll
    for (int u = 0; u < NH; ++u) HLD(u);
    f32x16 af[2] = {z16, z16};
    bf16x8 W[LA + 1][4];
#pragma unroll
    for (int u = 0; u < 28; ++u) {
      constexpr auto total = [](int u_) { return 2 * NH + 2 * (u_ < E ? u_ : E) + (u_ > E ? 3 : 0); };
      asm volatile("s_waitcnt vmcnt(%2)" : "+v"(hf[u % NH][0]), "+v"(hf[u % NH][1]) : "n"(total(u) - 2 - 2 * u) : "memory");
      if (u == 9 - LA) { lds_wait(SY_RDY, 4, 1, lane); }
#pragma unroll
      for (int n = 0; n < 4; ++n) {
        af[n & 1] = mfma(u < 9 ? WH[u < 9 ? u : 0][n] : W[u % (LA + 1)][n], asbf(hf[u % NH][n >> 1]), af[n & 1]); SB;
        if (u + LA >= 9 && u + LA < 28) RDW(W[(u + LA) % (LA + 1)], n, (u + LA - 3) % 19);
        SB;
      }
      if (u == 16 - LA) lds_put(SY_DONE2 + cw * 4, 1);
      if (u == 20 - LA) lds_put(SY_DONE2 + cw * 4, 2);
      if (u < E) HLD(u + NH);
      else if (u == E) FIN_LD;
    }
    __builtin_amdgcn_s_setprio(0);
    lds_wait(SY_DONE2, 4, 2, lane);
    PUT_TILES(114688 + cw * 8192, af);
    WAIT_VM(0);
  }
  }
  if (pure) {
    const int id = xc * 4 + (ix >> 3), hd = id >> 1;
    lds_wait(SY_DONE, 4, 8, lane);
    f32x4 a, b;
#pragma unroll 1
    for (unsigned sp = 0; sp < SPIN_CAP; ++sp) {
      u32x4 pr[4];
#pragma unroll
      for (int n = 0; n < 4; ++n) LD4(pr[n], v16, rws, WS_KVP + (hd * 16 + 2 * w + (n >> 1)) * 4096u + (id & 1) * 2048u + (n & 1) * 1024u, "sc1");
      WAIT_VM(0);
      unsigned bad = 0;
#pragma unroll
      for (int n = 0; n < 4; ++n) { PIN(pr[n]); bad |= (pr[n].y ^ tag.x) | (pr[n].w ^ tag.x); }
#define F4(n) (f32x4){bflo(pr[n].x), bfhi(pr[n].x), bflo(pr[n].z), bfhi(pr[n].z)}
      a = F4(0) + F4(2); b = F4(1) + F4(3);
      if (__all(bad == 0)) break;
      __builtin_amdgcn_s_sleep(4);
    }
    *(LDS f32x4*)(w * 2048 + v16) = a; *(LDS f32x4*)(w * 2048 + 1024 + v16) = b;
    BARRIER;
    if (w == 4) {
      a = *(LDS const f32x4*)v16; b = *(LDS const f32x4*)(1024 + v16);
#pragma unroll
      for (int n = 1; n < 8; ++n) { a += *(LDS const f32x4*)(n * 2048 + v16); b += *(LDS const f32x4*)(n * 2048 + 1024 + v16); }
      u32x4 o = {pk2(a[0], a[1]), pk2(a[2], a[3]), pk2(b[0], b[1]), pk2(b[2], b[3])};
      if ((id & 1) && hh != (hd & 1)) { if (hd & 2) { o.x = 0; o.y = 0; } else { o.z = 0; o.w = 0; } }
      ST4(o, v16, rws, WS_KV + id * 1024u, "sc1");
      WAIT_VM(0);
      ST4(((u32x4){tag.x, tag.y, tag.x, tag.y}), (lane >> 3) * 32 * FS + (lane & 7) * 16, rws, WS_KF + id * FS, "sc1");
    } else if (w == 5 && bid == 255) {
#pragma unroll 1
      for (int n = 0; n < 4; ++n) tag_wait((unsigned)((lane >> 4) * 64 + (lane & 15) * 2) * FS, rws, WS_FL + n * 256 * FS, ~0ull, tag);
      if (lane == 0) ST2(tag, 0u + lane, rws, WS_EP, "sc1");
    }
    return;
  }
  BARRIER;
  {
    const unsigned a0 = 49152 + (tid >> 8) * 8192 + (tid & 7) * 1024 + ((((tid >> 3) & 31) ^ ((tid & 7) * 2)) << 4);
    f32x4 f[2][2], sp[4][2];
#pragma unroll
    for (int n = 0; n < 2; ++n) { f[n][0] = *(LDS const f32x4*)(a0 + 65536 + n * 16384); f[n][1] = *(LDS const f32x4*)(a0 + 65536 + n * 16384 + 512); }
#pragma unroll
    for (int n = 0; n < 4; ++n) { sp[n][0] = *(LDS const f32x4*)(a0 + n * 16384); sp[n][1] = *(LDS const f32x4*)(a0 + n * 16384 + 512); }
    SB;
    PIN(xr); PIN(br); PIN(sg_);
    const float rs = __builtin_amdgcn_rcpf(bfhi(sg_)), rs2 = rs * rs;
    u32x4 ov;
#pragma unroll
    for (int e = 0; e < 4; ++e) {
      const int hf_ = e >> 1, j = (e & 1) * 2;
      const float t0 = fmaf(f[0][hf_][j] + f[1][hf_][j], rs2, bflo(br[e])), t1 = fmaf(f[0][hf_][j + 1] + f[1][hf_][j + 1], rs2, bfhi(br[e]));
      const float g0 = t0 * __builtin_amdgcn_rcpf(1.f + __expf(-t0)), g1 = t1 * __builtin_amdgcn_rcpf(1.f + __expf(-t1));
      const float s0_ = (sp[0][hf_][j] + sp[1][hf_][j]) + (sp[2][hf_][j] + sp[3][hf_][j]);
      const float s1_ = (sp[0][hf_][j + 1] + sp[1][hf_][j + 1]) + (sp[2][hf_][j + 1] + sp[3][hf_][j + 1]);
      ov[e] = pk2(fmaf(s0_, g0, bflo(xr[e])), fmaf(s1_, g1, bfhi(xr[e])));
    }
    ST4(ov, fxo, mk_rsrc(out, S * D * 2), 0u, "sc0 sc1");
  }
}

extern "C" void kernel_launch(void* const* d_in, const int* in_sizes, int n_in, void* d_out, int out_size, void* d_ws,
                              size_t ws_size, hipStream_t stream) {
  (void)hipFuncSetAttribute((const void*)fused, hipFuncAttributeMaxDynamicSharedMemorySize, L_END);
  fused<<<256, 512, L_END, stream>>>((const u16*)d_in[0], (const u16*)d_in[1], (const u16*)d_in[4], (char*)d_ws, (const u16*)d_in[2],
                                         (const u16*)d_in[5], (const u16*)d_in[6], (const u16*)d_in[7], (u16*)d_out);
}
```

```cpp
#include <hip/hip_runtime.h>
#include <hip/hip_bf16.h>
#include <stdint.h>
#pragma clang diagnostic ignored "-Wint-to-pointer-cast"

#define HMOD "sc1"
#define PRIO_H 3
#define PRIO_C 2
#define LSLEEP 1
#define STW 99
#define PF0 2
#define PFN 0
#define FILLAT 8
#define THR_N 0
#define THR_V 4
#define NHW 7
#define TSLEEP 4
#define TOUCH 0
#define S0MODE 1
#define FS 128u
#define POLL_LATE 0
#define TCH2 5
#define PRIO_S 1
#define RSLEEP 4
#define NX 9
#define PFA 9
#define PRIO_P 0
#define PRIO_W 0
#define EARLY 0
#define CTROT 1
#define ROTM 1
#define LA 2
#define NOKV 0
#define PRIO_M 1
#define PRIO_K 3
#define PFE 4
#define PD 0
#define PMASK 2
#define PD2 0
#define PMASK2 12
#define LNOP 4
#define WD 0
#define LFROM 0
#define LTO 4
#define LNA 4
#define LTA 4
#define KNA 0
#define TA1 2
#define TA0 4
#define TB1 4
#define TB0 4
#define FMASK 0
#define TF 1
#define TQ 4
typedef unsigned short u16;
typedef unsigned long long u64;
typedef __bf16 bf16x8 __attribute__((ext_vector_type(8)));
typedef __bf16 bf16x2 __attribute__((ext_vector_type(2)));
typedef short s16x4 __attribute__((ext_vector_type(4)));
typedef float f32x4 __attribute__((ext_vector_type(4)));
typedef float f32x16 __attribute__((ext_vector_type(16)));
typedef float f32x2 __attribute__((ext_vector_type(2)));
typedef unsigned u32x4 __attribute__((ext_vector_type(4)));
typedef unsigned u32x2 __attribute__((ext_vector_type(2)));
#define LDS __attribute__((address_space(3)))
#define DI __device__ __forceinline__

constexpr int S = 2048, D = 448, F = 1792;
constexpr unsigned WS_EP = 0;
constexpr unsigned WS_KF = 1024;
constexpr unsigned WS_FL = WS_KF + 256 * FS;
constexpr unsigned WS_ST = WS_FL + 1024 * FS;
constexpr unsigned WS_CS = WS_ST + 32768;
constexpr unsigned WS_KV = WS_CS + 16 * 7 * 2048;
constexpr unsigned WS_KVP = WS_KV + 32768;
constexpr unsigned WS_PQ = WS_KVP + 16 * 16 * 4096;
constexpr unsigned WS_H = WS_PQ + 64 * 28 * 1024;
constexpr unsigned WS_END = WS_H + 64 * 112 * 1024;
constexpr unsigned L_CS = 155648, L_ST = L_CS + 2048, L_RS = L_ST + 512, L_SY = L_RS + 512, L_END = L_SY + 256;
constexpr unsigned SY_FULL = L_SY, SY_AUX = L_SY + 16, SY_DONE = L_SY + 32, SY_RDY = L_SY + 48, SY_FULL2 = L_SY + 64, SY_DONE2 = L_SY + 80,
                   SY_GO = L_SY + 112, SY_WOK = L_SY + 128, SY_KOK = L_SY + 96, SY_HOK = L_SY + 100, SY_TAG = L_SY + 104;
constexpr unsigned CODE_BYTES = 33024;
constexpr unsigned SPIN_CAP = 1u << 17;

#define LDS_BYTES L_END
#define TSW(n, wv)
#define TSC(n, wv)
#define TS_TOP
#define TS_INIT
#define TS_DUMP

DI u32x4 mk_rsrc(const void* p, unsigned bytes) {
  uint64_t a = (uint64_t)p;
  u32x4 r; r.x = (unsigned)a; r.y = (unsigned)(a >> 32) & 0xffffu; r.z = bytes; r.w = 0x00020000u; return r;
}
DI void dma16(unsigned lds_addr, unsigned voff, u32x4 rsrc, unsigned soff) {
  asm volatile("s_mov_b32 m0, %0\n\ts_nop 0\n\tbuffer_load_dwordx4 %1, %2, %3 offen lds"
               :: "s"(lds_addr), "v"(voff), "s"(rsrc), "s"(soff) : "memory");
}
template <unsigned OFF> DI void dma16o(unsigned lds_base, unsigned voff, u32x4 rsrc, unsigned soff) {
  asm volatile("s_add_u32 m0, %0, %4\n\ts_nop 0\n\tbuffer_load_dwordx4 %1, %2, %3 offen lds"
               :: "s"(lds_base), "v"(voff), "s"(rsrc), "s"(soff), "n"(OFF) : "memory", "scc");
}
#define SB __builtin_amdgcn_sched_barrier(0)
#define FENCE asm volatile("" ::: "memory")
#define PIN(a) asm volatile("" : "+v"(a))
#define WAIT_VM(n) asm volatile("s_waitcnt vmcnt(%0)" :: "n"(n) : "memory")
#define WAIT_LDS asm volatile("s_waitcnt lgkmcnt(0)" ::: "memory")
#define BARRIER asm volatile("s_waitcnt lgkmcnt(0)\n\ts_barrier" ::: "memory")
#define LD4(dst, voff, rsrc, soff, mod) asm volatile("buffer_load_dwordx4 %0, %1, %2, %3 offen " mod : "=v"(dst) : "v"(voff), "s"(rsrc), "s"(soff) : "memory")
#define LD2(dst, voff, rsrc, soff, mod) asm volatile("buffer_load_dwordx2 %0, %1, %2, %3 offen " mod : "=v"(dst) : "v"(voff), "s"(rsrc), "s"(soff) : "memory")
#define LD1(dst, voff, rsrc, soff, mod) asm volatile("buffer_load_dword %0, %1, %2, %3 offen " mod : "=v"(dst) : "v"(voff), "s"(rsrc), "s"(soff) : "memory")
#define ST4(src, voff, rsrc, soff, mod) asm volatile("buffer_store_dwordx4 %0, %1, %2, %3 offen " mod "\n\ts_nop 1" :: "v"(src), "v"(voff), "s"(rsrc), "s"(soff) : "memory")
#define ST2(src, voff, rsrc, soff, mod) asm volatile("buffer_store_dwordx2 %0, %1, %2, %3 offen " mod :: "v"(src), "v"(voff), "s"(rsrc), "s"(soff) : "memory")
DI f32x4 mfma16(bf16x8 a, bf16x8 b, f32x4 c) { return __builtin_amdgcn_mfma_f32_16x16x32_bf16(a, b, c, 0, 0, 0); }
DI f32x16 mfma(bf16x8 a, bf16x8 b, f32x16 c) { return __builtin_amdgcn_mfma_f32_32x32x16_bf16(a, b, c, 0, 0, 0); }
DI bf16x8 asbf(u32x4 v) { return __builtin_bit_cast(bf16x8, v); }
DI bf16x8 ld_frag(unsigned a) { return *(LDS const bf16x8*)a; }
DI bf16x8 tr_frag(unsigned a0, unsigned a1) {
  s16x4 lo = __builtin_amdgcn_ds_read_tr16_b64_v4i16((LDS s16x4*)a0);
  s16x4 hi = __builtin_amdgcn_ds_read_tr16_b64_v4i16((LDS s16x4*)a1);
  return __builtin_bit_cast(bf16x8, __builtin_shufflevector(lo, hi, 0, 1, 2, 3, 4, 5, 6, 7));
}
DI unsigned pk2(float a, float b) { f32x2 v = {a, b}; return __builtin_bit_cast(unsigned, __builtin_convertvector(v, bf16x2)); }
DI float bflo(unsigned u) { return __uint_as_float(u << 16); }
DI float bfhi(unsigned u) { return __uint_as_float(u & 0xffff0000u); }
DI float rbf(float f) { return bflo(pk2(f, 0.f)); }
constexpr float LOG2E = 1.44269504f, EPS2 = 1e-6f * LOG2E;
DI f32x2 phi_u(float a0, float a1, f32x2 rl) { return __builtin_elementwise_fma((f32x2){a0, a1}, rl, (f32x2){EPS2, EPS2}); }
#define PHI_E(j0) _Pragma("unroll") for (int j_ = (j0); j_ < (j0) + 2; ++j_) pe[j_] = (f32x2){__builtin_amdgcn_fmed3f(__builtin_amdgcn_exp2f(u[j_].x), 0.f, 1.f), \
                                                                                     __builtin_amdgcn_fmed3f(__builtin_amdgcn_exp2f(u[j_].y), 0.f, 1.f)}; SB
#define PHI_B(o) _Pragma("unroll") for (int j_ = 0; j_ < 4; ++j_) u[j_] = (f32x2){fmaxf(u[j_].x, EPS2), fmaxf(u[j_].y, EPS2)}; SB; \
              _Pragma("unroll") for (int j_ = 0; j_ < 4; ++j_) u[j_] = __builtin_elementwise_fma(u[j_], (f32x2){1.f / LOG2E, 1.f / LOG2E}, pe[j_]); SB; \
              o = (u32x4){pk2(u[0].x, u[0].y), pk2(u[1].x, u[1].y), pk2(u[2].x, u[2].y), pk2(u[3].x, u[3].y)}
DI unsigned relu2(unsigned v) {
  typedef short s16x2 __attribute__((ext_vector_type(2)));
  return __builtin_bit_cast(unsigned, __builtin_elementwise_max(__builtin_bit_cast(s16x2, v), (s16x2){0, 0}));
}
DI float mul1(float a, float b) { float r; asm("v_mul_f32_e32 %0, %1, %2" : "=v"(r) : "v"(a), "v"(b)); return r; }
DI float sum8(float v) {
  v += __uint_as_float(__builtin_amdgcn_update_dpp(0u, __float_as_uint(v), 0xB1, 0xf, 0xf, true));
  v += __uint_as_float(__builtin_amdgcn_update_dpp(0u, __float_as_uint(v), 0x4E, 0xf, 0xf, true));
  v += __uint_as_float(__builtin_amdgcn_update_dpp(0u, __float_as_uint(v), 0x141, 0xf, 0xf, true));
  return v;
}
DI void lds_put(unsigned a, unsigned v) { FENCE; *(volatile LDS unsigned*)a = v; FENCE; }
DI void lds_wait(unsigned a, int n, unsigned need, int lane) {
  const unsigned ad = a + (lane & (n - 1)) * 4;
#pragma unroll 1
  for (unsigned sp = 0; sp < SPIN_CAP; ++sp) {
    FENCE; const unsigned v = *(volatile LDS unsigned*)ad; FENCE;
    if (__all(v >= need)) break;
    __builtin_amdgcn_s_sleep(LSLEEP);
  }
}
DI u64 tag_wait(unsigned voff, u32x4 rs, unsigned soff, u64 mask, u32x2 tag) {
  u64 bl = 0;
#pragma unroll 1
  for (unsigned sp = 0; sp < SPIN_CAP; ++sp) {
    u32x2 v;
    asm volatile("buffer_load_dwordx2 %0, %1, %2, %3 offen sc1\n\ts_waitcnt vmcnt(0)" : "=&v"(v) : "v"(voff), "s"(rs), "s"(soff) : "memory");
    bl = __ballot(v.x == tag.x && v.y == tag.y);
    if ((bl & mask) == mask) break;
    __builtin_amdgcn_s_sleep(TSLEEP);
  }
  return bl;
}

__global__ __launch_bounds__(512, 2) void fused(const u16* __restrict__ x, const u16* __restrict__ wqkv, const u16* __restrict__ w1, char* ws,
                                                const u16* __restrict__ wo, const u16* __restrict__ b1, const u16* __restrict__ w2,
                                                const u16* __restrict__ b2, u16* __restrict__ out) {
  TS_TOP;
  const int bid = blockIdx.x, tid = threadIdx.x, lane = tid & 63;
  unsigned cpf;
  {
    u64 pc; asm volatile("s_getpc_b64 %0" : "=s"(pc));
    LD1(cpf, (unsigned)(tid >> 6) * 4096u + (unsigned)lane * 32768u, mk_rsrc((const void*)(pc & ~255ull), CODE_BYTES), (unsigned)(bid >> 3) * 128u, "");
  }
  const int w = __builtin_amdgcn_readfirstlane(tid >> 6);
  const int xc = bid & 7, ix = bid >> 3, sl = ix & 7, rl = ix >> 3;
  const int r = (xc >> 1) * 4 + rl, s0 = r >= 14 ? 0 : r >> 1;
  const u32x4 rx = mk_rsrc(x, S * D * 2);
  const unsigned vx0 = (unsigned)(r * 128 + (w & 1) * 64 + (lane >> 3)) * 896u + (unsigned)(((lane & 7) ^ (lane >> 4)) << 4), vx1 = (vx0 ^ 64u) + 7168u;
#define XGROUP(n) do { dma16o<(n) * 2048>(lx, vx0, rx, sg * 128 + (n) * 14336); dma16o<(n) * 2048 + 1024>(lx, vx1, rx, sg * 128 + (n) * 14336); } while (0)
  asm volatile("" :: "s"(wqkv), "s"(w1), "s"(ws), "s"(wo), "s"(w2), "s"(b1), "s"(b2), "s"(out), "v"(vx0), "v"(vx1));
  if (w < 2) { const unsigned lx = w * 8192; const int sg = s0; XGROUP(0); XGROUP(1); XGROUP(2); XGROUP(3); }
  const int c = (int)(((xc & 1) ? 0xEFB98765u : 0xDCA43210u) >> (4 * sl)) & 15;
  const bool kvt = sl >= 6, pure = sl == 7;
  const int rt = xc * 4 + rl, ct = sl + 2 * rl - (sl + 2 * rl >= 7 ? 7 : 0), rot2 = 7 * rl + xc - (7 * rl + xc >= 28 ? 28 : 0);
  const int l31 = lane & 31, hh = lane >> 5, q = (lane >> 2) & 3, p = lane & 3, blk = (lane >> 4) & 1;
  const unsigned slot_b = kvt ? 49152u : 40960u;
  const u32x4 rws = mk_rsrc(ws, WS_END);
  const unsigned v16 = lane * 16, v8 = lane * 8;
  TS_INIT;
  u32x2 tag;
  const unsigned swz = (unsigned)(((lane & 7) ^ ((lane >> 2) & 4)) << 4);
  f32x16 z16;
#pragma unroll
  for (int e = 0; e < 16; ++e) z16[e] = 0.f;

#define PUT_TILES(base, t) do { _Pragma("unroll") for (int n_ = 0; n_ < 8; ++n_) *(LDS f32x4*)((base) + n_ * 1024 + (v16 ^ (n_ * 32))) = \
      (f32x4){t[n_ >> 2][4 * (n_ & 3)], t[n_ >> 2][4 * (n_ & 3) + 1], t[n_ >> 2][4 * (n_ & 3) + 2], t[n_ >> 2][4 * (n_ & 3) + 3]}; } while (0)
  if (__builtin_expect(w < 4, 1)) {
    const int h = w, hp = h >> 1, hs_ = h & 1;
    const u32x4 rw2 = mk_rsrc(w2, F * D * 2), rwo = mk_rsrc(wo, D * D * 2);
    const unsigned vwo = (unsigned)(lane >> 3) * 896u + ct * 128 + swz;
    const unsigned vw2 = vwo + (unsigned)((h >> 1) * 896 + (h & 1) * 16) * 896u;
    const int hg = (h + xc + rl) & 3;
    u32x4 R[18];
#define ROT(u) ((u) + rot2 >= 28 ? (u) + rot2 - 28 : (u) + rot2)
#define PFLD(n) LD4(R[n], vw2, rw2, ROT(19 + ((n) >> 1)) * 28672u + ((n) & 1) * 7168u, "")
    auto fill = [&](int u, int g, int cnt) {
#pragma unroll 1
      for (int e = u + cnt; u < e; ++u, ++g) {
        const unsigned k = ROT(u) * 28672u;
        dma16(g * 8192 + h * 2048, vw2, rw2, k); dma16(g * 8192 + h * 2048 + 1024, vw2, rw2, k + 7168u);
      }
    };
    {
      __builtin_amdgcn_s_setprio(PRIO_H);
      const bool a1 = c < 10, b1_ = c < 9;
      const u32x4 r0 = mk_rsrc(a1 ? w1 : wqkv, a1 ? D * F * 2 : D * 3 * D * 2), r1 = mk_rsrc(b1_ ? w1 : wqkv, b1_ ? D * F * 2 : D * 3 * D * 2);
      const unsigned p0 = a1 ? 3584u : 2688u, p1 = b1_ ? 3584u : 2688u;
      unsigned V0, V1, V2, V3;
      {
        const unsigned kl = 32 * hs_ + (lane >> 3);
        const unsigned c0 = (a1 ? c : c - 10) * 384, c1 = c == 9 ? 512 : c0;
        const unsigned vk = (32 * hs_ + (lane >> 4)) * 2688 + ((224 * c - 1792) & ~63) + ((q ^ (lane >> 4)) << 6) + p * 16;
        V0 = kvt ? vk : kl * p0 + c0 + swz;
        V1 = kvt ? vk + 4 * 2688 : kl * p1 + c1 + swz + 128;
        V2 = kvt ? V0 + 896 : V1 + 128;
        V3 = V1 + 896;
      }
      int sg = (S0MODE ? s0 : 0) + hp; sg = sg == 7 ? 0 : sg;
      unsigned so = hp * slot_b;
#define GROUP(n) do { const unsigned kk = sg * 64 + 8 * (n), sa = kk * p0, sb_ = kk * p1; \
          if (i || hp) XGROUP(n); \
          dma16o<16384 + (n) * 1024>(lw, V0, r0, sa); \
          dma16o<24576 + (n) * 1024>(lw, V1, r1, sb_); \
          dma16o<32768 + (n) * 1024>(lw, V2, r1, sb_); \
          if (kvt) dma16o<40960 + (n) * 1024>(lw, V3, r1, sb_); } while (0)
      if (hp) BARRIER;
      unsigned t2;
#pragma unroll
      for (int i = 0; i < 4; ++i) {
        const int t = hp + 2 * i;
        if (i == 3 && hp) break;
        if (i >= 2 || (i == 1 && hp)) lds_wait(SY_DONE, 4, t - 2, lane);
        TSW(32 + t, 2 * hp);
        const unsigned lx = so + hs_ * 8192, lw = so + hs_ * 4096;
        GROUP(0); GROUP(1); GROUP(2); GROUP(3);
        TSW(40 + t, 2 * hp);
        if (i == 0 && !hp) BARRIER;
        if (i == 2 && hp) break;
        if (i == 3) lds_put(SY_GO, 1);
        if (i == 1) {
          LD1(t2, lane < (hp ? 0 : hs_ ? 13 : 49) ? lane * 128u : 0x80000000u, hs_ ? rwo : rw2, bid * (hs_ ? 1664u : 6272u), "");
          WAIT_VM(1);
        } else WAIT_VM(0);
        if (i == 0) asm volatile("" :: "v"(cpf));
        if (i == 2) asm volatile("" :: "v"(t2));
        lds_put(SY_FULL + h * 4, t + 2);
        TSW(48 + t, 2 * hp);
        if (!pure && ((i == 1 && hp) || i == 2)) {
#pragma unroll
          for (int n = 0; n < (i == 2 ? PFE : PFA); ++n) PFLD(n);
        }
        so = so == 0 ? 2 * slot_b : so - slot_b;
        sg = sg >= 5 ? sg - 5 : sg + 2;
      }
      tag = *(volatile LDS u32x2*)SY_TAG;
      if (hp) {
        u32x4 xf[8];
        unsigned bi;
        const bool chk = !hs_ || kvt || lane < 32;
        const u32x4 rb1 = mk_rsrc(b1, c < 10 ? F * 2 : 0);
#pragma unroll 1
        for (unsigned sp = 0; sp < SPIN_CAP; ++sp) {
#pragma unroll
          for (int k = 0; k < 7; ++k) LD4(xf[k], v16, rws, WS_CS + c * 14336u + hs_ * 1024u + k * 2048u, "sc1");
          LD4(xf[7], v16, rws, WS_ST + r * 2048u + hs_ * 1024u, "sc1");
          LD1(bi, lane * 4u, rb1, c * 384u + hs_ * 256u, "");
          WAIT_VM(9); lds_put(SY_FULL + h * 4, 7);
          TSW(53, 2);
          WAIT_VM(0);
          PIN(xf[0]); PIN(xf[7]); PIN(bi);
          if (kvt && l31 == 30) bi = 0x3f80u;
          unsigned bad = (xf[0].y ^ tag.x) | (xf[0].w ^ tag.x);
          float ax_ = __uint_as_float(xf[0].x), ay_ = __uint_as_float(xf[0].z);
#pragma unroll
          for (int k = 1; k < 7; ++k) {
            PIN(xf[k]);
            ax_ += __uint_as_float(xf[k].x); ay_ += __uint_as_float(xf[k].z); PIN(ax_); PIN(ay_);
            bad |= (xf[k].y ^ tag.x) | (xf[k].w ^ tag.x); PIN(bad);
          }
          const unsigned hi = pk2(ax_, ay_); ax_ -= bflo(hi); PIN(ax_); ay_ -= bfhi(hi);
          const unsigned mi = pk2(ax_, ay_); ax_ -= bflo(mi); PIN(ax_); ay_ -= bfhi(mi);
          const unsigned lo = pk2(ax_, ay_);
          *(LDS u32x4*)(L_CS + hs_ * 1024 + v16) = (u32x4){(hi & 0xffffu) | (mi << 16), (lo & 0xffffu) | (bi << 16),
                                                           (hi >> 16) | (mi & 0xffff0000u), (lo >> 16) | (bi & 0xffff0000u)};
          *(LDS unsigned*)(L_ST + hs_ * 256 + lane * 4) = xf[7].x;
          *(LDS float*)(L_RS + hs_ * 256 + lane * 4) = __builtin_amdgcn_rcpf(bfhi(xf[7].x)) * LOG2E;
          if (__builtin_expect(__all((bad == 0 || !chk) && xf[7].y == tag.x), 1)) break;
          __builtin_amdgcn_s_sleep(8);
        }
        lds_put(SY_FULL + h * 4, 8);
        TSW(3, 2);
        if (!pure) {
#pragma unroll
          for (int n = PFA; n < 18; ++n) PFLD(n);
        }
        lds_wait(SY_GO, 1, 1, lane);
        asm volatile("" :: "v"(t2));
      }
      __builtin_amdgcn_s_setprio(PRIO_P);
    }
    if (!pure) {
#pragma unroll
    for (int n = PFE; n < 18; ++n) { if (h < 2) PFLD(n); }
    const unsigned wb = 49152 + (h ^ 2) * 14336;
    lds_wait(SY_DONE, 4, h < 2 ? 6 : 5, lane);
#pragma unroll 1
    for (int n = 0; n < 14; ++n) dma16(wb + n * 1024, vwo, rwo, hg * 100352u + n * 7168u);
    lds_wait(SY_DONE, 4, 6, lane);
    fill(0, 16, 3);
    TSW(23, 0);
    WAIT_VM(6);
    bf16x8 WO[4][2][2];
    {
      const unsigned a0 = wb + (4 * hh + q) * 128 + (((q >> 1) & 1) << 6) + blk * 32 + p * 8, a1_ = a0 ^ 64u;
#pragma unroll
      for (int n = 0; n < 4; ++n)
#pragma unroll
        for (int sg = 0; sg < 2; ++sg) {
          const unsigned o = n * 3584 + sg * 2048;
          WO[n][sg][0] = tr_frag(a0 + o, a0 + o + 1024); WO[n][sg][1] = tr_frag(a1_ + o, a1_ + o + 1024);
        }
    }
#pragma unroll
    for (int n = 0; n < 8; ++n) {
      u32x4 t = __builtin_bit_cast(u32x4, WO[n >> 1][1][n & 1]);
      if (hh) { t.z = 0; t.w = 0; }
      WO[n >> 1][1][n & 1] = asbf(t);
    }
#pragma unroll
    for (int n = 0; n < 16; ++n) PIN(WO[n >> 2][(n >> 1) & 1][n & 1]);
    lds_put(SY_WOK + h * 4, 1);
    TSW(29, 0);
    if (EARLY) fill(16, 13, 3);
    lds_wait(SY_DONE, 4, c < 10 ? FILLAT : 7, lane);
    fill(3, 0, 6);
    lds_wait(SY_WOK, 4, 1, lane);
    fill(9, 6, 3);
    WAIT_VM(6); lds_put(SY_FULL2 + h * 4, 1);
    TSW(31, 0);
    fill(12, 9, EARLY ? 4 : 7);
    TSW(20, 0);
    lds_wait(SY_DONE, 4, 9, lane);
#pragma unroll
    for (int n = 0; n < 18; ++n) { PIN(R[n]); *(LDS u32x4*)((n < 6 ? 131072 : -24576) + h * 2048 + v16 + (n >> 1) * 8192 + (n & 1) * 1024) = R[n]; }
    WAIT_VM(0); lds_put(SY_RDY + h * 4, 1);
    TSW(4, 0);
    u32x4 PQ[2][7], KV[8];
    __builtin_amdgcn_s_setprio(PRIO_S);
    lds_wait(SY_HOK, 1, 1, lane);
    if (*(volatile LDS unsigned*)SY_HOK < 2u) tag_wait(min(lane, 4) * FS, rws, WS_FL + (rt * 32 + 19) * FS, ~0ull, tag);
#pragma unroll
    for (int n = 0; n < 14; ++n) LD4(PQ[n / 7][n % 7], v16, rws, WS_PQ + ((rt * 2 + n / 7) * 28 + hg * 7 + n % 7) * 1024u, "sc1");
    TSW(19, 0);
    __builtin_amdgcn_s_setprio(PRIO_K);
    tag_wait(min(lane, 7) * FS, rws, WS_KF + (xc * 32 + hg * 8) * FS, ~0ull, tag);
#pragma unroll
    for (int n = 0; n < 8; ++n) LD4(KV[n], v16, rws, WS_KV + hg * 8192u + n * 1024u, "sc1");
    __builtin_amdgcn_s_setprio(PRIO_M);
    TSW(5, 0);
    u32x4 PB[2][4];
#pragma unroll
    for (int n = 0; n < 14; ++n) PIN(PQ[n / 7][n % 7]);
#pragma unroll
    for (int u = 0; u < 8; ++u) {
      const u32x4* P = PQ[u >> 2]; const int n = u & 3, lo = n < 3 ? 2 * n + 1 : 5, hi = n ? 2 * n - 1 : 1;
      PB[u >> 2][n] = (u32x4){P[lo].x, P[lo].y, P[hi].z, P[hi].w}; PIN(PB[u >> 2][n]);
    }
    WAIT_VM(4);
    TSW(16, 0);
#pragma unroll
    for (int n = 0; n < 4; ++n) PIN(KV[n]);
    f32x16 nm[3], as[2][2] = {{z16, z16}, {z16, z16}};
    u32x4 O[2][2]; float rd[2];
#define NUM0(u) nm[(u) % 3] = mfma(asbf(KV[2 * ((u) & 3)]), asbf(PQ[(u) >> 2][2 * ((u) & 3)]), z16)
#define NUM1(u) nm[(u) % 3] = mfma(asbf(KV[2 * ((u) & 3) + 1]), asbf(PB[(u) >> 2][(u) & 3]), nm[(u) % 3])
#define DEN(u) __uint_as_float(__builtin_amdgcn_permlane32_swap(__float_as_uint(nm[(u) % 3][12]), __float_as_uint(nm[(u) % 3][12]), false, false)[1])
#define APP(u, i) as[(u) >> 2][(i) & 1] = mfma(WO[(u) & 3][(i) >> 1][(i) & 1], asbf(O[(u) & 1][(i) >> 1]), as[(u) >> 2][(i) & 1])
#define MULS(a, b) _Pragma("unroll") for (int e = (a); e < (b); ++e) o[e] = mul1(nm[u % 3][e], rd[u & 1])
#define CVTS(a, b) _Pragma("unroll") for (int e = (a); e < (b); ++e) O[u & 1][e >> 2][e & 3] = pk2(o[2 * e], o[2 * e + 1])
    NUM0(0); NUM1(0); NUM0(1); NUM1(1);
    rd[0] = __builtin_amdgcn_rcpf(DEN(0) + 1e-6f);
    asm volatile("s_waitcnt vmcnt(0)" : "+v"(rd[0]), "+v"(KV[4]), "+v"(KV[5]), "+v"(KV[6]), "+v"(KV[7]) :: "memory");
    SB;
    unsigned d2 = 0; bool put0 = false;
#pragma unroll
    for (int u = 0; u < 8; ++u) {
      float o[16], dn;
      if (u) APP(u - 1, 0); SB; MULS(0, 6); SB;
      if (u) APP(u - 1, 1); SB; MULS(6, 10); CVTS(0, 2); SB;
      if (u) APP(u - 1, 2); SB; MULS(10, 13); CVTS(2, 4); SB;
      if (u) APP(u - 1, 3); SB; MULS(13, 16); CVTS(4, 6); SB;
      if (u < 6) NUM0(u + 2); SB; if (u < 7) dn = DEN(u + 1) + 1e-6f; SB;
      if (u < 6) NUM1(u + 2); SB; if (u < 7) rd[(u + 1) & 1] = __builtin_amdgcn_rcpf(dn); SB; CVTS(6, 8);
      if (u == 4) d2 = *(volatile LDS unsigned*)(SY_DONE2 + p * 4);
      if (u == 5) { put0 = __all(d2 >= 1u); if (put0) PUT_TILES(49152 + h * 16384, as[0]); }
      SB;
    }
    APP(7, 0); APP(7, 2); APP(7, 1); APP(7, 3);
    TSW(21, 0);
    if (!put0) { lds_wait(SY_DONE2, 4, 1, lane); PUT_TILES(49152 + h * 16384, as[0]); }
    PUT_TILES(57344 + h * 16384, as[1]);
    TSW(17, 0);
    BARRIER;
    }
  } else {
  const int cw = w - 4, wl = cw & 1, wh = cw >> 1;
  u32x4 xs[7];
  if (cw == 3) {
    const unsigned va = (unsigned)(r * 128 + c * 8 + (lane >> 3)) * 896u + (lane & 7) * 16;
#pragma unroll
    for (int k = 0; k < 7; ++k) LD4(xs[k], va, rx, k * 128u, "");
  }
  *(LDS unsigned*)(L_SY + lane * 4) = (lane >> 1) == 1; BARRIER;
  __builtin_amdgcn_s_setprio(PRIO_C);
  bf16x8 ones = asbf((u32x4){0x3f803f80u, 0x3f803f80u, 0x3f803f80u, 0x3f803f80u});
  PIN(ones);
  {
    {
      u64 ep;
      asm volatile("s_load_dwordx2 %0, %1, 0x0\n\ts_waitcnt lgkmcnt(0) vmcnt(0)" : "=&s"(ep) : "s"(ws) : "memory");
      tag.x = (unsigned)ep + 1u; tag.y = (unsigned)(ep >> 32) + 1u;
    }
    asm volatile("" :: "v"(cpf));
    if (cw == 0) *(volatile LDS u32x2*)SY_TAG = tag;
    if (cw == 3) {
      float sq[2] = {0.f, 0.f}, sm[2] = {0.f, 0.f};
      const bf16x2 one2 = __builtin_bit_cast(bf16x2, 0x3f803f80u);
#pragma unroll
      for (int k = 0; k < 7; ++k) {
        PIN(xs[k]);
#pragma unroll
        for (int e = 0; e < 4; ++e) {
          const unsigned u = xs[k][e];
          const bf16x2 v = __builtin_bit_cast(bf16x2, u);
          sq[e & 1] = __builtin_amdgcn_fdot2_f32_bf16(v, v, sq[e & 1], false); sm[e & 1] = __builtin_amdgcn_fdot2_f32_bf16(v, one2, sm[e & 1], false);
        }
      }
      const float m = sum8(sm[0] + sm[1]) * (1.f / D);
      const float var = fmaxf(sum8(sq[0] + sq[1]) * (1.f / D) - m * m, 0.f);
      const unsigned sv = __builtin_amdgcn_ds_bpermute(lane * 32, pk2(-m, __builtin_amdgcn_sqrtf(rbf(rbf(var) + 1e-5f))));
      const u32x4 gr = {sv, tag.x, sv, tag.x};
      if (lane < 8) ST4(gr, v16, rws, WS_ST + (r * 128 + c * 8) * 16, "sc1");
    }
  }
  const unsigned fl_off = WS_FL + ((r * 2 + wl) * 32 + 2 * c + wh) * FS;
  const unsigned fa = SY_FULL + p * 4;
#define CHECK_FULL(v, need) do { if (__builtin_expect(__any((v) < (need)), 0)) lds_wait(SY_FULL, 4, need, lane); FENCE; } while (0)
#define RAMP do { if (WD && (sl & 1)) __builtin_amdgcn_s_sleep(WD); if (PD && ((PMASK >> cw) & 1)) __builtin_amdgcn_s_sleep(PD); if (PD2 && ((PMASK2 >> cw) & 1)) __builtin_amdgcn_s_sleep(PD2); } while (0)
#define COLSUM(a_, t_, tile) do { const unsigned a = (a_); bf16x8 f[4]; \
    _Pragma("unroll") for (int ks = 0; ks < 4; ++ks) f[ks] = tr_frag(a + ks * 2048, a + ks * 2048 + (t_)); \
    SB; f32x16 cs = z16; \
    _Pragma("unroll") for (int ks = 0; ks < 4; ++ks) cs = mfma(ones, f[ks], cs); \
    const u32x4 gr = {(unsigned)__builtin_amdgcn_ds_bpermute(lane * 8, __float_as_int(cs[0])), tag.x, (unsigned)__builtin_amdgcn_ds_bpermute(lane * 8 + 4, __float_as_int(cs[0])), tag.x}; \
    if (lane < 16) ST4(gr, v16, rws, WS_CS + (c * 7 + s0) * 2048 + (tile) * 256, "sc1"); } while (0)
  unsigned da = SY_DONE + cw * 4;
  PIN(da);
  const unsigned xm = hh ? 0u : ~0u;
  if (!kvt) {
    const unsigned xa = (wl * 64 + l31) * 128 + ((hh ^ ((l31 >> 1) & 7)) << 4);
    const unsigned wr = 16384 + (8 * hh + q) * 128 + blk * 32 + p * 8;
    const unsigned wa0 = wr + wh * 8192 + ((wh ^ (q >> 1)) << 6), wa1 = wr + wh * 16384 + ((wh ^ 1 ^ (q >> 1)) << 6);
    f32x16 acc[2][3] = {{z16, z16, z16}, {z16, z16, z16}};
    bf16x8 X[4][2], W[4][3];
#define RDA(n, ks) do { \
      if ((n) == 0 || (n) == 2) X[ks][(n) >> 1] = ld_frag(ax[ks] + ((n) >> 1) * 4096); \
      else { const unsigned a_ = ((n) == 3 ? a1_ : a0_) + ((n) == 4 ? 8192 : 0) + (ks) * 2048; W[ks][(n) == 1 ? 0 : (n) - 2] = tr_frag(a_, a_ + 512); } } while (0)
#define MMA(ks, m) acc[(m) & 1][(m) >> 1] = mfma(W[ks][(m) >> 1], X[ks][(m) & 1], acc[(m) & 1][(m) >> 1])
    unsigned ax[4] = {xa, xa ^ 32u, xa ^ 64u, xa ^ 96u}, a0_ = wa0, a1_ = wa1;
#pragma unroll
    for (int n = 0; n < 6; ++n) PIN(acc[n & 1][n >> 1]);
    lds_wait(SY_FULL, 4, 1, lane);
    RAMP;
    TSW(6, 4); TSC(24, 4);
    if (r < 14 && !((r & 1) && wl)) {
      const int b = (r & 1) ? 2 : wl;
      COLSUM(b == 1 ? wa1 : wa0 + b * 4096, 512, 3 * wh + b);
    }
#pragma unroll
    for (int n = 0; n < 10; ++n) { RDA(n % 5, n / 5); SB; }
    const int tla = c >= 10 ? 0 : (xc & 1) ? 2 : 4, tlb = c >= 10 ? 0 : 4;
    unsigned tv = 1; int ts = 0;
    PIN(tv);
#pragma unroll 1
    for (;;) {
      unsigned fv; int dl;
#pragma unroll
      for (int m = 0; m < 12; ++m) {
        MMA(m / 6, m % 6); SB;
        if (m % 6 < 5) RDA(m % 6, 2 + m / 6);
        if (m == 0) { dl = ((0x24 >> ts) & 1) ? -81920 : 40960; ++ts; asm volatile("" : "+s"(dl), "+s"(ts)); }
        if (m == 2) { ax[0] += dl; ax[1] += dl; PIN(ax[0]); PIN(ax[1]); }
        if (m == 5) { if (ts <= tla) __builtin_amdgcn_s_sleep(4); fv = *(volatile LDS unsigned*)fa; }
        if (m == 10) *(volatile LDS unsigned*)da = tv;
        if (m == 11) { a0_ += dl; a1_ += dl; ++tv; CHECK_FULL(fv, tv); TSW(55 + ts, STW); }
        SB;
      }
      if (__builtin_expect(ts == 7, 0)) break;
#pragma unroll
      for (int m = 0; m < 12; ++m) {
        MMA(2 + m / 6, m % 6); SB;
        if (m % 6 < 5) RDA(m % 6, m / 6);
        if (m == 5) { ax[2] += dl; ax[3] += dl; if (ts <= tlb) __builtin_amdgcn_s_sleep(4); }
        SB;
      }
    }
    TSW(7, 4); TSC(25, 4);
    unsigned st[2]; u32x2 cv[3];
#pragma unroll
    for (int a = 0; a < 2; ++a) st[a] = *(LDS const unsigned*)(L_ST + (wl * 64 + a * 32 + l31) * 4);
#pragma unroll
    for (int b = 0; b < 3; ++b) cv[b] = *(LDS const u32x2*)(L_CS + ((3 * wh + b) * 32 + l31) * 8);
    SB;
    MMA(2, 0); MMA(2, 1); MMA(3, 0); MMA(3, 1);
    SB;
    bf16x8 xe[2];
#pragma unroll
    for (int a = 0; a < 2; ++a) xe[a] = asbf((u32x4){((st[a] & 0xffffu) | (st[a] << 16)) & xm, st[a] & xm, 0u, 0u});
#define MME(m) acc[(m) & 1][(m) >> 1] = mfma(asbf((u32x4){cv[(m) >> 1].x, cv[(m) >> 1].y, cv[(m) >> 1].x, cv[(m) >> 1].y}), xe[(m) & 1], acc[(m) & 1][(m) >> 1])
    MME(0); MME(1);
    SB;
    TSW(18, 4);
    const int R0 = r * 4 + wl * 2;
    const unsigned sk = c == 9 ? 0x40000000u : 0u;
#define MM6(b, j) do { if ((j) < 4) MMA(2 + ((j) >> 1), 2 * (b) + ((j) & 1)); else MME(2 * (b) + ((j) & 1)); } while (0)
    if (2 * c + wh < 19) {
      const unsigned ob = WS_H + (R0 * 112 + 12 * c + 6 * wh) * 1024u;
#pragma unroll
      for (int b = 0; b < 3; ++b)
#pragma unroll
        for (int n = 0; n < 4; ++n) {
          const int a = n >> 1, e = 8 * (n & 1);
          float t[8]; u32x4 o;
          if (b < 2) MM6(b + 1, n < 2 ? 2 * n : n + 2);
          SB;
          if (n == 0) asm volatile("s_nop 10" : "+v"(acc[0][b]), "+v"(acc[1][b]));
          asm volatile("v_mul_f32_e64 %0, %8, |%8|\n\tv_mul_f32_e64 %1, %9, |%9|\n\tv_mul_f32_e64 %2, %10, |%10|\n\tv_mul_f32_e64 %3, %11, |%11|\n\t"
                       "v_mul_f32_e64 %4, %12, |%12|\n\tv_mul_f32_e64 %5, %13, |%13|\n\tv_mul_f32_e64 %6, %14, |%14|\n\tv_mul_f32_e64 %7, %15, |%15|"
                       : "=&v"(t[0]), "=&v"(t[1]), "=&v"(t[2]), "=&v"(t[3]), "=&v"(t[4]), "=&v"(t[5]), "=&v"(t[6]), "=&v"(t[7])
                       : "v"(acc[a][b][e]), "v"(acc[a][b][e + 1]), "v"(acc[a][b][e + 2]), "v"(acc[a][b][e + 3]), "v"(acc[a][b][e + 4]), "v"(acc[a][b][e + 5]),
                         "v"(acc[a][b][e + 6]), "v"(acc[a][b][e + 7]));
          SB;
          if (b < 2 && n < 2) MM6(b + 1, 2 * n + 1);
          SB;
#pragma unroll
          for (int j = 0; j < 4; ++j) o[j] = pk2(t[2 * j], t[2 * j + 1]);
#pragma unroll
          for (int j = 0; j < 4; ++j) o[j] = relu2(o[j]);
          ST4(o, v16, rws, ob + a * (112 * 1024u) + (2 * b + (n & 1)) * 1024u + (b == 2 ? sk : 0u), HMOD);
          SB;
        }
    } else {
#pragma unroll
      for (int j = 0; j < 12; ++j) MM6(1 + j / 6, j % 6);
      const unsigned ob = WS_PQ + (R0 * 28 + (c == 9 ? 22 : (c - 10) * 12 + wh * 6)) * 1024u;
      SB;
#pragma unroll
      for (int a = 0; a < 2; ++a) {
        const float rl = __builtin_amdgcn_rcpf(bfhi(st[a])) * LOG2E;
#pragma unroll
        for (int n = 0; n < 6; ++n) {
          f32x2 u[4], pe[4]; u32x4 o;
#pragma unroll
          for (int j = 0; j < 4; ++j) u[j] = phi_u(acc[a][n >> 1][8 * (n & 1) + 2 * j], acc[a][n >> 1][8 * (n & 1) + 2 * j + 1], (f32x2){rl, rl});
          SB; PHI_E(0); PHI_E(2); PHI_B(o);
          ST4(o, v16, rws, ob + a * (28 * 1024u) + n * 1024u + (n < 2 ? sk : 0u), HMOD);
          SB;
        }
      }
    }
  } else {
    const unsigned xa = l31 * 128 + ((hh ^ ((l31 >> 1) & 7)) << 4);
    const int ok_ = (c & 1) * 32 + 56 * cw + 8 * ((4 * blk + p + cw) & 7), ov_ = (c & 1) * 32 + 56 * cw + 32 * blk + 8 * p;
    const unsigned ka = 16384 + hh * 1024 + q * 256 + ((((ok_ >> 6) & 3) ^ q) << 6) + (ok_ & 63);
    const unsigned va = 32768 + hh * 1024 + q * 256 + ((((ov_ >> 6) & 3) ^ q) << 6) + (ov_ & 63) + ((lane & 19) == 19 ? 0x100000 : 0);
    f32x16 acc[4][2] = {{z16, z16}, {z16, z16}, {z16, z16}, {z16, z16}};
    bf16x8 X[4][4], W[4][2];
#define RDK(n, ks) do { \
      if ((n) == 1 || (n) == 5) { const unsigned a_ = ((n) == 1 ? a0_ : a1_) + (ks) * 2048; W[ks][(n) >> 2] = tr_frag(a_, a_ + 8192); } \
      else X[ks][(n) ? (n) - 1 : 0] = ld_frag(ax[ks] + ((n) ? (n) - 1 : 0) * 4096); } while (0)
#define MMK(ks, m) acc[(m) & 3][(m) >> 2] = mfma(X[ks][(m) & 3], W[ks][(m) >> 2], acc[(m) & 3][(m) >> 2])
    unsigned ax[4] = {xa, xa ^ 32u, xa ^ 64u, xa ^ 96u}, a0_ = ka, a1_ = va;
#pragma unroll
    for (int n = 0; n < 8; ++n) PIN(acc[n & 3][n >> 2]);
    lds_wait(SY_FULL, 4, 1, lane);
    if (r < 14) COLSUM((r & 1) ? va : ka, 8192, 2 * cw + (r & 1));
#pragma unroll
    for (int n = 0; n < 12; ++n) { RDK(n % 6, n / 6); SB; }
    unsigned tv = 1; int ts = 0;
    PIN(tv);
#pragma unroll 1
    for (;;) {
      unsigned fv; int dl;
#pragma unroll
      for (int m = 0; m < 16; ++m) {
        MMK(m >> 3, m & 7); SB;
        if ((m & 7) >= 1 && (m & 7) < 7) RDK((m & 7) - 1, 2 + (m >> 3));
        if (m == 0) { dl = ((0x24 >> ts) & 1) ? -98304 : 49152; ++ts; asm volatile("" : "+s"(dl), "+s"(ts)); }
        if (m == 7) { ax[0] += dl; ax[1] += dl; PIN(ax[0]); PIN(ax[1]); }
        if (m == 8) { if (KNA && ts <= 1) __builtin_amdgcn_s_sleep(KNA); fv = *(volatile LDS unsigned*)fa; }
        if (m == 14) *(volatile LDS unsigned*)da = tv;
        if (m == 15) { a0_ += dl; a1_ += dl; ++tv; CHECK_FULL(fv, tv); TSW(55 + ts, STW); }
        SB;
      }
      if (__builtin_expect(ts == 7, 0)) break;
#pragma unroll
      for (int m = 0; m < 16; ++m) {
        MMK(2 + (m >> 3), m & 7); SB;
        if ((m & 7) >= 1 && (m & 7) < 7) RDK((m & 7) - 1, m >> 3);
        if (m == 7) { ax[2] += dl; ax[3] += dl; }
        SB;
      }
    }
    TSW(8, 4);
    unsigned st[4]; u32x2 cv[2]; f32x4 rsv[2][4];
#pragma unroll
    for (int a = 0; a < 4; ++a) st[a] = *(LDS const unsigned*)(L_ST + (a * 32 + l31) * 4);
#pragma unroll
    for (int b = 0; b < 2; ++b) cv[b] = *(LDS const u32x2*)(L_CS + ((2 * cw + b) * 32 + l31) * 8);
    unsigned rsa = L_RS + hh * 16;
    PIN(rsa);
#define MK6(a, j) do { if ((j) < 4) MMK(2 + ((j) >> 1), (a) + 4 * ((j) & 1)); else acc[a][(j) & 1] = mfma(asbf((u32x4){((st[a] & 0xffffu) | (st[a] << 16)) & xm, st[a] & xm, 0u, 0u}), \
        asbf((u32x4){cv[(j) & 1].x, cv[(j) & 1].y, cv[(j) & 1].x, cv[(j) & 1].y}), acc[a][(j) & 1]); } while (0)
#pragma unroll
    for (int n = 0; n < 4; ++n) rsv[0][n] = *(LDS const f32x4*)(rsa + n * 32);
    SB;
#pragma unroll
    for (int j = 0; j < 6; ++j) MK6(0, j);
    SB;
    f32x16 kv = z16;
    u32x4 fk, fv;
#pragma unroll
    for (int gi = 0; gi < 8; ++gi) {
      const int a = gi >> 1, sg = gi & 1, e = 8 * sg;
      if (a < 3 && sg == 0) {
#pragma unroll
        for (int n = 0; n < 4; ++n) rsv[(a + 1) & 1][n] = *(LDS const f32x4*)(rsa + (a + 1) * 128 + n * 32);
      }
      const f32x4 ra_ = rsv[a & 1][2 * sg], rb_ = rsv[a & 1][2 * sg + 1];
      const f32x2 rl[4] = {{ra_[0], ra_[1]}, {ra_[2], ra_[3]}, {rb_[0], rb_[1]}, {rb_[2], rb_[3]}};
      f32x2 u[4], vv[4], pe[4];
#pragma unroll
      for (int j = 0; j < 4; ++j) u[j] = phi_u(acc[a][0][e + 2 * j], acc[a][0][e + 2 * j + 1], rl[j]);
#pragma unroll
      for (int j = 0; j < 4; ++j) vv[j] = (f32x2){acc[a][1][e + 2 * j], acc[a][1][e + 2 * j + 1]} * rl[j];
      SB;
      if (gi) kv = mfma(asbf(fk), asbf(fv), kv);
      SB;
      PHI_E(0);
      if (a < 3) MK6(a + 1, 3 * sg);
      SB;
      PHI_E(2);
      if (a < 3) MK6(a + 1, 3 * sg + 1);
      SB;
      fv = (u32x4){pk2(vv[0].x, vv[0].y), pk2(vv[1].x, vv[1].y), pk2(vv[2].x, vv[2].y), pk2(vv[3].x, vv[3].y)};
      SB;
      if (a < 3) MK6(a + 1, 3 * sg + 2);
      SB;
      PHI_B(fk);
      SB;
    }
    kv = mfma(asbf(fk), asbf(fv), kv);
    kv = kv * (l31 == 28 ? 1.f / 1.4453125f : 1.f / LOG2E);
    const unsigned ko = WS_KVP + (((c - 12) * 4 + cw) * 16 + r) * 4096u;
#pragma unroll
    for (int e = 0; e < 4; ++e) {
      const u32x4 o = {pk2(kv[4 * e], kv[4 * e + 1]), tag.x, pk2(kv[4 * e + 2], kv[4 * e + 3]), tag.x};
      ST4(o, v16, rws, ko + e * 1024u, "sc1");
    }
  }
  lds_put(da, 8);
  TSW(22, 4);
  bf16x8 WH[9][4];
  unsigned ra[3][2];
#pragma unroll
  for (int cb = 0; cb < 2; ++cb) {
    ra[0][cb] = wh * 4096 + (4 * hh + q) * 128 + ((cb ^ (q >> 1)) << 6) + blk * 32 + p * 8; ra[1][cb] = ra[0][cb] + 65536; ra[2][cb] = ra[0][cb] + 131072;
    PIN(ra[0][cb]); PIN(ra[1][cb]); PIN(ra[2][cb]);
  }
#define RDW(dst, n, reg) do { const unsigned a_ = ra[(reg) >> 3][(n) & 1] + ((reg) & 7) * 8192 + ((n) >> 1) * 2048; dst[n] = tr_frag(a_, a_ + 1024); } while (0)
  if (!kvt) WAIT_VM(0);
  if (lane < 8) ST4(((u32x4){tag.x, tag.y, tag.x, tag.y}), v16, rws, fl_off, "sc1");
  TSW(9, 4);
  if (!pure) {
    lds_wait(SY_FULL2, 4, 1, lane);
#pragma unroll
    for (int n = 0; n < 36; ++n) RDW(WH[n >> 2], n & 3, (16 + (n >> 2)) % 19);
    lds_put(da, 9);
  }
  TSW(30, 4);

  if (!pure) {
    const int kh = wh, R = rt * 2 + wl;
    if (cw == 0) {
      const u64 bl = tag_wait(min(lane, 23) * FS, rws, WS_FL + rt * 32 * FS, 0x7FFFFull, tag);
      lds_put(SY_HOK, (bl & 0xF80000ull) == 0xF80000ull ? 2 : 1);
    } else lds_wait(SY_HOK, 1, 1, lane);
    WAIT_VM(0);
    TSW(12, 4);
    const unsigned hs = WS_H + (R * 112 + kh * 56) * 1024u;
    constexpr int NH = NHW, E = 28 - NH;
    u32x4 hf[NH][2];
#define HLD(u) do { const unsigned o_ = hs + ROT(u) * 2048u; \
      LD4(hf[(u) % NH][0], v16, rws, o_, "sc1"); LD4(hf[(u) % NH][1], v16, rws, o_, "offset:1024 sc1"); } while (0)
#pragma unroll
    for (int u = 0; u < NH; ++u) HLD(u);
    u32x4 xr[2], br; unsigned sg_[2];
    const unsigned fxo = (unsigned)(rt * 64 + cw * 8 + (lane >> 3)) * 896u + ct * 128 + (lane & 7) * 16;
#define FIN_LD do { LD4(xr[0], fxo, rx, 0u, ""); LD4(xr[1], fxo, rx, 28672u, ""); LD4(br, (lane & 7) * 16u, mk_rsrc(b2, D * 2), ct * 128u, ""); \
      LD1(sg_[0], (unsigned)(lane & ~7) * 2u, rws, WS_ST + rt * 1024u + cw * 128u, "sc1"); LD1(sg_[1], (unsigned)(lane & ~7) * 2u, rws, WS_ST + rt * 1024u + cw * 128u, "offset:512 sc1"); } while (0)
    f32x16 af[2] = {z16, z16};
    bf16x8 W[LA + 1][4];
    TSW(13, 4); TSC(26, 4);
#pragma unroll
    for (int u = 0; u < 28; ++u) {
      constexpr auto total = [](int u_) { return 2 * NH + 2 * (u_ < E ? u_ : E) + (u_ > E ? 5 : 0); };
      asm volatile("s_waitcnt vmcnt(%2)" : "+v"(hf[u % NH][0]), "+v"(hf[u % NH][1]) : "n"(total(u) - 2 - 2 * u) : "memory");
      if (u == 9 - LA) { lds_wait(SY_RDY, 4, 1, lane); TSW(28, 4); }
#pragma unroll
      for (int n = 0; n < 4; ++n) {
        af[n & 1] = mfma(u < 9 ? WH[u < 9 ? u : 0][n] : W[u % (LA + 1)][n], asbf(hf[u % NH][n >> 1]), af[n & 1]); SB;
        if (u + LA >= 9 && u + LA < 28) RDW(W[(u + LA) % (LA + 1)], n, (u + LA - 3) % 19);
        SB;
      }
      if (u == 16 - LA) lds_put(SY_DONE2 + cw * 4, 1);
      if (u == 20 - LA) lds_put(SY_DONE2 + cw * 4, 2);
      if (u < E) HLD(u + NH);
      else if (u == E) FIN_LD;
    }
    TSW(14, 4); TSC(27, 4);
    __builtin_amdgcn_s_setprio(0);
    lds_wait(SY_DONE2, 4, 2, lane);
    PUT_TILES(114688 + cw * 8192, af);
    lds_put(SY_DONE2 + cw * 4, 3);
    WAIT_VM(0);
    unsigned a0 = 49152 + (lane & 7) * 1024 + (((cw * 8 + (lane >> 3)) ^ ((lane & 7) * 2)) << 4), a1 = a0 + 65536;
    PIN(a0); PIN(a1); PIN(br);
    lds_wait(SY_DONE2, 4, 3, lane);
    float gt[2][8];
    {
      f32x4 f[2][2][2];
#pragma unroll
      for (int n = 0; n < 8; ++n) f[n >> 2][(n >> 1) & 1][n & 1] = *(LDS const f32x4*)(a1 + (n >> 2) * 8192 + ((n >> 1) & 1) * 16384 + (n & 1) * 512);
      SB;
#pragma unroll
      for (int a = 0; a < 2; ++a) {
        PIN(sg_[a]);
        const float rs = __builtin_amdgcn_rcpf(bfhi(sg_[a])), rs2 = rs * rs;
#pragma unroll
        for (int e = 0; e < 8; ++e) {
          float t = f[a][0][e >> 2][e & 3] + f[a][1][e >> 2][e & 3]; PIN(t);
          t = fmaf(t, rs2, (e & 1) ? bfhi(br[e >> 1]) : bflo(br[e >> 1])); PIN(t);
          gt[a][e] = t * __builtin_amdgcn_rcpf(1.f + __expf(-t)); PIN(gt[a][e]);
        }
      }
    }
    float xv[2][8];
#pragma unroll
    for (int n = 0; n < 16; ++n) { if (!(n & 7)) PIN(xr[n >> 3]); xv[n >> 3][n & 7] = (n & 1) ? bfhi(xr[n >> 3][(n & 7) >> 1]) : bflo(xr[n >> 3][(n & 7) >> 1]); PIN(xv[n >> 3][n & 7]); }
    BARRIER;
    TSW(15, 4);
    f32x4 sp[2][4][2];
#pragma unroll
    for (int n = 0; n < 16; ++n) sp[n >> 3][(n >> 1) & 3][n & 1] = *(LDS const f32x4*)(a0 + (n >> 3) * 8192 + ((n >> 1) & 3) * 16384 + (n & 1) * 512);
    SB;
#pragma unroll
    for (int a = 0; a < 2; ++a) {
      const f32x4 s0_ = (sp[a][0][0] + sp[a][1][0]) + (sp[a][2][0] + sp[a][3][0]), s1_ = (sp[a][0][1] + sp[a][1][1]) + (sp[a][2][1] + sp[a][3][1]);
      u32x4 ov;
#pragma unroll
      for (int e = 0; e < 4; ++e) {
        const f32x4 sv = e < 2 ? s0_ : s1_;
        ov[e] = pk2(fmaf(sv[2 * (e & 1)], gt[a][2 * e], xv[a][2 * e]), fmaf(sv[2 * (e & 1) + 1], gt[a][2 * e + 1], xv[a][2 * e + 1]));
      }
      ST4(ov, fxo, mk_rsrc(out, S * D * 2), a * 28672u, "sc0 sc1");
    }
    TSW(11, 4);
    if (w == 4) TS_DUMP;
  }
  }
  if (pure) {
    const int id = xc * 4 + (ix >> 3), hd = id >> 1;
    lds_wait(SY_DONE, 4, 8, lane);
    f32x4 a, b;
#pragma unroll 1
    for (unsigned sp = 0; sp < SPIN_CAP; ++sp) {
      u32x4 pr[4];
#pragma unroll
      for (int n = 0; n < 4; ++n) LD4(pr[n], v16, rws, WS_KVP + (hd * 16 + 2 * w + (n >> 1)) * 4096u + (id & 1) * 2048u + (n & 1) * 1024u, "sc1");
      WAIT_VM(0);
      unsigned bad = 0;
#pragma unroll
      for (int n = 0; n < 4; ++n) { PIN(pr[n]); bad |= (pr[n].y ^ tag.x) | (pr[n].w ^ tag.x); }
#define F4(n) (f32x4){bflo(pr[n].x), bfhi(pr[n].x), bflo(pr[n].z), bfhi(pr[n].z)}
      a = F4(0) + F4(2); b = F4(1) + F4(3);
      if (__all(bad == 0)) break;
      __builtin_amdgcn_s_sleep(RSLEEP);
    }
    TSW(19, 4);
    *(LDS f32x4*)(w * 2048 + v16) = a; *(LDS f32x4*)(w * 2048 + 1024 + v16) = b;
    BARRIER;
    TSW(20, 4);
    if (w == 4) {
      a = *(LDS const f32x4*)v16; b = *(LDS const f32x4*)(1024 + v16);
#pragma unroll
      for (int n = 1; n < 8; ++n) { a += *(LDS const f32x4*)(n * 2048 + v16); b += *(LDS const f32x4*)(n * 2048 + 1024 + v16); }
      u32x4 o = {pk2(a[0], a[1]), pk2(a[2], a[3]), pk2(b[0], b[1]), pk2(b[2], b[3])};
      if ((id & 1) && hh != (hd & 1)) { if (hd & 2) { o.x = 0; o.y = 0; } else { o.z = 0; o.w = 0; } }
      ST4(o, v16, rws, WS_KV + id * 1024u, "sc1");
      TSW(21, 4);
      WAIT_VM(0);
      ST4(((u32x4){tag.x, tag.y, tag.x, tag.y}), (lane >> 3) * 32 * FS + (lane & 7) * 16, rws, WS_KF + id * FS, "sc1");
      TSW(10, 4); TS_DUMP;
    } else if (w == 5 && bid == 255) {
#pragma unroll 1
      for (int n = 0; n < 4; ++n) tag_wait((unsigned)((lane >> 4) * 64 + (lane & 15) * 2) * FS, rws, WS_FL + n * 256 * FS, ~0ull, tag);
      if (lane == 0) ST2(tag, 0u + lane, rws, WS_EP, "sc1");
    }
  }
}

extern "C" void kernel_launch(void* const* d_in, const int* in_sizes, int n_in, void* d_out, int out_size, void* d_ws,
                              size_t ws_size, hipStream_t stream) {
  (void)hipFuncSetAttribute((const void*)fused, hipFuncAttributeMaxDynamicSharedMemorySize, LDS_BYTES);
  fused<<<256, 512, LDS_BYTES, stream>>>((const u16*)d_in[0], (const u16*)d_in[1], (const u16*)d_in[4], (char*)d_ws, (const u16*)d_in[2],
                                         (const u16*)d_in[5], (const u16*)d_in[6], (const u16*)d_in[7], (u16*)d_out);
}
```

```cpp
#include <hip/hip_runtime.h>
#include <hip/hip_bf16.h>
#include <stdint.h>
#pragma clang diagnostic ignored "-Wint-to-pointer-cast"

typedef unsigned short u16;
typedef unsigned long long u64;
typedef __bf16 bf16x8 __attribute__((ext_vector_type(8)));
typedef __bf16 bf16x2 __attribute__((ext_vector_type(2)));
typedef short s16x4 __attribute__((ext_vector_type(4)));
typedef float f32x4 __attribute__((ext_vector_type(4)));
typedef float f32x16 __attribute__((ext_vector_type(16)));
typedef float f32x2 __attribute__((ext_vector_type(2)));
typedef unsigned u32x4 __attribute__((ext_vector_type(4)));
typedef unsigned u32x2 __attribute__((ext_vector_type(2)));
#define LDS __attribute__((address_space(3)))
#define DI __device__ __forceinline__

constexpr int S = 2048, D = 448, F = 1792;
constexpr unsigned FS = 128;
constexpr int PFA = 9, PFE = 4, NHW = 7, LA = 2;
constexpr unsigned WS_EP = 0;
constexpr unsigned WS_KF = 1024;
constexpr unsigned WS_FL = WS_KF + 256 * FS;
constexpr unsigned WS_ST = WS_FL + 1024 * FS;
constexpr unsigned WS_CS = WS_ST + 32768;
constexpr unsigned WS_KV = WS_CS + 16 * 7 * 2048;
constexpr unsigned WS_KVP = WS_KV + 32768;
constexpr unsigned WS_PQ = WS_KVP + 16 * 16 * 4096;
constexpr unsigned WS_H = WS_PQ + 64 * 28 * 1024;
constexpr unsigned WS_END = WS_H + 64 * 112 * 1024;
constexpr unsigned L_CS = 155648, L_ST = L_CS + 2048, L_RS = L_ST + 512, L_SY = L_RS + 512, L_END = L_SY + 256;
constexpr unsigned SY_FULL = L_SY, SY_DONE = L_SY + 32, SY_RDY = L_SY + 48, SY_FULL2 = L_SY + 64, SY_DONE2 = L_SY + 80,
                   SY_GO = L_SY + 112, SY_WOK = L_SY + 128, SY_HOK = L_SY + 100, SY_TAG = L_SY + 104;
constexpr unsigned CODE_BYTES = 32512;
constexpr unsigned SPIN_CAP = 1u << 19, LDS_CAP = 1u << 23;

DI u32x4 mk_rsrc(const void* p, unsigned bytes) {
  uint64_t a = (uint64_t)p;
  u32x4 r; r.x = (unsigned)a; r.y = (unsigned)(a >> 32) & 0xffffu; r.z = bytes; r.w = 0x00020000u; return r;
}
DI void dma16(unsigned lds_addr, unsigned voff, u32x4 rsrc, unsigned soff) {
  asm volatile("s_mov_b32 m0, %0\n\ts_nop 0\n\tbuffer_load_dwordx4 %1, %2, %3 offen lds"
               :: "s"(lds_addr), "v"(voff), "s"(rsrc), "s"(soff) : "memory");
}
template <unsigned OFF> DI void dma16o(unsigned lds_base, unsigned voff, u32x4 rsrc, unsigned soff) {
  asm volatile("s_add_u32 m0, %0, %4\n\ts_nop 0\n\tbuffer_load_dwordx4 %1, %2, %3 offen lds"
               :: "s"(lds_base), "v"(voff), "s"(rsrc), "s"(soff), "n"(OFF) : "memory", "scc");
}
#define SB __builtin_amdgcn_sched_barrier(0)
#define FENCE asm volatile("" ::: "memory")
#define PIN(a) asm volatile("" : "+v"(a))
#define WAIT_VM(n) asm volatile("s_waitcnt vmcnt(%0)" :: "n"(n) : "memory")
#define BARRIER asm volatile("s_waitcnt lgkmcnt(0)\n\ts_barrier" ::: "memory")
#define LD4(dst, voff, rsrc, soff, mod) asm volatile("buffer_load_dwordx4 %0, %1, %2, %3 offen " mod : "=v"(dst) : "v"(voff), "s"(rsrc), "s"(soff) : "memory")
#define LD1(dst, voff, rsrc, soff, mod) asm volatile("buffer_load_dword %0, %1, %2, %3 offen " mod : "=v"(dst) : "v"(voff), "s"(rsrc), "s"(soff) : "memory")
#define ST4(src, voff, rsrc, soff, mod) asm volatile("buffer_store_dwordx4 %0, %1, %2, %3 offen " mod "\n\ts_nop 1" :: "v"(src), "v"(voff), "s"(rsrc), "s"(soff) : "memory")
#define ST2(src, voff, rsrc, soff, mod) asm volatile("buffer_store_dwordx2 %0, %1, %2, %3 offen " mod :: "v"(src), "v"(voff), "s"(rsrc), "s"(soff) : "memory")
DI f32x16 mfma(bf16x8 a, bf16x8 b, f32x16 c) { return __builtin_amdgcn_mfma_f32_32x32x16_bf16(a, b, c, 0, 0, 0); }
DI bf16x8 asbf(u32x4 v) { return __builtin_bit_cast(bf16x8, v); }
DI bf16x8 ld_frag(unsigned a) { return *(LDS const bf16x8*)a; }
DI bf16x8 tr_frag(unsigned a0, unsigned a1) {
  s16x4 lo = __builtin_amdgcn_ds_read_tr16_b64_v4i16((LDS s16x4*)a0);
  s16x4 hi = __builtin_amdgcn_ds_read_tr16_b64_v4i16((LDS s16x4*)a1);
  return __builtin_bit_cast(bf16x8, __builtin_shufflevector(lo, hi, 0, 1, 2, 3, 4, 5, 6, 7));
}
DI unsigned pk2(float a, float b) { f32x2 v = {a, b}; return __builtin_bit_cast(unsigned, __builtin_convertvector(v, bf16x2)); }
DI float bflo(unsigned u) { return __uint_as_float(u << 16); }
DI float bfhi(unsigned u) { return __uint_as_float(u & 0xffff0000u); }
DI float rbf(float f) { return bflo(pk2(f, 0.f)); }
constexpr float LOG2E = 1.44269504f, EPS2 = 1e-6f * LOG2E;
DI f32x2 phi_u(float a0, float a1, f32x2 rl) { return __builtin_elementwise_fma((f32x2){a0, a1}, rl, (f32x2){EPS2, EPS2}); }
#define PHI_E(j0) _Pragma("unroll") for (int j_ = (j0); j_ < (j0) + 2; ++j_) pe[j_] = (f32x2){__builtin_amdgcn_fmed3f(__builtin_amdgcn_exp2f(u[j_].x), 0.f, 1.f), \
                                                                                     __builtin_amdgcn_fmed3f(__builtin_amdgcn_exp2f(u[j_].y), 0.f, 1.f)}; SB
#define PHI_B(o) _Pragma("unroll") for (int j_ = 0; j_ < 4; ++j_) u[j_] = (f32x2){fmaxf(u[j_].x, EPS2), fmaxf(u[j_].y, EPS2)}; SB; \
              _Pragma("unroll") for (int j_ = 0; j_ < 4; ++j_) u[j_] = __builtin_elementwise_fma(u[j_], (f32x2){1.f / LOG2E, 1.f / LOG2E}, pe[j_]); SB; \
              o = (u32x4){pk2(u[0].x, u[0].y), pk2(u[1].x, u[1].y), pk2(u[2].x, u[2].y), pk2(u[3].x, u[3].y)}
DI unsigned relu2(unsigned v) {
  typedef short s16x2 __attribute__((ext_vector_type(2)));
  return __builtin_bit_cast(unsigned, __builtin_elementwise_max(__builtin_bit_cast(s16x2, v), (s16x2){0, 0}));
}
DI float mul1(float a, float b) { float r; asm("v_mul_f32_e32 %0, %1, %2" : "=v"(r) : "v"(a), "v"(b)); return r; }
DI float sum8(float v) {
  v += __uint_as_float(__builtin_amdgcn_update_dpp(0u, __float_as_uint(v), 0xB1, 0xf, 0xf, true));
  v += __uint_as_float(__builtin_amdgcn_update_dpp(0u, __float_as_uint(v), 0x4E, 0xf, 0xf, true));
  v += __uint_as_float(__builtin_amdgcn_update_dpp(0u, __float_as_uint(v), 0x141, 0xf, 0xf, true));
  return v;
}
DI void lds_put(unsigned a, unsigned v) { FENCE; *(volatile LDS unsigned*)a = v; FENCE; }
DI void lds_wait(unsigned a, int n, unsigned need, int lane) {
  const unsigned ad = a + (lane & (n - 1)) * 4;
#pragma unroll 1
  for (unsigned sp = 0; sp < LDS_CAP; ++sp) {
    FENCE; const unsigned v = *(volatile LDS unsigned*)ad; FENCE;
    if (__all(v >= need)) break;
    __builtin_amdgcn_s_sleep(1);
  }
}
DI u64 tag_wait(unsigned voff, u32x4 rs, unsigned soff, u64 mask, u32x2 tag) {
  u64 bl = 0;
#pragma unroll 1
  for (unsigned sp = 0; sp < SPIN_CAP; ++sp) {
    u32x2 v;
    asm volatile("buffer_load_dwordx2 %0, %1, %2, %3 offen sc1\n\ts_waitcnt vmcnt(0)" : "=&v"(v) : "v"(voff), "s"(rs), "s"(soff) : "memory");
    bl = __ballot(v.x == tag.x && v.y == tag.y);
    if ((bl & mask) == mask) break;
    __builtin_amdgcn_s_sleep(4);
  }
  return bl;
}

__global__ __launch_bounds__(512, 2) void fused(const u16* __restrict__ x, const u16* __restrict__ wqkv, const u16* __restrict__ w1, char* ws,
                                                const u16* __restrict__ wo, const u16* __restrict__ b1, const u16* __restrict__ w2,
                                                const u16* __restrict__ b2, u16* __restrict__ out) {
  const int bid = blockIdx.x, tid = threadIdx.x, lane = tid & 63;
  unsigned cpf;
  {
    u64 pc; asm volatile("s_getpc_b64 %0" : "=s"(pc));
    LD1(cpf, (unsigned)(tid >> 6) * 4096u + (unsigned)lane * 32768u, mk_rsrc((const void*)(pc & ~255ull), CODE_BYTES), (unsigned)(bid >> 3) * 128u, "");
  }
  const int w = __builtin_amdgcn_readfirstlane(tid >> 6);
  const int xc = bid & 7, ix = bid >> 3, sl = ix & 7, rl = ix >> 3;
  const int r = (xc >> 1) * 4 + rl, s0 = r >= 14 ? 0 : r >> 1;
  const u32x4 rx = mk_rsrc(x, S * D * 2);
  const unsigned vx0 = (unsigned)(r * 128 + (w & 1) * 64 + (lane >> 3)) * 896u + (unsigned)(((lane & 7) ^ (lane >> 4)) << 4), vx1 = (vx0 ^ 64u) + 7168u;
#define XGROUP(n) do { dma16o<(n) * 2048>(lx, vx0, rx, sg * 128 + (n) * 14336); dma16o<(n) * 2048 + 1024>(lx, vx1, rx, sg * 128 + (n) * 14336); } while (0)
  asm volatile("" :: "s"(wqkv), "s"(w1), "s"(ws), "s"(wo), "s"(w2), "s"(b1), "s"(b2), "s"(out), "v"(vx0), "v"(vx1));
  if (w < 2) { const unsigned lx = w * 8192; const int sg = s0; XGROUP(0); XGROUP(1); XGROUP(2); XGROUP(3); }
  const int c = (int)(((xc & 1) ? 0xEFB98765u : 0xDCA43210u) >> (4 * sl)) & 15;
  const bool kvt = sl >= 6, pure = sl == 7;
  const int rt = xc * 4 + rl, ct = sl + 2 * rl - (sl + 2 * rl >= 7 ? 7 : 0), rot2 = 7 * rl + xc - (7 * rl + xc >= 28 ? 28 : 0);
  const int l31 = lane & 31, hh = lane >> 5, q = (lane >> 2) & 3, p = lane & 3, blk = (lane >> 4) & 1;
  const unsigned slot_b = kvt ? 49152u : 40960u;
  const u32x4 rws = mk_rsrc(ws, WS_END);
  const unsigned v16 = lane * 16;
  u32x2 tag;
  const unsigned swz = (unsigned)(((lane & 7) ^ ((lane >> 2) & 4)) << 4);
  f32x16 z16;
#pragma unroll
  for (int e = 0; e < 16; ++e) z16[e] = 0.f;

#define PUT_TILES(base, t) do { _Pragma("unroll") for (int n_ = 0; n_ < 8; ++n_) *(LDS f32x4*)((base) + n_ * 1024 + (v16 ^ (n_ * 32))) = \
      (f32x4){t[n_ >> 2][4 * (n_ & 3)], t[n_ >> 2][4 * (n_ & 3) + 1], t[n_ >> 2][4 * (n_ & 3) + 2], t[n_ >> 2][4 * (n_ & 3) + 3]}; } while (0)
  if (__builtin_expect(w < 4, 1)) {
    const int h = w, hp = h >> 1, hs_ = h & 1;
    const u32x4 rw2 = mk_rsrc(w2, F * D * 2), rwo = mk_rsrc(wo, D * D * 2);
    const unsigned vwo = (unsigned)(lane >> 3) * 896u + ct * 128 + swz;
    const unsigned vw2 = vwo + (unsigned)((h >> 1) * 896 + (h & 1) * 16) * 896u;
    const int hg = (h + xc + rl) & 3;
    u32x4 R[18];
#define ROT(u) ((u) + rot2 >= 28 ? (u) + rot2 - 28 : (u) + rot2)
#define PFLD(n) LD4(R[n], vw2, rw2, ROT(19 + ((n) >> 1)) * 28672u + ((n) & 1) * 7168u, "")
    auto fill = [&](int u, int g, int cnt) {
#pragma unroll 1
      for (int e = u + cnt; u < e; ++u, ++g) {
        const unsigned k = ROT(u) * 28672u;
        dma16(g * 8192 + h * 2048, vw2, rw2, k); dma16(g * 8192 + h * 2048 + 1024, vw2, rw2, k + 7168u);
      }
    };
    {
      __builtin_amdgcn_s_setprio(3);
      const bool a1 = c < 10, b1_ = c < 9;
      const u32x4 r0 = mk_rsrc(a1 ? w1 : wqkv, a1 ? D * F * 2 : D * 3 * D * 2), r1 = mk_rsrc(b1_ ? w1 : wqkv, b1_ ? D * F * 2 : D * 3 * D * 2);
      const unsigned p0 = a1 ? 3584u : 2688u, p1 = b1_ ? 3584u : 2688u;
      unsigned V0, V1, V2, V3;
      {
        const unsigned kl = 32 * hs_ + (lane >> 3);
        const unsigned c0 = (a1 ? c : c - 10) * 384, c1 = c == 9 ? 512 : c0;
        const unsigned vk = (32 * hs_ + (lane >> 4)) * 2688 + ((224 * c - 1792) & ~63) + ((q ^ (lane >> 4)) << 6) + p * 16;
        V0 = kvt ? vk : kl * p0 + c0 + swz;
        V1 = kvt ? vk + 4 * 2688 : kl * p1 + c1 + swz + 128;
        V2 = kvt ? V0 + 896 : V1 + 128;
        V3 = V1 + 896;
      }
      int sg = s0 + hp; sg = sg == 7 ? 0 : sg;
      unsigned so = hp * slot_b;
#define GROUP(n) do { const unsigned kk = sg * 64 + 8 * (n), sa = kk * p0, sb_ = kk * p1; \
          if (i || hp) XGROUP(n); \
          dma16o<16384 + (n) * 1024>(lw, V0, r0, sa); \
          dma16o<24576 + (n) * 1024>(lw, V1, r1, sb_); \
          dma16o<32768 + (n) * 1024>(lw, V2, r1, sb_); \
          if (kvt) dma16o<40960 + (n) * 1024>(lw, V3, r1, sb_); } while (0)
      if (hp) BARRIER;
      unsigned t2;
#pragma unroll
      for (int i = 0; i < 4; ++i) {
        const int t = hp + 2 * i;
        if (i == 3 && hp) break;
        if (i >= 2 || (i == 1 && hp)) lds_wait(SY_DONE, 4, t - 2, lane);
        const unsigned lx = so + hs_ * 8192, lw = so + hs_ * 4096;
        GROUP(0); GROUP(1); GROUP(2); GROUP(3);
        if (i == 0 && !hp) BARRIER;
        if (i == 2 && hp) break;
        if (i == 3) lds_put(SY_GO, 1);
        if (i == 1) {
          LD1(t2, lane < (hp ? 0 : hs_ ? 13 : 49) ? lane * 128u : 0x80000000u, hs_ ? rwo : rw2, bid * (hs_ ? 1664u : 6272u), "");
          WAIT_VM(1);
        } else WAIT_VM(0);
        if (i == 0) asm volatile("" :: "v"(cpf));
        if (i == 2) asm volatile("" :: "v"(t2));
        lds_put(SY_FULL + h * 4, t + 2);
        if (!pure && ((i == 1 && hp) || i == 2)) {
#pragma unroll
          for (int n = 0; n < (i == 2 ? PFE : PFA); ++n) PFLD(n);
        }
        so = so == 0 ? 2 * slot_b : so - slot_b;
        sg = sg >= 5 ? sg - 5 : sg + 2;
      }
      tag = *(volatile LDS u32x2*)SY_TAG;
      if (hp) {
        u32x4 xf[8];
        unsigned bi;
        const bool chk = !hs_ || kvt || lane < 32;
        const u32x4 rb1 = mk_rsrc(b1, c < 10 ? F * 2 : 0);
#pragma unroll 1
        for (unsigned sp = 0; sp < SPIN_CAP; ++sp) {
#pragma unroll
          for (int k = 0; k < 7; ++k) LD4(xf[k], v16, rws, WS_CS + c * 14336u + hs_ * 1024u + k * 2048u, "sc1");
          LD4(xf[7], v16, rws, WS_ST + r * 2048u + hs_ * 1024u, "sc1");
          LD1(bi, lane * 4u, rb1, c * 384u + hs_ * 256u, "");
          WAIT_VM(9); lds_put(SY_FULL + h * 4, 7);
          WAIT_VM(0);
          PIN(xf[0]); PIN(xf[7]); PIN(bi);
          if (kvt && l31 == 30) bi = 0x3f80u;
          unsigned bad = (xf[0].y ^ tag.x) | (xf[0].w ^ tag.x);
          float ax_ = __uint_as_float(xf[0].x), ay_ = __uint_as_float(xf[0].z);
#pragma unroll
          for (int k = 1; k < 7; ++k) {
            PIN(xf[k]);
            ax_ += __uint_as_float(xf[k].x); ay_ += __uint_as_float(xf[k].z); PIN(ax_); PIN(ay_);
            bad |= (xf[k].y ^ tag.x) | (xf[k].w ^ tag.x); PIN(bad);
          }
          const unsigned hi = pk2(ax_, ay_); ax_ -= bflo(hi); PIN(ax_); ay_ -= bfhi(hi);
          const unsigned mi = pk2(ax_, ay_); ax_ -= bflo(mi); PIN(ax_); ay_ -= bfhi(mi);
          const unsigned lo = pk2(ax_, ay_);
          *(LDS u32x4*)(L_CS + hs_ * 1024 + v16) = (u32x4){(hi & 0xffffu) | (mi << 16), (lo & 0xffffu) | (bi << 16),
                                                           (hi >> 16) | (mi & 0xffff0000u), (lo >> 16) | (bi & 0xffff0000u)};
          *(LDS unsigned*)(L_ST + hs_ * 256 + lane * 4) = xf[7].x;
          *(LDS float*)(L_RS + hs_ * 256 + lane * 4) = __builtin_amdgcn_rcpf(bfhi(xf[7].x)) * LOG2E;
          if (__builtin_expect(__all((bad == 0 || !chk) && xf[7].y == tag.x), 1)) break;
          __builtin_amdgcn_s_sleep(8);
        }
        lds_put(SY_FULL + h * 4, 8);
        if (!pure) {
#pragma unroll
          for (int n = PFA; n < 18; ++n) PFLD(n);
        }
        lds_wait(SY_GO, 1, 1, lane);
        asm volatile("" :: "v"(t2));
      }
      __builtin_amdgcn_s_setprio(0);
    }
    if (!pure) {
#pragma unroll
    for (int n = PFE; n < 18; ++n) { if (h < 2) PFLD(n); }
    const unsigned wb = 49152 + (h ^ 2) * 14336;
    lds_wait(SY_DONE, 4, h < 2 ? 6 : 5, lane);
#pragma unroll 1
    for (int n = 0; n < 14; ++n) dma16(wb + n * 1024, vwo, rwo, hg * 100352u + n * 7168u);
    lds_wait(SY_DONE, 4, 6, lane);
    fill(0, 16, 3);
    WAIT_VM(6);
    bf16x8 WO[4][2][2];
    {
      const unsigned a0 = wb + (4 * hh + q) * 128 + (((q >> 1) & 1) << 6) + blk * 32 + p * 8, a1_ = a0 ^ 64u;
#pragma unroll
      for (int n = 0; n < 4; ++n)
#pragma unroll
        for (int sg = 0; sg < 2; ++sg) {
          const unsigned o = n * 3584 + sg * 2048;
          WO[n][sg][0] = tr_frag(a0 + o, a0 + o + 1024); WO[n][sg][1] = tr_frag(a1_ + o, a1_ + o + 1024);
        }
    }
#pragma unroll
    for (int n = 0; n < 8; ++n) {
      u32x4 t = __builtin_bit_cast(u32x4, WO[n >> 1][1][n & 1]);
      if (hh) { t.z = 0; t.w = 0; }
      WO[n >> 1][1][n & 1] = asbf(t);
    }
#pragma unroll
    for (int n = 0; n < 16; ++n) PIN(WO[n >> 2][(n >> 1) & 1][n & 1]);
    lds_put(SY_WOK + h * 4, 1);
    lds_wait(SY_DONE, 4, c < 10 ? 8 : 7, lane);
    fill(3, 0, 6);
    lds_wait(SY_WOK, 4, 1, lane);
    fill(9, 6, 3);
    WAIT_VM(6); lds_put(SY_FULL2 + h * 4, 1);
    fill(12, 9, 7);
    lds_wait(SY_DONE, 4, 9, lane);
#pragma unroll
    for (int n = 0; n < 18; ++n) { PIN(R[n]); *(LDS u32x4*)((n < 6 ? 131072 : -24576) + h * 2048 + v16 + (n >> 1) * 8192 + (n & 1) * 1024) = R[n]; }
    WAIT_VM(0); lds_put(SY_RDY + h * 4, 1);
    u32x4 PQ[2][7], KV[8];
    __builtin_amdgcn_s_setprio(1);
    lds_wait(SY_HOK, 1, 1, lane);
    if (*(volatile LDS unsigned*)SY_HOK < 2u) tag_wait(min(lane, 4) * FS, rws, WS_FL + (rt * 32 + 19) * FS, ~0ull, tag);
#pragma unroll
    for (int n = 0; n < 14; ++n) LD4(PQ[n / 7][n % 7], v16, rws, WS_PQ + ((rt * 2 + n / 7) * 28 + hg * 7 + n % 7) * 1024u, "sc1");
    __builtin_amdgcn_s_setprio(3);
    tag_wait(min(lane, 7) * FS, rws, WS_KF + (xc * 32 + hg * 8) * FS, ~0ull, tag);
#pragma unroll
    for (int n = 0; n < 8; ++n) LD4(KV[n], v16, rws, WS_KV + hg * 8192u + n * 1024u, "sc1");
    __builtin_amdgcn_s_setprio(1);
    u32x4 PB[2][4];
#pragma unroll
    for (int n = 0; n < 14; ++n) PIN(PQ[n / 7][n % 7]);
#pragma unroll
    for (int u = 0; u < 8; ++u) {
      const u32x4* P = PQ[u >> 2]; const int n = u & 3, lo = n < 3 ? 2 * n + 1 : 5, hi = n ? 2 * n - 1 : 1;
      PB[u >> 2][n] = (u32x4){P[lo].x, P[lo].y, P[hi].z, P[hi].w}; PIN(PB[u >> 2][n]);
    }
    WAIT_VM(4);
#pragma unroll
    for (int n = 0; n < 4; ++n) PIN(KV[n]);
    f32x16 nm[3], as[2][2] = {{z16, z16}, {z16, z16}};
    u32x4 O[2][2]; float rd[2];
#define NUM0(u) nm[(u) % 3] = mfma(asbf(KV[2 * ((u) & 3)]), asbf(PQ[(u) >> 2][2 * ((u) & 3)]), z16)
#define NUM1(u) nm[(u) % 3] = mfma(asbf(KV[2 * ((u) & 3) + 1]), asbf(PB[(u) >> 2][(u) & 3]), nm[(u) % 3])
#define DEN(u) __uint_as_float(__builtin_amdgcn_permlane32_swap(__float_as_uint(nm[(u) % 3][12]), __float_as_uint(nm[(u) % 3][12]), false, false)[1])
#define APP(u, i) as[(u) >> 2][(i) & 1] = mfma(WO[(u) & 3][(i) >> 1][(i) & 1], asbf(O[(u) & 1][(i) >> 1]), as[(u) >> 2][(i) & 1])
#define MULS(a, b) _Pragma("unroll") for (int e = (a); e < (b); ++e) o[e] = mul1(nm[u % 3][e], rd[u & 1])
#define CVTS(a, b) _Pragma("unroll") for (int e = (a); e < (b); ++e) O[u & 1][e >> 2][e & 3] = pk2(o[2 * e], o[2 * e + 1])
    NUM0(0); NUM1(0); NUM0(1); NUM1(1);
    rd[0] = __builtin_amdgcn_rcpf(DEN(0) + 1e-6f);
    asm volatile("s_waitcnt vmcnt(0)" : "+v"(rd[0]), "+v"(KV[4]), "+v"(KV[5]), "+v"(KV[6]), "+v"(KV[7]) :: "memory");
    SB;
    unsigned d2 = 0; bool put0 = false;
#pragma unroll
    for (int u = 0; u < 8; ++u) {
      float o[16], dn;
      if (u) APP(u - 1, 0); SB; MULS(0, 6); SB;
      if (u) APP(u - 1, 1); SB; MULS(6, 10); CVTS(0, 2); SB;
      if (u) APP(u - 1, 2); SB; MULS(10, 13); CVTS(2, 4); SB;
      if (u) APP(u - 1, 3); SB; MULS(13, 16); CVTS(4, 6); SB;
      if (u < 6) NUM0(u + 2); SB; if (u < 7) dn = DEN(u + 1) + 1e-6f; SB;
      if (u < 6) NUM1(u + 2); SB; if (u < 7) rd[(u + 1) & 1] = __builtin_amdgcn_rcpf(dn); SB; CVTS(6, 8);
      if (u == 4) d2 = *(volatile LDS unsigned*)(SY_DONE2 + p * 4);
      if (u == 5) { put0 = __all(d2 >= 1u); if (put0) PUT_TILES(49152 + h * 16384, as[0]); }
      SB;
    }
    APP(7, 0); APP(7, 2); APP(7, 1); APP(7, 3);
    if (!put0) { lds_wait(SY_DONE2, 4, 1, lane); PUT_TILES(49152 + h * 16384, as[0]); }
    PUT_TILES(57344 + h * 16384, as[1]);
    BARRIER;
    }
  } else {
  const int cw = w - 4, wl = cw & 1, wh = cw >> 1;
  u32x4 xs[7];
  if (cw == 3) {
    const unsigned va = (unsigned)(r * 128 + c * 8 + (lane >> 3)) * 896u + (lane & 7) * 16;
#pragma unroll
    for (int k = 0; k < 7; ++k) LD4(xs[k], va, rx, k * 128u, "");
  }
  *(LDS unsigned*)(L_SY + lane * 4) = (lane >> 1) == 1; BARRIER;
  __builtin_amdgcn_s_setprio(2);
  bf16x8 ones = asbf((u32x4){0x3f803f80u, 0x3f803f80u, 0x3f803f80u, 0x3f803f80u});
  PIN(ones);
  {
    {
      u64 ep;
      asm volatile("s_load_dwordx2 %0, %1, 0x0\n\ts_waitcnt lgkmcnt(0) vmcnt(0)" : "=&s"(ep) : "s"(ws) : "memory");
      tag.x = (unsigned)ep + 1u; tag.y = (unsigned)(ep >> 32) + 1u;
    }
    asm volatile("" :: "v"(cpf));
    if (cw == 0) *(volatile LDS u32x2*)SY_TAG = tag;
    if (cw == 3) {
      float sq[2] = {0.f, 0.f}, sm[2] = {0.f, 0.f};
      const bf16x2 one2 = __builtin_bit_cast(bf16x2, 0x3f803f80u);
#pragma unroll
      for (int k = 0; k < 7; ++k) {
        PIN(xs[k]);
#pragma unroll
        for (int e = 0; e < 4; ++e) {
          const unsigned u = xs[k][e];
          const bf16x2 v = __builtin_bit_cast(bf16x2, u);
          sq[e & 1] = __builtin_amdgcn_fdot2_f32_bf16(v, v, sq[e & 1], false); sm[e & 1] = __builtin_amdgcn_fdot2_f32_bf16(v, one2, sm[e & 1], false);
        }
      }
      const float m = sum8(sm[0] + sm[1]) * (1.f / D);
      const float var = fmaxf(sum8(sq[0] + sq[1]) * (1.f / D) - m * m, 0.f);
      const unsigned sv = __builtin_amdgcn_ds_bpermute(lane * 32, pk2(-m, __builtin_amdgcn_sqrtf(rbf(rbf(var) + 1e-5f))));
      const u32x4 gr = {sv, tag.x, sv, tag.x};
      if (lane < 8) ST4(gr, v16, rws, WS_ST + (r * 128 + c * 8) * 16, "sc1");
    }
  }
  const unsigned fl_off = WS_FL + ((r * 2 + wl) * 32 + 2 * c + wh) * FS;
  const unsigned fa = SY_FULL + p * 4;
#define CHECK_FULL(v, need) do { if (__builtin_expect(__any((v) < (need)), 0)) lds_wait(SY_FULL, 4, need, lane); FENCE; } while (0)
#define COLSUM(a_, t_, tile) do { const unsigned a = (a_); bf16x8 f[4]; \
    _Pragma("unroll") for (int ks = 0; ks < 4; ++ks) f[ks] = tr_frag(a + ks * 2048, a + ks * 2048 + (t_)); \
    SB; f32x16 cs = z16; \
    _Pragma("unroll") for (int ks = 0; ks < 4; ++ks) cs = mfma(ones, f[ks], cs); \
    const u32x4 gr = {(unsigned)__builtin_amdgcn_ds_bpermute(lane * 8, __float_as_int(cs[0])), tag.x, (unsigned)__builtin_amdgcn_ds_bpermute(lane * 8 + 4, __float_as_int(cs[0])), tag.x}; \
    if (lane < 16) ST4(gr, v16, rws, WS_CS + (c * 7 + s0) * 2048 + (tile) * 256, "sc1"); } while (0)
  unsigned da = SY_DONE + cw * 4;
  PIN(da);
  const unsigned xm = hh ? 0u : ~0u;
  if (!kvt) {
    const unsigned xa = (wl * 64 + l31) * 128 + ((hh ^ ((l31 >> 1) & 7)) << 4);
    const unsigned wr = 16384 + (8 * hh + q) * 128 + blk * 32 + p * 8;
    const unsigned wa0 = wr + wh * 8192 + ((wh ^ (q >> 1)) << 6), wa1 = wr + wh * 16384 + ((wh ^ 1 ^ (q >> 1)) << 6);
    f32x16 acc[2][3] = {{z16, z16, z16}, {z16, z16, z16}};
    bf16x8 X[4][2], W[4][3];
#define RDA(n, ks) do { \
      if ((n) == 0 || (n) == 2) X[ks][(n) >> 1] = ld_frag(ax[ks] + ((n) >> 1) * 4096); \
      else { const unsigned a_ = ((n) == 3 ? a1_ : a0_) + ((n) == 4 ? 8192 : 0) + (ks) * 2048; W[ks][(n) == 1 ? 0 : (n) - 2] = tr_frag(a_, a_ + 512); } } while (0)
#define MMA(ks, m) acc[(m) & 1][(m) >> 1] = mfma(W[ks][(m) >> 1], X[ks][(m) & 1], acc[(m) & 1][(m) >> 1])
    unsigned ax[4] = {xa, xa ^ 32u, xa ^ 64u, xa ^ 96u}, a0_ = wa0, a1_ = wa1;
#pragma unroll
    for (int n = 0; n < 6; ++n) PIN(acc[n & 1][n >> 1]);
    lds_wait(SY_FULL, 4, 1, lane);
    if (r < 14 && !((r & 1) && wl)) {
      const int b = (r & 1) ? 2 : wl;
      COLSUM(b == 1 ? wa1 : wa0 + b * 4096, 512, 3 * wh + b);
    }
#pragma unroll
    for (int n = 0; n < 10; ++n) { RDA(n % 5, n / 5); SB; }
    const int tla = c >= 10 ? 0 : (xc & 1) ? 2 : 4, tlb = c >= 10 ? 0 : 4;
    unsigned tv = 1; int ts = 0;
    PIN(tv);
#pragma unroll 1
    for (;;) {
      unsigned fv; int dl;
#pragma unroll
      for (int m = 0; m < 12; ++m) {
        MMA(m / 6, m % 6); SB;
        if (m % 6 < 5) RDA(m % 6, 2 + m / 6);
        if (m == 0) { dl = ((0x24 >> ts) & 1) ? -81920 : 40960; ++ts; asm volatile("" : "+s"(dl), "+s"(ts)); }
        if (m == 2) { ax[0] += dl; ax[1] += dl; PIN(ax[0]); PIN(ax[1]); }
        if (m == 5) { if (ts <= tla) __builtin_amdgcn_s_sleep(4); fv = *(volatile LDS unsigned*)fa; }
        if (m == 10) *(volatile LDS unsigned*)da = tv;
        if (m == 11) { a0_ += dl; a1_ += dl; ++tv; CHECK_FULL(fv, tv); }
        SB;
      }
      if (__builtin_expect(ts == 7, 0)) break;
#pragma unroll
      for (int m = 0; m < 12; ++m) {
        MMA(2 + m / 6, m % 6); SB;
        if (m % 6 < 5) RDA(m % 6, m / 6);
        if (m == 5) { ax[2] += dl; ax[3] += dl; if (ts <= tlb) __builtin_amdgcn_s_sleep(4); }
        SB;
      }
    }
    unsigned st[2]; u32x2 cv[3];
#pragma unroll
    for (int a = 0; a < 2; ++a) st[a] = *(LDS const unsigned*)(L_ST + (wl * 64 + a * 32 + l31) * 4);
#pragma unroll
    for (int b = 0; b < 3; ++b) cv[b] = *(LDS const u32x2*)(L_CS + ((3 * wh + b) * 32 + l31) * 8);
    SB;
    MMA(2, 0); MMA(2, 1); MMA(3, 0); MMA(3, 1);
    SB;
    bf16x8 xe[2];
#pragma unroll
    for (int a = 0; a < 2; ++a) xe[a] = asbf((u32x4){((st[a] & 0xffffu) | (st[a] << 16)) & xm, st[a] & xm, 0u, 0u});
#define MME(m) acc[(m) & 1][(m) >> 1] = mfma(asbf((u32x4){cv[(m) >> 1].x, cv[(m) >> 1].y, cv[(m) >> 1].x, cv[(m) >> 1].y}), xe[(m) & 1], acc[(m) & 1][(m) >> 1])
    MME(0); MME(1);
    SB;
    const int R0 = r * 4 + wl * 2;
    const unsigned sk = c == 9 ? 0x40000000u : 0u;
#define MM6(b, j) do { if ((j) < 4) MMA(2 + ((j) >> 1), 2 * (b) + ((j) & 1)); else MME(2 * (b) + ((j) & 1)); } while (0)
    if (2 * c + wh < 19) {
      const unsigned ob = WS_H + (R0 * 112 + 12 * c + 6 * wh) * 1024u;
#pragma unroll
      for (int b = 0; b < 3; ++b)
#pragma unroll
        for (int n = 0; n < 4; ++n) {
          const int a = n >> 1, e = 8 * (n & 1);
          float t[8]; u32x4 o;
          if (b < 2) MM6(b + 1, n < 2 ? 2 * n : n + 2);
          SB;
          if (n == 0) asm volatile("s_nop 10" : "+v"(acc[0][b]), "+v"(acc[1][b]));
          asm volatile("v_mul_f32_e64 %0, %8, |%8|\n\tv_mul_f32_e64 %1, %9, |%9|\n\tv_mul_f32_e64 %2, %10, |%10|\n\tv_mul_f32_e64 %3, %11, |%11|\n\t"
                       "v_mul_f32_e64 %4, %12, |%12|\n\tv_mul_f32_e64 %5, %13, |%13|\n\tv_mul_f32_e64 %6, %14, |%14|\n\tv_mul_f32_e64 %7, %15, |%15|"
                       : "=&v"(t[0]), "=&v"(t[1]), "=&v"(t[2]), "=&v"(t[3]), "=&v"(t[4]), "=&v"(t[5]), "=&v"(t[6]), "=&v"(t[7])
                       : "v"(acc[a][b][e]), "v"(acc[a][b][e + 1]), "v"(acc[a][b][e + 2]), "v"(acc[a][b][e + 3]), "v"(acc[a][b][e + 4]), "v"(acc[a][b][e + 5]),
                         "v"(acc[a][b][e + 6]), "v"(acc[a][b][e + 7]));
          SB;
          if (b < 2 && n < 2) MM6(b + 1, 2 * n + 1);
          SB;
#pragma unroll
          for (int j = 0; j < 4; ++j) o[j] = pk2(t[2 * j], t[2 * j + 1]);
#pragma unroll
          for (int j = 0; j < 4; ++j) o[j] = relu2(o[j]);
          ST4(o, v16, rws, ob + a * (112 * 1024u) + (2 * b + (n & 1)) * 1024u + (b == 2 ? sk : 0u), "sc1");
          SB;
        }
    } else {
#pragma unroll
      for (int j = 0; j < 12; ++j) MM6(1 + j / 6, j % 6);
      const unsigned ob = WS_PQ + (R0 * 28 + (c == 9 ? 22 : (c - 10) * 12 + wh * 6)) * 1024u;
      SB;
#pragma unroll
      for (int a = 0; a < 2; ++a) {
        const float rl = __builtin_amdgcn_rcpf(bfhi(st[a])) * LOG2E;
#pragma unroll
        for (int n = 0; n < 6; ++n) {
          f32x2 u[4], pe[4]; u32x4 o;
#pragma unroll
          for (int j = 0; j < 4; ++j) u[j] = phi_u(acc[a][n >> 1][8 * (n & 1) + 2 * j], acc[a][n >> 1][8 * (n & 1) + 2 * j + 1], (f32x2){rl, rl});
          SB; PHI_E(0); PHI_E(2); PHI_B(o);
          ST4(o, v16, rws, ob + a * (28 * 1024u) + n * 1024u + (n < 2 ? sk : 0u), "sc1");
          SB;
        }
      }
    }
  } else {
    const unsigned xa = l31 * 128 + ((hh ^ ((l31 >> 1) & 7)) << 4);
    const int ok_ = (c & 1) * 32 + 56 * cw + 8 * ((4 * blk + p + cw) & 7), ov_ = (c & 1) * 32 + 56 * cw + 32 * blk + 8 * p;
    const unsigned ka = 16384 + hh * 1024 + q * 256 + ((((ok_ >> 6) & 3) ^ q) << 6) + (ok_ & 63);
    const unsigned va = 32768 + hh * 1024 + q * 256 + ((((ov_ >> 6) & 3) ^ q) << 6) + (ov_ & 63) + ((lane & 19) == 19 ? 0x100000 : 0);
    f32x16 acc[4][2] = {{z16, z16}, {z16, z16}, {z16, z16}, {z16, z16}};
    bf16x8 X[4][4], W[4][2];
#define RDK(n, ks) do { \
      if ((n) == 1 || (n) == 5) { const unsigned a_ = ((n) == 1 ? a0_ : a1_) + (ks) * 2048; W[ks][(n) >> 2] = tr_frag(a_, a_ + 8192); } \
      else X[ks][(n) ? (n) - 1 : 0] = ld_frag(ax[ks] + ((n) ? (n) - 1 : 0) * 4096); } while (0)
#define MMK(ks, m) acc[(m) & 3][(m) >> 2] = mfma(X[ks][(m) & 3], W[ks][(m) >> 2], acc[(m) & 3][(m) >> 2])
    unsigned ax[4] = {xa, xa ^ 32u, xa ^ 64u, xa ^ 96u}, a0_ = ka, a1_ = va;
#pragma unroll
    for (int n = 0; n < 8; ++n) PIN(acc[n & 3][n >> 2]);
    lds_wait(SY_FULL, 4, 1, lane);
    if (r < 14) COLSUM((r & 1) ? va : ka, 8192, 2 * cw + (r & 1));
#pragma unroll
    for (int n = 0; n < 12; ++n) { RDK(n % 6, n / 6); SB; }
    unsigned tv = 1; int ts = 0;
    PIN(tv);
#pragma unroll 1
    for (;;) {
      unsigned fv; int dl;
#pragma unroll
      for (int m = 0; m < 16; ++m) {
        MMK(m >> 3, m & 7); SB;
        if ((m & 7) >= 1 && (m & 7) < 7) RDK((m & 7) - 1, 2 + (m >> 3));
        if (m == 0) { dl = ((0x24 >> ts) & 1) ? -98304 : 49152; ++ts; asm volatile("" : "+s"(dl), "+s"(ts)); }
        if (m == 7) { ax[0] += dl; ax[1] += dl; PIN(ax[0]); PIN(ax[1]); }
        if (m == 8) fv = *(volatile LDS unsigned*)fa;
        if (m == 14) *(volatile LDS unsigned*)da = tv;
        if (m == 15) { a0_ += dl; a1_ += dl; ++tv; CHECK_FULL(fv, tv); }
        SB;
      }
      if (__builtin_expect(ts == 7, 0)) break;
#pragma unroll
      for (int m = 0; m < 16; ++m) {
        MMK(2 + (m >> 3), m & 7); SB;
        if ((m & 7) >= 1 && (m & 7) < 7) RDK((m & 7) - 1, m >> 3);
        if (m == 7) { ax[2] += dl; ax[3] += dl; }
        SB;
      }
    }
    unsigned st[4]; u32x2 cv[2]; f32x4 rsv[2][4];
#pragma unroll
    for (int a = 0; a < 4; ++a) st[a] = *(LDS const unsigned*)(L_ST + (a * 32 + l31) * 4);
#pragma unroll
    for (int b = 0; b < 2; ++b) cv[b] = *(LDS const u32x2*)(L_CS + ((2 * cw + b) * 32 + l31) * 8);
    unsigned rsa = L_RS + hh * 16;
    PIN(rsa);
#define MK6(a, j) do { if ((j) < 4) MMK(2 + ((j) >> 1), (a) + 4 * ((j) & 1)); else acc[a][(j) & 1] = mfma(asbf((u32x4){((st[a] & 0xffffu) | (st[a] << 16)) & xm, st[a] & xm, 0u, 0u}), \
        asbf((u32x4){cv[(j) & 1].x, cv[(j) & 1].y, cv[(j) & 1].x, cv[(j) & 1].y}), acc[a][(j) & 1]); } while (0)
#pragma unroll
    for (int n = 0; n < 4; ++n) rsv[0][n] = *(LDS const f32x4*)(rsa + n * 32);
    SB;
#pragma unroll
    for (int j = 0; j < 6; ++j) MK6(0, j);
    SB;
    f32x16 kv = z16;
    u32x4 fk, fv;
#pragma unroll
    for (int gi = 0; gi < 8; ++gi) {
      const int a = gi >> 1, sg = gi & 1, e = 8 * sg;
      if (a < 3 && sg == 0) {
#pragma unroll
        for (int n = 0; n < 4; ++n) rsv[(a + 1) & 1][n] = *(LDS const f32x4*)(rsa + (a + 1) * 128 + n * 32);
      }
      const f32x4 ra_ = rsv[a & 1][2 * sg], rb_ = rsv[a & 1][2 * sg + 1];
      const f32x2 rl[4] = {{ra_[0], ra_[1]}, {ra_[2], ra_[3]}, {rb_[0], rb_[1]}, {rb_[2], rb_[3]}};
      f32x2 u[4], vv[4], pe[4];
#pragma unroll
      for (int j = 0; j < 4; ++j) u[j] = phi_u(acc[a][0][e + 2 * j], acc[a][0][e + 2 * j + 1], rl[j]);
#pragma unroll
      for (int j = 0; j < 4; ++j) vv[j] = (f32x2){acc[a][1][e + 2 * j], acc[a][1][e + 2 * j + 1]} * rl[j];
      SB;
      if (gi) kv = mfma(asbf(fk), asbf(fv), kv);
      SB;
      PHI_E(0);
      if (a < 3) MK6(a + 1, 3 * sg);
      SB;
      PHI_E(2);
      if (a < 3) MK6(a + 1, 3 * sg + 1);
      SB;
      fv = (u32x4){pk2(vv[0].x, vv[0].y), pk2(vv[1].x, vv[1].y), pk2(vv[2].x, vv[2].y), pk2(vv[3].x, vv[3].y)};
      SB;
      if (a < 3) MK6(a + 1, 3 * sg + 2);
      SB;
      PHI_B(fk);
      SB;
    }
    kv = mfma(asbf(fk), asbf(fv), kv);
    kv = kv * (l31 == 28 ? 1.f / 1.4453125f : 1.f / LOG2E);
    const unsigned ko = WS_KVP + (((c - 12) * 4 + cw) * 16 + r) * 4096u;
#pragma unroll
    for (int e = 0; e < 4; ++e) {
      const u32x4 o = {pk2(kv[4 * e], kv[4 * e + 1]), tag.x, pk2(kv[4 * e + 2], kv[4 * e + 3]), tag.x};
      ST4(o, v16, rws, ko + e * 1024u, "sc1");
    }
  }
  lds_put(da, 8);
  bf16x8 WH[9][4];
  unsigned ra[3][2];
#pragma unroll
  for (int cb = 0; cb < 2; ++cb) {
    ra[0][cb] = wh * 4096 + (4 * hh + q) * 128 + ((cb ^ (q >> 1)) << 6) + blk * 32 + p * 8; ra[1][cb] = ra[0][cb] + 65536; ra[2][cb] = ra[0][cb] + 131072;
    PIN(ra[0][cb]); PIN(ra[1][cb]); PIN(ra[2][cb]);
  }
#define RDW(dst, n, reg) do { const unsigned a_ = ra[(reg) >> 3][(n) & 1] + ((reg) & 7) * 8192 + ((n) >> 1) * 2048; dst[n] = tr_frag(a_, a_ + 1024); } while (0)
  if (!kvt) WAIT_VM(0);
  if (lane < 8) ST4(((u32x4){tag.x, tag.y, tag.x, tag.y}), v16, rws, fl_off, "sc1");
  if (!pure) {
    lds_wait(SY_FULL2, 4, 1, lane);
#pragma unroll
    for (int n = 0; n < 36; ++n) RDW(WH[n >> 2], n & 3, (16 + (n >> 2)) % 19);
    lds_put(da, 9);
  }

  if (!pure) {
    const int kh = wh, R = rt * 2 + wl;
    if (cw == 0) {
      const u64 bl = tag_wait(min(lane, 23) * FS, rws, WS_FL + rt * 32 * FS, 0x7FFFFull, tag);
      lds_put(SY_HOK, (bl & 0xF80000ull) == 0xF80000ull ? 2 : 1);
    } else lds_wait(SY_HOK, 1, 1, lane);
    WAIT_VM(0);
    const unsigned hs = WS_H + (R * 112 + kh * 56) * 1024u;
    constexpr int NH = NHW, E = 28 - NH;
    u32x4 hf[NH][2];
#define HLD(u) do { const unsigned o_ = hs + ROT(u) * 2048u; \
      LD4(hf[(u) % NH][0], v16, rws, o_, "sc1"); LD4(hf[(u) % NH][1], v16, rws, o_, "offset:1024 sc1"); } while (0)
#pragma unroll
    for (int u = 0; u < NH; ++u) HLD(u);
    u32x4 xr[2], br; unsigned sg_[2];
    const unsigned fxo = (unsigned)(rt * 64 + cw * 8 + (lane >> 3)) * 896u + ct * 128 + (lane & 7) * 16;
#define FIN_LD do { LD4(xr[0], fxo, rx, 0u, ""); LD4(xr[1], fxo, rx, 28672u, ""); LD4(br, (lane & 7) * 16u, mk_rsrc(b2, D * 2), ct * 128u, ""); \
      LD1(sg_[0], (unsigned)(lane & ~7) * 2u, rws, WS_ST + rt * 1024u + cw * 128u, "sc1"); LD1(sg_[1], (unsigned)(lane & ~7) * 2u, rws, WS_ST + rt * 1024u + cw * 128u, "offset:512 sc1"); } while (0)
    f32x16 af[2] = {z16, z16};
    bf16x8 W[LA + 1][4];
#pragma unroll
    for (int u = 0; u < 28; ++u) {
      constexpr auto total = [](int u_) { return 2 * NH + 2 * (u_ < E ? u_ : E) + (u_ > E ? 5 : 0); };
      asm volatile("s_waitcnt vmcnt(%2)" : "+v"(hf[u % NH][0]), "+v"(hf[u % NH][1]) : "n"(total(u) - 2 - 2 * u) : "memory");
      if (u == 9 - LA) { lds_wait(SY_RDY, 4, 1, lane); }
#pragma unroll
      for (int n = 0; n < 4; ++n) {
        af[n & 1] = mfma(u < 9 ? WH[u < 9 ? u : 0][n] : W[u % (LA + 1)][n], asbf(hf[u % NH][n >> 1]), af[n & 1]); SB;
        if (u + LA >= 9 && u + LA < 28) RDW(W[(u + LA) % (LA + 1)], n, (u + LA - 3) % 19);
        SB;
      }
      if (u == 16 - LA) lds_put(SY_DONE2 + cw * 4, 1);
      if (u == 20 - LA) lds_put(SY_DONE2 + cw * 4, 2);
      if (u < E) HLD(u + NH);
      else if (u == E) FIN_LD;
    }
    __builtin_amdgcn_s_setprio(0);
    lds_wait(SY_DONE2, 4, 2, lane);
    PUT_TILES(114688 + cw * 8192, af);
    lds_put(SY_DONE2 + cw * 4, 3);
    WAIT_VM(0);
    unsigned a0 = 49152 + (lane & 7) * 1024 + (((cw * 8 + (lane >> 3)) ^ ((lane & 7) * 2)) << 4), a1 = a0 + 65536;
    PIN(a0); PIN(a1); PIN(br);
    lds_wait(SY_DONE2, 4, 3, lane);
    float gt[2][8];
    {
      f32x4 f[2][2][2];
#pragma unroll
      for (int n = 0; n < 8; ++n) f[n >> 2][(n >> 1) & 1][n & 1] = *(LDS const f32x4*)(a1 + (n >> 2) * 8192 + ((n >> 1) & 1) * 16384 + (n & 1) * 512);
      SB;
#pragma unroll
      for (int a = 0; a < 2; ++a) {
        PIN(sg_[a]);
        const float rs = __builtin_amdgcn_rcpf(bfhi(sg_[a])), rs2 = rs * rs;
#pragma unroll
        for (int e = 0; e < 8; ++e) {
          float t = f[a][0][e >> 2][e & 3] + f[a][1][e >> 2][e & 3]; PIN(t);
          t = fmaf(t, rs2, (e & 1) ? bfhi(br[e >> 1]) : bflo(br[e >> 1])); PIN(t);
          gt[a][e] = t * __builtin_amdgcn_rcpf(1.f + __expf(-t)); PIN(gt[a][e]);
        }
      }
    }
    float xv[2][8];
#pragma unroll
    for (int n = 0; n < 16; ++n) { if (!(n & 7)) PIN(xr[n >> 3]); xv[n >> 3][n & 7] = (n & 1) ? bfhi(xr[n >> 3][(n & 7) >> 1]) : bflo(xr[n >> 3][(n & 7) >> 1]); PIN(xv[n >> 3][n & 7]); }
    BARRIER;
    f32x4 sp[2][4][2];
#pragma unroll
    for (int n = 0; n < 16; ++n) sp[n >> 3][(n >> 1) & 3][n & 1] = *(LDS const f32x4*)(a0 + (n >> 3) * 8192 + ((n >> 1) & 3) * 16384 + (n & 1) * 512);
    SB;
#pragma unroll
    for (int a = 0; a < 2; ++a) {
      const f32x4 s0_ = (sp[a][0][0] + sp[a][1][0]) + (sp[a][2][0] + sp[a][3][0]), s1_ = (sp[a][0][1] + sp[a][1][1]) + (sp[a][2][1] + sp[a][3][1]);
      u32x4 ov;
#pragma unroll
      for (int e = 0; e < 4; ++e) {
        const f32x4 sv = e < 2 ? s0_ : s1_;
        ov[e] = pk2(fmaf(sv[2 * (e & 1)], gt[a][2 * e], xv[a][2 * e]), fmaf(sv[2 * (e & 1) + 1], gt[a][2 * e + 1], xv[a][2 * e + 1]));
      }
      ST4(ov, fxo, mk_rsrc(out, S * D * 2), a * 28672u, "sc0 sc1");
    }
  }
  }
  if (pure) {
    const int id = xc * 4 + (ix >> 3), hd = id >> 1;
    lds_wait(SY_DONE, 4, 8, lane);
    f32x4 a, b;
#pragma unroll 1
    for (unsigned sp = 0; sp < SPIN_CAP; ++sp) {
      u32x4 pr[4];
#pragma unroll
      for (int n = 0; n < 4; ++n) LD4(pr[n], v16, rws, WS_KVP + (hd * 16 + 2 * w + (n >> 1)) * 4096u + (id & 1) * 2048u + (n & 1) * 1024u, "sc1");
      WAIT_VM(0);
      unsigned bad = 0;
#pragma unroll
      for (int n = 0; n < 4; ++n) { PIN(pr[n]); bad |= (pr[n].y ^ tag.x) | (pr[n].w ^ tag.x); }
#define F4(n) (f32x4){bflo(pr[n].x), bfhi(pr[n].x), bflo(pr[n].z), bfhi(pr[n].z)}
      a = F4(0) + F4(2); b = F4(1) + F4(3);
      if (__all(bad == 0)) break;
      __builtin_amdgcn_s_sleep(4);
    }
    *(LDS f32x4*)(w * 2048 + v16) = a; *(LDS f32x4*)(w * 2048 + 1024 + v16) = b;
    BARRIER;
    if (w == 4) {
      a = *(LDS const f32x4*)v16; b = *(LDS const f32x4*)(1024 + v16);
#pragma unroll
      for (int n = 1; n < 8; ++n) { a += *(LDS const f32x4*)(n * 2048 + v16); b += *(LDS const f32x4*)(n * 2048 + 1024 + v16); }
      u32x4 o = {pk2(a[0], a[1]), pk2(a[2], a[3]), pk2(b[0], b[1]), pk2(b[2], b[3])};
      if ((id & 1) && hh != (hd & 1)) { if (hd & 2) { o.x = 0; o.y = 0; } else { o.z = 0; o.w = 0; } }
      ST4(o, v16, rws, WS_KV + id * 1024u, "sc1");
      WAIT_VM(0);
      ST4(((u32x4){tag.x, tag.y, tag.x, tag.y}), (lane >> 3) * 32 * FS + (lane & 7) * 16, rws, WS_KF + id * FS, "sc1");
    } else if (w == 5 && bid == 255) {
#pragma unroll 1
      for (int n = 0; n < 4; ++n) tag_wait((unsigned)((lane >> 4) * 64 + (lane & 15) * 2) * FS, rws, WS_FL + n * 256 * FS, ~0ull, tag);
      if (lane == 0) ST2(tag, 0u + lane, rws, WS_EP, "sc1");
    }
  }
}

extern "C" void kernel_launch(void* const* d_in, const int* in_sizes, int n_in, void* d_out, int out_size, void* d_ws,
                              size_t ws_size, hipStream_t stream) {
  (void)hipFuncSetAttribute((const void*)fused, hipFuncAttributeMaxDynamicSharedMemorySize, L_END);
  fused<<<256, 512, L_END, stream>>>((const u16*)d_in[0], (const u16*)d_in[1], (const u16*)d_in[4], (char*)d_ws, (const u16*)d_in[2],
                                         (const u16*)d_in[5], (const u16*)d_in[6], (const u16*)d_in[7], (u16*)d_out);
}
```

```cpp
#include <hip/hip_runtime.h>
#include <hip/hip_bf16.h>
#include <stdint.h>
#pragma clang diagnostic ignored "-Wint-to-pointer-cast"

typedef unsigned short u16;
typedef unsigned long long u64;
typedef __bf16 bf16x8 __attribute__((ext_vector_type(8)));
typedef __bf16 bf16x2 __attribute__((ext_vector_type(2)));
typedef short s16x4 __attribute__((ext_vector_type(4)));
typedef float f32x4 __attribute__((ext_vector_type(4)));
typedef float f32x16 __attribute__((ext_vector_type(16)));
typedef float f32x2 __attribute__((ext_vector_type(2)));
typedef unsigned u32x4 __attribute__((ext_vector_type(4)));
typedef unsigned u32x2 __attribute__((ext_vector_type(2)));
#define LDS __attribute__((address_space(3)))
#define DI __device__ __forceinline__

constexpr int S = 2048, D = 448, F = 1792;
constexpr unsigned FS = 128;
constexpr int P1 = 8, P2 = 20, NHW = 7, LA = 2;
constexpr unsigned WS_EP = 0;
constexpr unsigned WS_KF = 1024;
constexpr unsigned WS_FL = WS_KF + 256 * FS;
constexpr unsigned WS_ST = WS_FL + 1024 * FS;
constexpr unsigned WS_CS = WS_ST + 32768;
constexpr unsigned WS_KV = WS_CS + 16 * 7 * 2048;
constexpr unsigned WS_KVP = WS_KV + 32768;
constexpr unsigned WS_PQ = WS_KVP + 16 * 16 * 4096;
constexpr unsigned WS_H = WS_PQ + 64 * 28 * 1024;
constexpr unsigned WS_END = WS_H + 64 * 112 * 1024;
constexpr unsigned L_CS = 155648, L_ST = L_CS + 2048, L_RS = L_ST + 512, L_SY = L_RS + 512, L_END = L_SY + 256;
constexpr unsigned SY_FULL = L_SY, SY_DONE = L_SY + 32, SY_RDY = L_SY + 48, SY_FULL2 = L_SY + 64, SY_DONE2 = L_SY + 80,
                   SY_GO = L_SY + 112, SY_WOK = L_SY + 128, SY_HOK = L_SY + 100, SY_TAG = L_SY + 104;
constexpr unsigned CODE_BYTES = 32640;
constexpr unsigned SPIN_CAP = 1u << 19, LDS_CAP = 1u << 23;

DI u32x4 mk_rsrc(const void* p, unsigned bytes) {
  uint64_t a = (uint64_t)p;
  u32x4 r; r.x = (unsigned)a; r.y = (unsigned)(a >> 32) & 0xffffu; r.z = bytes; r.w = 0x00020000u; return r;
}
DI void dma16(unsigned lds_addr, unsigned voff, u32x4 rsrc, unsigned soff) {
  asm volatile("s_mov_b32 m0, %0\n\ts_nop 0\n\tbuffer_load_dwordx4 %1, %2, %3 offen lds"
               :: "s"(lds_addr), "v"(voff), "s"(rsrc), "s"(soff) : "memory");
}
template <unsigned OFF> DI void dma16o(unsigned lds_base, unsigned voff, u32x4 rsrc, unsigned soff) {
  asm volatile("s_add_u32 m0, %0, %4\n\ts_nop 0\n\tbuffer_load_dwordx4 %1, %2, %3 offen lds"
               :: "s"(lds_base), "v"(voff), "s"(rsrc), "s"(soff), "n"(OFF) : "memory", "scc");
}
#define SB __builtin_amdgcn_sched_barrier(0)
#define FENCE asm volatile("" ::: "memory")
#define PIN(a) asm volatile("" : "+v"(a))
#define WAIT_VM(n) asm volatile("s_waitcnt vmcnt(%0)" :: "n"(n) : "memory")
#define BARRIER asm volatile("s_waitcnt lgkmcnt(0)\n\ts_barrier" ::: "memory")
#define LD4(dst, voff, rsrc, soff, mod) asm volatile("buffer_load_dwordx4 %0, %1, %2, %3 offen " mod : "=v"(dst) : "v"(voff), "s"(rsrc), "s"(soff) : "memory")
#define LD1(dst, voff, rsrc, soff, mod) asm volatile("buffer_load_dword %0, %1, %2, %3 offen " mod : "=v"(dst) : "v"(voff), "s"(rsrc), "s"(soff) : "memory")
#define ST4(src, voff, rsrc, soff, mod) asm volatile("buffer_store_dwordx4 %0, %1, %2, %3 offen " mod "\n\ts_nop 1" :: "v"(src), "v"(voff), "s"(rsrc), "s"(soff) : "memory")
#define ST2(src, voff, rsrc, soff, mod) asm volatile("buffer_store_dwordx2 %0, %1, %2, %3 offen " mod :: "v"(src), "v"(voff), "s"(rsrc), "s"(soff) : "memory")
DI f32x16 mfma(bf16x8 a, bf16x8 b, f32x16 c) { return __builtin_amdgcn_mfma_f32_32x32x16_bf16(a, b, c, 0, 0, 0); }
DI bf16x8 asbf(u32x4 v) { return __builtin_bit_cast(bf16x8, v); }
DI bf16x8 ld_frag(unsigned a) { return *(LDS const bf16x8*)a; }
DI bf16x8 tr_frag(unsigned a0, unsigned a1) {
  s16x4 lo = __builtin_amdgcn_ds_read_tr16_b64_v4i16((LDS s16x4*)a0);
  s16x4 hi = __builtin_amdgcn_ds_read_tr16_b64_v4i16((LDS s16x4*)a1);
  return __builtin_bit_cast(bf16x8, __builtin_shufflevector(lo, hi, 0, 1, 2, 3, 4, 5, 6, 7));
}
DI unsigned pk2(float a, float b) { f32x2 v = {a, b}; return __builtin_bit_cast(unsigned, __builtin_convertvector(v, bf16x2)); }
DI float bflo(unsigned u) { return __uint_as_float(u << 16); }
DI float bfhi(unsigned u) { return __uint_as_float(u & 0xffff0000u); }
DI float rbf(float f) { return bflo(pk2(f, 0.f)); }
constexpr float LOG2E = 1.44269504f, EPS2 = 1e-6f * LOG2E;
DI f32x2 phi_u(float a0, float a1, f32x2 rl) { return __builtin_elementwise_fma((f32x2){a0, a1}, rl, (f32x2){EPS2, EPS2}); }
#define PHI_E(j0) _Pragma("unroll") for (int j_ = (j0); j_ < (j0) + 2; ++j_) pe[j_] = (f32x2){__builtin_amdgcn_fmed3f(__builtin_amdgcn_exp2f(u[j_].x), 0.f, 1.f), \
                                                                                     __builtin_amdgcn_fmed3f(__builtin_amdgcn_exp2f(u[j_].y), 0.f, 1.f)}; SB
#define PHI_B(o) _Pragma("unroll") for (int j_ = 0; j_ < 4; ++j_) u[j_] = (f32x2){fmaxf(u[j_].x, EPS2), fmaxf(u[j_].y, EPS2)}; SB; \
              _Pragma("unroll") for (int j_ = 0; j_ < 4; ++j_) u[j_] = __builtin_elementwise_fma(u[j_], (f32x2){1.f / LOG2E, 1.f / LOG2E}, pe[j_]); SB; \
              o = (u32x4){pk2(u[0].x, u[0].y), pk2(u[1].x, u[1].y), pk2(u[2].x, u[2].y), pk2(u[3].x, u[3].y)}
DI unsigned relu2(unsigned v) {
  typedef short s16x2 __attribute__((ext_vector_type(2)));
  return __builtin_bit_cast(unsigned, __builtin_elementwise_max(__builtin_bit_cast(s16x2, v), (s16x2){0, 0}));
}
DI float mul1(float a, float b) { float r; asm("v_mul_f32_e32 %0, %1, %2" : "=v"(r) : "v"(a), "v"(b)); return r; }
DI float sum8(float v) {
  v += __uint_as_float(__builtin_amdgcn_update_dpp(0u, __float_as_uint(v), 0xB1, 0xf, 0xf, true));
  v += __uint_as_float(__builtin_amdgcn_update_dpp(0u, __float_as_uint(v), 0x4E, 0xf, 0xf, true));
  v += __uint_as_float(__builtin_amdgcn_update_dpp(0u, __float_as_uint(v), 0x141, 0xf, 0xf, true));
  return v;
}
DI void lds_put(unsigned a, unsigned v) { FENCE; *(volatile LDS unsigned*)a = v; FENCE; }
DI void lds_wait(unsigned a, int n, unsigned need, int lane) {
  const unsigned ad = a + (lane & (n - 1)) * 4;
#pragma unroll 1
  for (unsigned sp = 0; sp < LDS_CAP; ++sp) {
    FENCE; const unsigned v = *(volatile LDS unsigned*)ad; FENCE;
    if (__all(v >= need)) break;
    __builtin_amdgcn_s_sleep(1);
  }
}
DI u64 tag_wait(unsigned voff, u32x4 rs, unsigned soff, u64 mask, u32x2 tag) {
  u64 bl = 0;
#pragma unroll 1
  for (unsigned sp = 0; sp < SPIN_CAP; ++sp) {
    u32x2 v;
    asm volatile("buffer_load_dwordx2 %0, %1, %2, %3 offen sc1\n\ts_waitcnt vmcnt(0)" : "=&v"(v) : "v"(voff), "s"(rs), "s"(soff) : "memory");
    bl = __ballot(v.x == tag.x && v.y == tag.y);
    if ((bl & mask) == mask) break;
    __builtin_amdgcn_s_sleep(4);
  }
  return bl;
}

__global__ __launch_bounds__(512, 2) void fused(const u16* __restrict__ x, const u16* __restrict__ wqkv, const u16* __restrict__ w1, char* ws,
                                                const u16* __restrict__ wo, const u16* __restrict__ b1, const u16* __restrict__ w2,
                                                const u16* __restrict__ b2, u16* __restrict__ out) {
  const int bid = blockIdx.x, tid = threadIdx.x, lane = tid & 63;
  unsigned cpf;
  {
    u64 pc; asm volatile("s_getpc_b64 %0" : "=s"(pc));
    LD1(cpf, (unsigned)(tid >> 6) * 4096u + (unsigned)lane * 32768u, mk_rsrc((const void*)(pc & ~255ull), CODE_BYTES), (unsigned)(bid >> 3) * 128u, "");
  }
  const int w = __builtin_amdgcn_readfirstlane(tid >> 6);
  const int xc = bid & 7, ix = bid >> 3, sl = ix & 7, rl = ix >> 3;
  const int r = (xc >> 1) * 4 + rl, s0 = r >= 14 ? 0 : r >> 1;
  const u32x4 rx = mk_rsrc(x, S * D * 2);
  const unsigned vx0 = (unsigned)(r * 128 + (w & 1) * 64 + (lane >> 3)) * 896u + (unsigned)(((lane & 7) ^ (lane >> 4)) << 4), vx1 = (vx0 ^ 64u) + 7168u;
#define XGROUP(n) do { dma16o<(n) * 2048>(lx, vx0, rx, sg * 128 + (n) * 14336); dma16o<(n) * 2048 + 1024>(lx, vx1, rx, sg * 128 + (n) * 14336); } while (0)
  asm volatile("" :: "s"(wqkv), "s"(w1), "s"(ws), "s"(wo), "s"(w2), "s"(b1), "s"(b2), "s"(out), "v"(vx0), "v"(vx1));
  if (w < 2) { const unsigned lx = w * 8192; const int sg = s0; XGROUP(0); XGROUP(1); XGROUP(2); XGROUP(3); }
  const int c = (int)(((xc & 1) ? 0xEFB98765u : 0xDCA43210u) >> (4 * sl)) & 15;
  const bool kvt = sl >= 6, pure = sl == 7;
  const int rt = xc * 4 + rl, ct = sl + 2 * rl - (sl + 2 * rl >= 7 ? 7 : 0), rot2 = 7 * rl + xc - (7 * rl + xc >= 28 ? 28 : 0);
  const int l31 = lane & 31, hh = lane >> 5, q = (lane >> 2) & 3, p = lane & 3, blk = (lane >> 4) & 1;
  const unsigned slot_b = kvt ? 49152u : 40960u;
  const u32x4 rws = mk_rsrc(ws, WS_END);
  const unsigned v16 = lane * 16;
  u32x2 tag;
  const unsigned swz = (unsigned)(((lane & 7) ^ ((lane >> 2) & 4)) << 4);
  f32x16 z16;
#pragma unroll
  for (int e = 0; e < 16; ++e) z16[e] = 0.f;

#define PUT_TILES(base, t) do { _Pragma("unroll") for (int n_ = 0; n_ < 8; ++n_) *(LDS f32x4*)((base) + n_ * 1024 + (v16 ^ (n_ * 32))) = \
      (f32x4){t[n_ >> 2][4 * (n_ & 3)], t[n_ >> 2][4 * (n_ & 3) + 1], t[n_ >> 2][4 * (n_ & 3) + 2], t[n_ >> 2][4 * (n_ & 3) + 3]}; } while (0)
  if (__builtin_expect(w < 4, 1)) {
    const int h = w, hp = h >> 1, hs_ = h & 1;
    const u32x4 rw2 = mk_rsrc(w2, F * D * 2), rwo = mk_rsrc(wo, D * D * 2);
    const unsigned vwo = (unsigned)(lane >> 3) * 896u + ct * 128 + swz;
    const unsigned vw2 = vwo + (unsigned)((h >> 1) * 896 + (h & 1) * 16) * 896u;
    const int hg = (h + xc + rl) & 3;
    u32x4 R[36];
#define ROT(u) ((u) + rot2 >= 28 ? (u) + rot2 - 28 : (u) + rot2)
#define PFLD(n) LD4(R[n], vw2, rw2, ROT((n) < 12 ? 3 + ((n) >> 1) : 10 + ((n) >> 1)) * 28672u + ((n) & 1) * 7168u, "")
    auto fill = [&](int u, int g, int cnt) {
#pragma unroll 1
      for (int e = u + cnt; u < e; ++u, ++g) {
        const unsigned k = ROT(u) * 28672u;
        dma16(g * 8192 + h * 2048, vw2, rw2, k); dma16(g * 8192 + h * 2048 + 1024, vw2, rw2, k + 7168u);
      }
    };
    {
      __builtin_amdgcn_s_setprio(3);
      const bool a1 = c < 10, b1_ = c < 9;
      const u32x4 r0 = mk_rsrc(a1 ? w1 : wqkv, a1 ? D * F * 2 : D * 3 * D * 2), r1 = mk_rsrc(b1_ ? w1 : wqkv, b1_ ? D * F * 2 : D * 3 * D * 2);
      const unsigned p0 = a1 ? 3584u : 2688u, p1 = b1_ ? 3584u : 2688u;
      unsigned V0, V1, V2, V3;
      {
        const unsigned kl = 32 * hs_ + (lane >> 3);
        const unsigned c0 = (a1 ? c : c - 10) * 384, c1 = c == 9 ? 512 : c0;
        const unsigned vk = (32 * hs_ + (lane >> 4)) * 2688 + ((224 * c - 1792) & ~63) + ((q ^ (lane >> 4)) << 6) + p * 16;
        V0 = kvt ? vk : kl * p0 + c0 + swz;
        V1 = kvt ? vk + 4 * 2688 : kl * p1 + c1 + swz + 128;
        V2 = kvt ? V0 + 896 : V1 + 128;
        V3 = V1 + 896;
      }
      int sg = s0 + hp; sg = sg == 7 ? 0 : sg;
      unsigned so = hp * slot_b;
#define GROUP(n) do { const unsigned kk = sg * 64 + 8 * (n), sa = kk * p0, sb_ = kk * p1; \
          if (i || hp) XGROUP(n); \
          dma16o<16384 + (n) * 1024>(lw, V0, r0, sa); \
          dma16o<24576 + (n) * 1024>(lw, V1, r1, sb_); \
          dma16o<32768 + (n) * 1024>(lw, V2, r1, sb_); \
          if (kvt) dma16o<40960 + (n) * 1024>(lw, V3, r1, sb_); } while (0)
      if (hp) BARRIER;
      unsigned t2;
#pragma unroll
      for (int i = 0; i < 4; ++i) {
        const int t = hp + 2 * i;
        if (i == 3 && hp) break;
        if (i >= 2 || (i == 1 && hp)) lds_wait(SY_DONE, 4, t - 2, lane);
        const unsigned lx = so + hs_ * 8192, lw = so + hs_ * 4096;
        GROUP(0); GROUP(1); GROUP(2); GROUP(3);
        if (i == 0 && !hp) BARRIER;
        if (i == 2 && hp) break;
        if (i == 3) lds_put(SY_GO, 1);
        if (i == 1) LD1(t2, lane < (hp ? 0 : hs_ ? 13 : 49) ? lane * 128u : 0x80000000u, hs_ ? rwo : rw2, bid * (hs_ ? 1664u : 6272u), "");
        if (i == 0 || pure) WAIT_VM(i == 1);
        if (i == 1 && !hp) WAIT_VM(1);
        if (!pure && (i >= 2 || (i == 1 && hp))) {
#pragma unroll
          for (int n = (i == 3 ? P1 : 0); n < (i == 3 ? P2 : P1); ++n) PFLD(n);
        }
        if (i) WAIT_VM(i == 3 ? P2 - P1 : P1);
        if (i == 0) asm volatile("" :: "v"(cpf));
        if (i == 2) asm volatile("" :: "v"(t2));
        lds_put(SY_FULL + h * 4, t + 2);
        so = so == 0 ? 2 * slot_b : so - slot_b;
        sg = sg >= 5 ? sg - 5 : sg + 2;
      }
      tag = *(volatile LDS u32x2*)SY_TAG;
      if (hp) {
        u32x4 xf[8];
        unsigned bi;
        const bool chk = !hs_ || kvt || lane < 32;
        const u32x4 rb1 = mk_rsrc(b1, c < 10 ? F * 2 : 0);
#pragma unroll 1
        for (unsigned sp = 0; sp < SPIN_CAP; ++sp) {
#pragma unroll
          for (int k = 0; k < 7; ++k) LD4(xf[k], v16, rws, WS_CS + c * 14336u + hs_ * 1024u + k * 2048u, "sc1");
          LD4(xf[7], v16, rws, WS_ST + r * 2048u + hs_ * 1024u, "sc1");
          LD1(bi, lane * 4u, rb1, c * 384u + hs_ * 256u, "");
          WAIT_VM(9); lds_put(SY_FULL + h * 4, 7);
          WAIT_VM(0);
          PIN(xf[0]); PIN(xf[7]); PIN(bi);
          if (kvt && l31 == 30) bi = 0x3f80u;
          unsigned bad = (xf[0].y ^ tag.x) | (xf[0].w ^ tag.x);
          float ax_ = __uint_as_float(xf[0].x), ay_ = __uint_as_float(xf[0].z);
#pragma unroll
          for (int k = 1; k < 7; ++k) {
            PIN(xf[k]);
            ax_ += __uint_as_float(xf[k].x); ay_ += __uint_as_float(xf[k].z); PIN(ax_); PIN(ay_);
            bad |= (xf[k].y ^ tag.x) | (xf[k].w ^ tag.x); PIN(bad);
          }
          const unsigned hi = pk2(ax_, ay_); ax_ -= bflo(hi); PIN(ax_); ay_ -= bfhi(hi);
          const unsigned mi = pk2(ax_, ay_); ax_ -= bflo(mi); PIN(ax_); ay_ -= bfhi(mi);
          const unsigned lo = pk2(ax_, ay_);
          *(LDS u32x4*)(L_CS + hs_ * 1024 + v16) = (u32x4){(hi & 0xffffu) | (mi << 16), (lo & 0xffffu) | (bi << 16),
                                                           (hi >> 16) | (mi & 0xffff0000u), (lo >> 16) | (bi & 0xffff0000u)};
          *(LDS unsigned*)(L_ST + hs_ * 256 + lane * 4) = xf[7].x;
          *(LDS float*)(L_RS + hs_ * 256 + lane * 4) = __builtin_amdgcn_rcpf(bfhi(xf[7].x)) * LOG2E;
          if (__builtin_expect(__all((bad == 0 || !chk) && xf[7].y == tag.x), 1)) break;
          __builtin_amdgcn_s_sleep(8);
        }
        lds_put(SY_FULL + h * 4, 8);
        if (!pure) {
#pragma unroll
          for (int n = P1; n < P2; ++n) PFLD(n);
        }
        lds_wait(SY_GO, 1, 1, lane);
        asm volatile("" :: "v"(t2));
      }
      __builtin_amdgcn_s_setprio(0);
    }
    if (!pure) {
#pragma unroll
    for (int n = P2; n < 36; ++n) PFLD(n);
    const unsigned wb = 49152 + (h ^ 2) * 14336;
    lds_wait(SY_DONE, 4, h < 2 ? 6 : 5, lane);
#pragma unroll 1
    for (int n = 0; n < 14; ++n) dma16(wb + n * 1024, vwo, rwo, hg * 100352u + n * 7168u);
    lds_wait(SY_DONE, 4, 6, lane);
    fill(0, 16, 3);
    WAIT_VM(6);
    bf16x8 WO[4][2][2];
    {
      const unsigned a0 = wb + (4 * hh + q) * 128 + (((q >> 1) & 1) << 6) + blk * 32 + p * 8, a1_ = a0 ^ 64u;
#pragma unroll
      for (int n = 0; n < 4; ++n)
#pragma unroll
        for (int sg = 0; sg < 2; ++sg) {
          const unsigned o = n * 3584 + sg * 2048;
          WO[n][sg][0] = tr_frag(a0 + o, a0 + o + 1024); WO[n][sg][1] = tr_frag(a1_ + o, a1_ + o + 1024);
        }
    }
#pragma unroll
    for (int n = 0; n < 8; ++n) {
      u32x4 t = __builtin_bit_cast(u32x4, WO[n >> 1][1][n & 1]);
      if (hh) { t.z = 0; t.w = 0; }
      WO[n >> 1][1][n & 1] = asbf(t);
    }
#pragma unroll
    for (int n = 0; n < 16; ++n) PIN(WO[n >> 2][(n >> 1) & 1][n & 1]);
    lds_put(SY_WOK + h * 4, 1);
    lds_wait(SY_DONE, 4, 7, lane);
#pragma unroll
    for (int n = 0; n < 12; ++n) { PIN(R[n]); *(LDS u32x4*)(h * 2048 + v16 + (n >> 1) * 8192 + (n & 1) * 1024) = R[n]; }
    WAIT_VM(0); lds_put(SY_FULL2 + h * 4, 1);
#pragma unroll
    for (int n = 12; n < 18; ++n) { PIN(R[n]); *(LDS u32x4*)(57344 + h * 2048 + v16 + (n >> 1) * 8192 + (n & 1) * 1024) = R[n]; }
    lds_wait(SY_DONE, 4, c < 10 ? 8 : 7, lane);
    lds_wait(SY_WOK, 4, 1, lane);
    fill(9, 6, 7);
    lds_wait(SY_DONE, 4, 9, lane);
    WAIT_VM(14);
#pragma unroll
    for (int n = 0; n < 18; ++n) { PIN(R[18 + n]); *(LDS u32x4*)((n < 6 ? 131072 : -24576) + h * 2048 + v16 + (n >> 1) * 8192 + (n & 1) * 1024) = R[18 + n]; }
    WAIT_VM(0); lds_put(SY_RDY + h * 4, 1);
    u32x4 PQ[2][7], KV[8];
    __builtin_amdgcn_s_setprio(3);
    lds_wait(SY_HOK, 1, 1, lane);
    if (*(volatile LDS unsigned*)SY_HOK < 2u) tag_wait(min(lane, 4) * FS, rws, WS_FL + (rt * 32 + 19) * FS, ~0ull, tag);
#pragma unroll
    for (int n = 0; n < 7; ++n) { LD4(PQ[n / 7][n % 7], v16, rws, WS_PQ + ((rt * 2 + n / 7) * 28 + hg * 7 + n % 7) * 1024u, "sc1"); __builtin_amdgcn_s_sleep(8); }
    tag_wait(min(lane, 7) * FS, rws, WS_KF + (xc * 32 + hg * 8) * FS, ~0ull, tag);
#pragma unroll
    for (int n = 0; n < 8; ++n) LD4(KV[n], v16, rws, WS_KV + hg * 8192u + n * 1024u, "sc1");
#pragma unroll
    for (int n = 7; n < 14; ++n) LD4(PQ[1][n - 7], v16, rws, WS_PQ + ((rt * 2 + 1) * 28 + hg * 7 + n - 7) * 1024u, "sc1");
    __builtin_amdgcn_s_setprio(1);
    u32x4 PB[2][4];
#define PBS(a) do { _Pragma("unroll") for (int n = 0; n < 7; ++n) PIN(PQ[a][n]); \
      _Pragma("unroll") for (int n = 0; n < 4; ++n) { const int lo = n < 3 ? 2 * n + 1 : 5, hi = n ? 2 * n - 1 : 1; \
        PB[a][n] = (u32x4){PQ[a][lo].x, PQ[a][lo].y, PQ[a][hi].z, PQ[a][hi].w}; PIN(PB[a][n]); } } while (0)
    PBS(0);
    WAIT_VM(11);
#pragma unroll
    for (int n = 0; n < 4; ++n) PIN(KV[n]);
    f32x16 nm[3], as[2][2] = {{z16, z16}, {z16, z16}};
    u32x4 O[2][2]; float rd[2];
#define NUM0(u) nm[(u) % 3] = mfma(asbf(KV[2 * ((u) & 3)]), asbf(PQ[(u) >> 2][2 * ((u) & 3)]), z16)
#define NUM1(u) nm[(u) % 3] = mfma(asbf(KV[2 * ((u) & 3) + 1]), asbf(PB[(u) >> 2][(u) & 3]), nm[(u) % 3])
#define DEN(u) __uint_as_float(__builtin_amdgcn_permlane32_swap(__float_as_uint(nm[(u) % 3][12]), __float_as_uint(nm[(u) % 3][12]), false, false)[1])
#define APP(u, i) as[(u) >> 2][(i) & 1] = mfma(WO[(u) & 3][(i) >> 1][(i) & 1], asbf(O[(u) & 1][(i) >> 1]), as[(u) >> 2][(i) & 1])
#define MULS(a, b) _Pragma("unroll") for (int e = (a); e < (b); ++e) o[e] = mul1(nm[u % 3][e], rd[u & 1])
#define CVTS(a, b) _Pragma("unroll") for (int e = (a); e < (b); ++e) O[u & 1][e >> 2][e & 3] = pk2(o[2 * e], o[2 * e + 1])
    NUM0(0); NUM1(0); NUM0(1); NUM1(1);
    rd[0] = __builtin_amdgcn_rcpf(DEN(0) + 1e-6f);
    asm volatile("s_waitcnt vmcnt(7)" : "+v"(rd[0]), "+v"(KV[4]), "+v"(KV[5]), "+v"(KV[6]), "+v"(KV[7]) :: "memory");
    SB;
    unsigned d2 = 0; bool put0 = false;
#pragma unroll
    for (int u = 0; u < 8; ++u) {
      float o[16], dn;
      if (u) APP(u - 1, 0); SB; MULS(0, 6); SB;
      if (u) APP(u - 1, 1); SB; MULS(6, 10); CVTS(0, 2); SB;
      if (u) APP(u - 1, 2); SB; MULS(10, 13); CVTS(2, 4); SB;
      if (u) APP(u - 1, 3); SB; MULS(13, 16); CVTS(4, 6); SB;
      if (u == 2) { WAIT_VM(0); PBS(1); SB; }
      if (u < 6) NUM0(u + 2); SB; if (u < 7) dn = DEN(u + 1) + 1e-6f; SB;
      if (u < 6) NUM1(u + 2); SB; if (u < 7) rd[(u + 1) & 1] = __builtin_amdgcn_rcpf(dn); SB; CVTS(6, 8);
      if (u == 4) d2 = *(volatile LDS unsigned*)(SY_DONE2 + p * 4);
      if (u == 5) { put0 = __all(d2 >= 1u); if (put0) PUT_TILES(49152 + h * 16384, as[0]); }
      SB;
    }
    APP(7, 0); APP(7, 2); APP(7, 1); APP(7, 3);
    if (!put0) { lds_wait(SY_DONE2, 4, 1, lane); PUT_TILES(49152 + h * 16384, as[0]); }
    PUT_TILES(57344 + h * 16384, as[1]);
    BARRIER;
    }
  } else {
  const int cw = w - 4, wl = cw & 1, wh = cw >> 1;
  u32x4 xs[7];
  if (cw == 3) {
    const unsigned va = (unsigned)(r * 128 + c * 8 + (lane >> 3)) * 896u + (lane & 7) * 16;
#pragma unroll
    for (int k = 0; k < 7; ++k) LD4(xs[k], va, rx, k * 128u, "");
  }
  *(LDS unsigned*)(L_SY + lane * 4) = (lane >> 1) == 1; BARRIER;
  __builtin_amdgcn_s_setprio(2);
  bf16x8 ones = asbf((u32x4){0x3f803f80u, 0x3f803f80u, 0x3f803f80u, 0x3f803f80u});
  PIN(ones);
  {
    {
      u64 ep;
      asm volatile("s_load_dwordx2 %0, %1, 0x0\n\ts_waitcnt lgkmcnt(0) vmcnt(0)" : "=&s"(ep) : "s"(ws) : "memory");
      tag.x = (unsigned)ep + 1u; tag.y = (unsigned)(ep >> 32) + 1u;
    }
    asm volatile("" :: "v"(cpf));
    if (cw == 0) *(volatile LDS u32x2*)SY_TAG = tag;
    if (cw == 3) {
      float sq[2] = {0.f, 0.f}, sm[2] = {0.f, 0.f};
      const bf16x2 one2 = __builtin_bit_cast(bf16x2, 0x3f803f80u);
#pragma unroll
      for (int k = 0; k < 7; ++k) {
        PIN(xs[k]);
#pragma unroll
        for (int e = 0; e < 4; ++e) {
          const unsigned u = xs[k][e];
          const bf16x2 v = __builtin_bit_cast(bf16x2, u);
          sq[e & 1] = __builtin_amdgcn_fdot2_f32_bf16(v, v, sq[e & 1], false); sm[e & 1] = __builtin_amdgcn_fdot2_f32_bf16(v, one2, sm[e & 1], false);
        }
      }
      const float m = sum8(sm[0] + sm[1]) * (1.f / D);
      const float var = fmaxf(sum8(sq[0] + sq[1]) * (1.f / D) - m * m, 0.f);
      const unsigned sv = __builtin_amdgcn_ds_bpermute(lane * 32, pk2(-m, __builtin_amdgcn_sqrtf(rbf(rbf(var) + 1e-5f))));
      const u32x4 gr = {sv, tag.x, sv, tag.x};
      if (lane < 8) ST4(gr, v16, rws, WS_ST + (r * 128 + c * 8) * 16, "sc1");
    }
  }
  const unsigned fl_off = WS_FL + ((r * 2 + wl) * 32 + 2 * c + wh) * FS;
  const unsigned fa = SY_FULL + p * 4;
#define CHECK_FULL(v, need) do { if (__builtin_expect(__any((v) < (need)), 0)) lds_wait(SY_FULL, 4, need, lane); FENCE; } while (0)
#define COLSUM(a_, t_, tile) do { const unsigned a = (a_); bf16x8 f[4]; \
    _Pragma("unroll") for (int ks = 0; ks < 4; ++ks) f[ks] = tr_frag(a + ks * 2048, a + ks * 2048 + (t_)); \
    SB; f32x16 cs = z16; \
    _Pragma("unroll") for (int ks = 0; ks < 4; ++ks) cs = mfma(ones, f[ks], cs); \
    const u32x4 gr = {(unsigned)__builtin_amdgcn_ds_bpermute(lane * 8, __float_as_int(cs[0])), tag.x, (unsigned)__builtin_amdgcn_ds_bpermute(lane * 8 + 4, __float_as_int(cs[0])), tag.x}; \
    if (lane < 16) ST4(gr, v16, rws, WS_CS + (c * 7 + s0) * 2048 + (tile) * 256, "sc1"); } while (0)
  unsigned da = SY_DONE + cw * 4;
  PIN(da);
  const unsigned xm = hh ? 0u : ~0u;
  if (!kvt) {
    const unsigned xa = (wl * 64 + l31) * 128 + ((hh ^ ((l31 >> 1) & 7)) << 4);
    const unsigned wr = 16384 + (8 * hh + q) * 128 + blk * 32 + p * 8;
    const unsigned wa0 = wr + wh * 8192 + ((wh ^ (q >> 1)) << 6), wa1 = wr + wh * 16384 + ((wh ^ 1 ^ (q >> 1)) << 6);
    f32x16 acc[2][3] = {{z16, z16, z16}, {z16, z16, z16}};
    bf16x8 X[4][2], W[4][3];
#define RDA(n, ks) do { \
      if ((n) == 0 || (n) == 2) X[ks][(n) >> 1] = ld_frag(ax[ks] + ((n) >> 1) * 4096); \
      else { const unsigned a_ = ((n) == 3 ? a1_ : a0_) + ((n) == 4 ? 8192 : 0) + (ks) * 2048; W[ks][(n) == 1 ? 0 : (n) - 2] = tr_frag(a_, a_ + 512); } } while (0)
#define MMA(ks, m) acc[(m) & 1][(m) >> 1] = mfma(W[ks][(m) >> 1], X[ks][(m) & 1], acc[(m) & 1][(m) >> 1])
    unsigned ax[4] = {xa, xa ^ 32u, xa ^ 64u, xa ^ 96u}, a0_ = wa0, a1_ = wa1;
#pragma unroll
    for (int n = 0; n < 6; ++n) PIN(acc[n & 1][n >> 1]);
    lds_wait(SY_FULL, 4, 1, lane);
    if (r < 14 && !((r & 1) && wl)) {
      const int b = (r & 1) ? 2 : wl;
      COLSUM(b == 1 ? wa1 : wa0 + b * 4096, 512, 3 * wh + b);
    }
#pragma unroll
    for (int n = 0; n < 10; ++n) { RDA(n % 5, n / 5); SB; }
    const int tla = c >= 10 ? 2 : (xc & 1) ? 3 : 4, tlb = c >= 10 ? 2 : 4;
    unsigned tv = 1; int ts = 0;
    PIN(tv);
#pragma unroll 1
    for (;;) {
      unsigned fv; int dl;
#pragma unroll
      for (int m = 0; m < 12; ++m) {
        MMA(m / 6, m % 6); SB;
        if (m % 6 < 5) RDA(m % 6, 2 + m / 6);
        if (m == 0) { dl = ((0x24 >> ts) & 1) ? -81920 : 40960; ++ts; asm volatile("" : "+s"(dl), "+s"(ts)); }
        if (m == 2) { ax[0] += dl; ax[1] += dl; PIN(ax[0]); PIN(ax[1]); }
        if (m == 5) { if (ts <= tla) __builtin_amdgcn_s_sleep(4); fv = *(volatile LDS unsigned*)fa; }
        if (m == 10) *(volatile LDS unsigned*)da = tv;
        if (m == 11) { a0_ += dl; a1_ += dl; ++tv; CHECK_FULL(fv, tv); }
        SB;
      }
      if (__builtin_expect(ts == 7, 0)) break;
#pragma unroll
      for (int m = 0; m < 12; ++m) {
        MMA(2 + m / 6, m % 6); SB;
        if (m % 6 < 5) RDA(m % 6, m / 6);
        if (m == 5) { ax[2] += dl; ax[3] += dl; if (ts <= tlb) __builtin_amdgcn_s_sleep(4); }
        SB;
      }
    }
    unsigned st[2]; u32x2 cv[3];
#pragma unroll
    for (int a = 0; a < 2; ++a) st[a] = *(LDS const unsigned*)(L_ST + (wl * 64 + a * 32 + l31) * 4);
#pragma unroll
    for (int b = 0; b < 3; ++b) cv[b] = *(LDS const u32x2*)(L_CS + ((3 * wh + b) * 32 + l31) * 8);
    SB;
    MMA(2, 0); MMA(2, 1); MMA(3, 0); MMA(3, 1);
    SB;
    bf16x8 xe[2];
#pragma unroll
    for (int a = 0; a < 2; ++a) xe[a] = asbf((u32x4){((st[a] & 0xffffu) | (st[a] << 16)) & xm, st[a] & xm, 0u, 0u});
#define MME(m) acc[(m) & 1][(m) >> 1] = mfma(asbf((u32x4){cv[(m) >> 1].x, cv[(m) >> 1].y, cv[(m) >> 1].x, cv[(m) >> 1].y}), xe[(m) & 1], acc[(m) & 1][(m) >> 1])
    MME(0); MME(1);
    SB;
    const int R0 = r * 4 + wl * 2;
    const unsigned sk = c == 9 ? 0x40000000u : 0u;
#define MM6(b, j) do { if ((j) < 4) MMA(2 + ((j) >> 1), 2 * (b) + ((j) & 1)); else MME(2 * (b) + ((j) & 1)); } while (0)
    if (2 * c + wh < 19) {
      const unsigned ob = WS_H + (R0 * 112 + 12 * c + 6 * wh) * 1024u;
#pragma unroll
      for (int b = 0; b < 3; ++b)
#pragma unroll
        for (int n = 0; n < 4; ++n) {
          const int a = n >> 1, e = 8 * (n & 1);
          float t[8]; u32x4 o;
          if (b < 2) MM6(b + 1, n < 2 ? 2 * n : n + 2);
          SB;
          if (n == 0) asm volatile("s_nop 10" : "+v"(acc[0][b]), "+v"(acc[1][b]));
          asm volatile("v_mul_f32_e64 %0, %8, |%8|\n\tv_mul_f32_e64 %1, %9, |%9|\n\tv_mul_f32_e64 %2, %10, |%10|\n\tv_mul_f32_e64 %3, %11, |%11|\n\t"
                       "v_mul_f32_e64 %4, %12, |%12|\n\tv_mul_f32_e64 %5, %13, |%13|\n\tv_mul_f32_e64 %6, %14, |%14|\n\tv_mul_f32_e64 %7, %15, |%15|"
                       : "=&v"(t[0]), "=&v"(t[1]), "=&v"(t[2]), "=&v"(t[3]), "=&v"(t[4]), "=&v"(t[5]), "=&v"(t[6]), "=&v"(t[7])
                       : "v"(acc[a][b][e]), "v"(acc[a][b][e + 1]), "v"(acc[a][b][e + 2]), "v"(acc[a][b][e + 3]), "v"(acc[a][b][e + 4]), "v"(acc[a][b][e + 5]),
                         "v"(acc[a][b][e + 6]), "v"(acc[a][b][e + 7]));
          SB;
          if (b < 2 && n < 2) MM6(b + 1, 2 * n + 1);
          SB;
#pragma unroll
          for (int j = 0; j < 4; ++j) o[j] = pk2(t[2 * j], t[2 * j + 1]);
#pragma unroll
          for (int j = 0; j < 4; ++j) o[j] = relu2(o[j]);
          ST4(o, v16, rws, ob + a * (112 * 1024u) + (2 * b + (n & 1)) * 1024u + (b == 2 ? sk : 0u), "sc1");
          SB;
        }
    } else {
#pragma unroll
      for (int j = 0; j < 12; ++j) MM6(1 + j / 6, j % 6);
      const unsigned ob = WS_PQ + (R0 * 28 + (c == 9 ? 22 : (c - 10) * 12 + wh * 6)) * 1024u;
      SB;
#pragma unroll
      for (int a = 0; a < 2; ++a) {
        const float rl = __builtin_amdgcn_rcpf(bfhi(st[a])) * LOG2E;
#pragma unroll
        for (int n = 0; n < 6; ++n) {
          f32x2 u[4], pe[4]; u32x4 o;
#pragma unroll
          for (int j = 0; j < 4; ++j) u[j] = phi_u(acc[a][n >> 1][8 * (n & 1) + 2 * j], acc[a][n >> 1][8 * (n & 1) + 2 * j + 1], (f32x2){rl, rl});
          SB; PHI_E(0); PHI_E(2); PHI_B(o);
          ST4(o, v16, rws, ob + a * (28 * 1024u) + n * 1024u + (n < 2 ? sk : 0u), "sc1");
          SB;
        }
      }
    }
  } else {
    const unsigned xa = l31 * 128 + ((hh ^ ((l31 >> 1) & 7)) << 4);
    const int ok_ = (c & 1) * 32 + 56 * cw + 8 * ((4 * blk + p + cw) & 7), ov_ = (c & 1) * 32 + 56 * cw + 32 * blk + 8 * p;
    const unsigned ka = 16384 + hh * 1024 + q * 256 + ((((ok_ >> 6) & 3) ^ q) << 6) + (ok_ & 63);
    const unsigned va = 32768 + hh * 1024 + q * 256 + ((((ov_ >> 6) & 3) ^ q) << 6) + (ov_ & 63) + ((lane & 19) == 19 ? 0x100000 : 0);
    f32x16 acc[4][2] = {{z16, z16}, {z16, z16}, {z16, z16}, {z16, z16}};
    bf16x8 X[4][4], W[4][2];
#define RDK(n, ks) do { \
      if ((n) == 1 || (n) == 5) { const unsigned a_ = ((n) == 1 ? a0_ : a1_) + (ks) * 2048; W[ks][(n) >> 2] = tr_frag(a_, a_ + 8192); } \
      else X[ks][(n) ? (n) - 1 : 0] = ld_frag(ax[ks] + ((n) ? (n) - 1 : 0) * 4096); } while (0)
#define MMK(ks, m) acc[(m) & 3][(m) >> 2] = mfma(X[ks][(m) & 3], W[ks][(m) >> 2], acc[(m) & 3][(m) >> 2])
    unsigned ax[4] = {xa, xa ^ 32u, xa ^ 64u, xa ^ 96u}, a0_ = ka, a1_ = va;
#pragma unroll
    for (int n = 0; n < 8; ++n) PIN(acc[n & 3][n >> 2]);
    lds_wait(SY_FULL, 4, 1, lane);
    if (r < 14) COLSUM((r & 1) ? va : ka, 8192, 2 * cw + (r & 1));
#pragma unroll
    for (int n = 0; n < 12; ++n) { RDK(n % 6, n / 6); SB; }
    unsigned tv = 1; int ts = 0;
    PIN(tv);
#pragma unroll 1
    for (;;) {
      unsigned fv; int dl;
#pragma unroll
      for (int m = 0; m < 16; ++m) {
        MMK(m >> 3, m & 7); SB;
        if ((m & 7) >= 1 && (m & 7) < 7) RDK((m & 7) - 1, 2 + (m >> 3));
        if (m == 0) { dl = ((0x24 >> ts) & 1) ? -98304 : 49152; ++ts; asm volatile("" : "+s"(dl), "+s"(ts)); }
        if (m == 7) { ax[0] += dl; ax[1] += dl; PIN(ax[0]); PIN(ax[1]); }
        if (m == 8) { if (ts <= 2) __builtin_amdgcn_s_sleep(4); fv = *(volatile LDS unsigned*)fa; }
        if (m == 14) *(volatile LDS unsigned*)da = tv;
        if (m == 15) { a0_ += dl; a1_ += dl; ++tv; CHECK_FULL(fv, tv); }
        SB;
      }
      if (__builtin_expect(ts == 7, 0)) break;
#pragma unroll
      for (int m = 0; m < 16; ++m) {
        MMK(2 + (m >> 3), m & 7); SB;
        if ((m & 7) >= 1 && (m & 7) < 7) RDK((m & 7) - 1, m >> 3);
        if (m == 7) { ax[2] += dl; ax[3] += dl; }
        SB;
      }
    }
    unsigned st[4]; u32x2 cv[2]; f32x4 rsv[2][4];
#pragma unroll
    for (int a = 0; a < 4; ++a) st[a] = *(LDS const unsigned*)(L_ST + (a * 32 + l31) * 4);
#pragma unroll
    for (int b = 0; b < 2; ++b) cv[b] = *(LDS const u32x2*)(L_CS + ((2 * cw + b) * 32 + l31) * 8);
    unsigned rsa = L_RS + hh * 16;
    PIN(rsa);
#define MK6(a, j) do { if ((j) < 4) MMK(2 + ((j) >> 1), (a) + 4 * ((j) & 1)); else acc[a][(j) & 1] = mfma(asbf((u32x4){((st[a] & 0xffffu) | (st[a] << 16)) & xm, st[a] & xm, 0u, 0u}), \
        asbf((u32x4){cv[(j) & 1].x, cv[(j) & 1].y, cv[(j) & 1].x, cv[(j) & 1].y}), acc[a][(j) & 1]); } while (0)
#pragma unroll
    for (int n = 0; n < 4; ++n) rsv[0][n] = *(LDS const f32x4*)(rsa + n * 32);
    SB;
#pragma unroll
    for (int j = 0; j < 6; ++j) MK6(0, j);
    SB;
    f32x16 kv = z16;
    u32x4 fk, fv;
#pragma unroll
    for (int gi = 0; gi < 8; ++gi) {
      const int a = gi >> 1, sg = gi & 1, e = 8 * sg;
      if (a < 3 && sg == 0) {
#pragma unroll
        for (int n = 0; n < 4; ++n) rsv[(a + 1) & 1][n] = *(LDS const f32x4*)(rsa + (a + 1) * 128 + n * 32);
      }
      const f32x4 ra_ = rsv[a & 1][2 * sg], rb_ = rsv[a & 1][2 * sg + 1];
      const f32x2 rl[4] = {{ra_[0], ra_[1]}, {ra_[2], ra_[3]}, {rb_[0], rb_[1]}, {rb_[2], rb_[3]}};
      f32x2 u[4], vv[4], pe[4];
#pragma unroll
      for (int j = 0; j < 4; ++j) u[j] = phi_u(acc[a][0][e + 2 * j], acc[a][0][e + 2 * j + 1], rl[j]);
#pragma unroll
      for (int j = 0; j < 4; ++j) vv[j] = (f32x2){acc[a][1][e + 2 * j], acc[a][1][e + 2 * j + 1]} * rl[j];
      SB;
      if (gi) kv = mfma(asbf(fk), asbf(fv), kv);
      SB;
      PHI_E(0);
      if (a < 3) MK6(a + 1, 3 * sg);
      SB;
      PHI_E(2);
      if (a < 3) MK6(a + 1, 3 * sg + 1);
      SB;
      fv = (u32x4){pk2(vv[0].x, vv[0].y), pk2(vv[1].x, vv[1].y), pk2(vv[2].x, vv[2].y), pk2(vv[3].x, vv[3].y)};
      SB;
      if (a < 3) MK6(a + 1, 3 * sg + 2);
      SB;
      PHI_B(fk);
      SB;
    }
    kv = mfma(asbf(fk), asbf(fv), kv);
    kv = kv * (l31 == 28 ? 1.f / 1.4453125f : 1.f / LOG2E);
    const unsigned ko = WS_KVP + (((c - 12) * 4 + cw) * 16 + r) * 4096u;
#pragma unroll
    for (int e = 0; e < 4; ++e) {
      const u32x4 o = {pk2(kv[4 * e], kv[4 * e + 1]), tag.x, pk2(kv[4 * e + 2], kv[4 * e + 3]), tag.x};
      ST4(o, v16, rws, ko + e * 1024u, "sc1");
    }
  }
  lds_put(da, 8);
  bf16x8 WH[9][4];
  unsigned ra[3][2];
#pragma unroll
  for (int cb = 0; cb < 2; ++cb) {
    ra[0][cb] = wh * 4096 + (4 * hh + q) * 128 + ((cb ^ (q >> 1)) << 6) + blk * 32 + p * 8; ra[1][cb] = ra[0][cb] + 65536; ra[2][cb] = ra[0][cb] + 131072;
    PIN(ra[0][cb]); PIN(ra[1][cb]); PIN(ra[2][cb]);
  }
#define RDW(dst, n, reg) do { const unsigned a_ = ra[(reg) >> 3][(n) & 1] + ((reg) & 7) * 8192 + ((n) >> 1) * 2048; dst[n] = tr_frag(a_, a_ + 1024); } while (0)
  if (!kvt) WAIT_VM(0);
  if (lane < 8) ST4(((u32x4){tag.x, tag.y, tag.x, tag.y}), v16, rws, fl_off, "sc1");
  if (!pure) {
    lds_wait(SY_FULL2, 4, 1, lane);
#pragma unroll
    for (int n = 0; n < 36; ++n) RDW(WH[n >> 2], n & 3, (16 + (n >> 2)) % 19);
    lds_put(da, 9);
  }

  if (!pure) {
    const int kh = wh, R = rt * 2 + wl;
    if (cw == 0) {
      if (c < 10) __builtin_amdgcn_s_sleep(12);
      const u64 bl = tag_wait(min(lane, 23) * FS, rws, WS_FL + rt * 32 * FS, 0x7FFFFull, tag);
      lds_put(SY_HOK, (bl & 0xF80000ull) == 0xF80000ull ? 2 : 1);
    } else lds_wait(SY_HOK, 1, 1, lane);
    WAIT_VM(0);
    const unsigned hs = WS_H + (R * 112 + kh * 56) * 1024u;
    constexpr int NH = NHW, E = 28 - NH;
    u32x4 hf[NH][2];
#define HLD(u) do { const unsigned o_ = hs + ROT(u) * 2048u; \
      LD4(hf[(u) % NH][0], v16, rws, o_, "sc1"); LD4(hf[(u) % NH][1], v16, rws, o_, "offset:1024 sc1"); } while (0)
#pragma unroll
    for (int u = 0; u < NH; ++u) HLD(u);
    u32x4 xr[2], br; unsigned sg_[2];
    const unsigned fxo = (unsigned)(rt * 64 + cw * 8 + (lane >> 3)) * 896u + ct * 128 + (lane & 7) * 16;
#define FIN_LD do { LD4(xr[0], fxo, rx, 0u, ""); LD4(xr[1], fxo, rx, 28672u, ""); LD4(br, (lane & 7) * 16u, mk_rsrc(b2, D * 2), ct * 128u, ""); \
      LD1(sg_[0], (unsigned)(lane & ~7) * 2u, rws, WS_ST + rt * 1024u + cw * 128u, "sc1"); LD1(sg_[1], (unsigned)(lane & ~7) * 2u, rws, WS_ST + rt * 1024u + cw * 128u, "offset:512 sc1"); } while (0)
    f32x16 af[2] = {z16, z16};
    bf16x8 W[LA + 1][4];
#pragma unroll
    for (int u = 0; u < 28; ++u) {
      constexpr auto total = [](int u_) { return 2 * NH + 2 * (u_ < E ? u_ : E) + (u_ > E ? 5 : 0); };
      asm volatile("s_waitcnt vmcnt(%2)" : "+v"(hf[u % NH][0]), "+v"(hf[u % NH][1]) : "n"(total(u) - 2 - 2 * u) : "memory");
      if (u == 9 - LA) { lds_wait(SY_RDY, 4, 1, lane); }
#pragma unroll
      for (int n = 0; n < 4; ++n) {
        af[n & 1] = mfma(u < 9 ? WH[u < 9 ? u : 0][n] : W[u % (LA + 1)][n], asbf(hf[u % NH][n >> 1]), af[n & 1]); SB;
        if (u + LA >= 9 && u + LA < 28) RDW(W[(u + LA) % (LA + 1)], n, (u + LA - 3) % 19);
        SB;
      }
      if (u == 16 - LA) lds_put(SY_DONE2 + cw * 4, 1);
      if (u == 20 - LA) lds_put(SY_DONE2 + cw * 4, 2);
      if (u < E) HLD(u + NH);
      else if (u == E) FIN_LD;
    }
    __builtin_amdgcn_s_setprio(0);
    lds_wait(SY_DONE2, 4, 2, lane);
    PUT_TILES(114688 + cw * 8192, af);
    lds_put(SY_DONE2 + cw * 4, 3);
    WAIT_VM(0);
    unsigned a0 = 49152 + (lane & 7) * 1024 + (((cw * 8 + (lane >> 3)) ^ ((lane & 7) * 2)) << 4), a1 = a0 + 65536;
    PIN(a0); PIN(a1); PIN(br);
    lds_wait(SY_DONE2, 4, 3, lane);
    float gt[2][8];
    {
      f32x4 f[2][2][2];
#pragma unroll
      for (int n = 0; n < 8; ++n) f[n >> 2][(n >> 1) & 1][n & 1] = *(LDS const f32x4*)(a1 + (n >> 2) * 8192 + ((n >> 1) & 1) * 16384 + (n & 1) * 512);
      SB;
#pragma unroll
      for (int a = 0; a < 2; ++a) {
        PIN(sg_[a]);
        const float rs = __builtin_amdgcn_rcpf(bfhi(sg_[a])), rs2 = rs * rs;
#pragma unroll
        for (int e = 0; e < 8; ++e) {
          float t = f[a][0][e >> 2][e & 3] + f[a][1][e >> 2][e & 3]; PIN(t);
          t = fmaf(t, rs2, (e & 1) ? bfhi(br[e >> 1]) : bflo(br[e >> 1])); PIN(t);
          gt[a][e] = t * __builtin_amdgcn_rcpf(1.f + __expf(-t)); PIN(gt[a][e]);
        }
      }
    }
    float xv[2][8];
#pragma unroll
    for (int n = 0; n < 16; ++n) { if (!(n & 7)) PIN(xr[n >> 3]); xv[n >> 3][n & 7] = (n & 1) ? bfhi(xr[n >> 3][(n & 7) >> 1]) : bflo(xr[n >> 3][(n & 7) >> 1]); PIN(xv[n >> 3][n & 7]); }
    BARRIER;
    f32x4 sp[2][4][2];
#pragma unroll
    for (int n = 0; n < 16; ++n) sp[n >> 3][(n >> 1) & 3][n & 1] = *(LDS const f32x4*)(a0 + (n >> 3) * 8192 + ((n >> 1) & 3) * 16384 + (n & 1) * 512);
    SB;
#pragma unroll
    for (int a = 0; a < 2; ++a) {
      const f32x4 s0_ = (sp[a][0][0] + sp[a][1][0]) + (sp[a][2][0] + sp[a][3][0]), s1_ = (sp[a][0][1] + sp[a][1][1]) + (sp[a][2][1] + sp[a][3][1]);
      u32x4 ov;
#pragma unroll
      for (int e = 0; e < 4; ++e) {
        const f32x4 sv = e < 2 ? s0_ : s1_;
        ov[e] = pk2(fmaf(sv[2 * (e & 1)], gt[a][2 * e], xv[a][2 * e]), fmaf(sv[2 * (e & 1) + 1], gt[a][2 * e + 1], xv[a][2 * e + 1]));
      }
      ST4(ov, fxo, mk_rsrc(out, S * D * 2), a * 28672u, "sc0 sc1");
    }
  }
  }
  if (pure) {
    const int id = xc * 4 + (ix >> 3), hd = id >> 1;
    lds_wait(SY_DONE, 4, 8, lane);
    f32x4 a, b;
#pragma unroll 1
    for (unsigned sp = 0; sp < SPIN_CAP; ++sp) {
      u32x4 pr[4];
#pragma unroll
      for (int n = 0; n < 4; ++n) LD4(pr[n], v16, rws, WS_KVP + (hd * 16 + 2 * w + (n >> 1)) * 4096u + (id & 1) * 2048u + (n & 1) * 1024u, "sc1");
      WAIT_VM(0);
      unsigned bad = 0;
#pragma unroll
      for (int n = 0; n < 4; ++n) { PIN(pr[n]); bad |= (pr[n].y ^ tag.x) | (pr[n].w ^ tag.x); }
#define F4(n) (f32x4){bflo(pr[n].x), bfhi(pr[n].x), bflo(pr[n].z), bfhi(pr[n].z)}
      a = F4(0) + F4(2); b = F4(1) + F4(3);
      if (__all(bad == 0)) break;
      __builtin_amdgcn_s_sleep(4);
    }
    *(LDS f32x4*)(w * 2048 + v16) = a; *(LDS f32x4*)(w * 2048 + 1024 + v16) = b;
    BARRIER;
    if (w == 4) {
      a = *(LDS const f32x4*)v16; b = *(LDS const f32x4*)(1024 + v16);
#pragma unroll
      for (int n = 1; n < 8; ++n) { a += *(LDS const f32x4*)(n * 2048 + v16); b += *(LDS const f32x4*)(n * 2048 + 1024 + v16); }
      u32x4 o = {pk2(a[0], a[1]), pk2(a[2], a[3]), pk2(b[0], b[1]), pk2(b[2], b[3])};
      if ((id & 1) && hh != (hd & 1)) { if (hd & 2) { o.x = 0; o.y = 0; } else { o.z = 0; o.w = 0; } }
      ST4(o, v16, rws, WS_KV + id * 1024u, "sc1");
      WAIT_VM(0);
      ST4(((u32x4){tag.x, tag.y, tag.x, tag.y}), (lane >> 3) * 32 * FS + (lane & 7) * 16, rws, WS_KF + id * FS, "sc1");
    } else if (w == 5 && bid == 255) {
#pragma unroll 1
      for (int n = 0; n < 4; ++n) tag_wait((unsigned)((lane >> 4) * 64 + (lane & 15) * 2) * FS, rws, WS_FL + n * 256 * FS, ~0ull, tag);
      if (lane == 0) ST2(tag, 0u + lane, rws, WS_EP, "sc1");
    }
  }
}

extern "C" void kernel_launch(void* const* d_in, const int* in_sizes, int n_in, void* d_out, int out_size, void* d_ws,
                              size_t ws_size, hipStream_t stream) {
  (void)hipFuncSetAttribute((const void*)fused, hipFuncAttributeMaxDynamicSharedMemorySize, L_END);
  fused<<<256, 512, L_END, stream>>>((const u16*)d_in[0], (const u16*)d_in[1], (const u16*)d_in[4], (char*)d_ws, (const u16*)d_in[2],
                                         (const u16*)d_in[5], (const u16*)d_in[6], (const u16*)d_in[7], (u16*)d_out);
}
```

```cpp
#include <hip/hip_runtime.h>
#include <hip/hip_bf16.h>
#include <stdint.h>
#pragma clang diagnostic ignored "-Wint-to-pointer-cast"

typedef unsigned short u16;
typedef unsigned long long u64;
typedef __bf16 bf16x8 __attribute__((ext_vector_type(8)));
typedef __bf16 bf16x2 __attribute__((ext_vector_type(2)));
typedef short s16x4 __attribute__((ext_vector_type(4)));
typedef float f32x4 __attribute__((ext_vector_type(4)));
typedef float f32x16 __attribute__((ext_vector_type(16)));
typedef float f32x2 __attribute__((ext_vector_type(2)));
typedef unsigned u32x4 __attribute__((ext_vector_type(4)));
typedef unsigned u32x2 __attribute__((ext_vector_type(2)));
#define LDS __attribute__((address_space(3)))
#define DI __device__ __forceinline__

constexpr int S = 2048, D = 448, F = 1792;
constexpr unsigned FS = 128;
constexpr int Q1 = 8, Q2 = 16, Q3 = 26, NHW = 7, LA = 2;
constexpr unsigned WS_EP = 0;
constexpr unsigned WS_KF = 1024;
constexpr unsigned WS_FL = WS_KF + 256 * FS;
constexpr unsigned WS_ST = WS_FL + 1024 * FS;
constexpr unsigned WS_CS = WS_ST + 32768;
constexpr unsigned WS_KV = WS_CS + 16 * 7 * 2048;
constexpr unsigned WS_KVP = WS_KV + 32768;
constexpr unsigned WS_PQ = WS_KVP + 16 * 16 * 4096;
constexpr unsigned WS_H = WS_PQ + 64 * 28 * 1024;
constexpr unsigned WS_END = WS_H + 64 * 112 * 1024;
constexpr unsigned L_CS = 155648, L_ST = L_CS + 2048, L_RS = L_ST + 512, L_SY = L_RS + 512, L_END = L_SY + 256;
constexpr unsigned SY_FULL = L_SY, SY_DONE = L_SY + 32, SY_RDY = L_SY + 48, SY_FULL2 = L_SY + 64, SY_DONE2 = L_SY + 80,
                   SY_GO = L_SY + 112, SY_WOK = L_SY + 128, SY_HOK = L_SY + 100, SY_TAG = L_SY + 104;
constexpr unsigned CODE_BYTES = 32128;
constexpr unsigned SPIN_CAP = 1u << 19, LDS_CAP = 1u << 23;

DI u32x4 mk_rsrc(const void* p, unsigned bytes) {
  uint64_t a = (uint64_t)p;
  u32x4 r; r.x = (unsigned)a; r.y = (unsigned)(a >> 32) & 0xffffu; r.z = bytes; r.w = 0x00020000u; return r;
}
DI void dma16(unsigned lds_addr, unsigned voff, u32x4 rsrc, unsigned soff) {
  asm volatile("s_mov_b32 m0, %0\n\ts_nop 0\n\tbuffer_load_dwordx4 %1, %2, %3 offen lds"
               :: "s"(lds_addr), "v"(voff), "s"(rsrc), "s"(soff) : "memory");
}
template <unsigned OFF> DI void dma16o(unsigned lds_base, unsigned voff, u32x4 rsrc, unsigned soff) {
  asm volatile("s_add_u32 m0, %0, %4\n\ts_nop 0\n\tbuffer_load_dwordx4 %1, %2, %3 offen lds"
               :: "s"(lds_base), "v"(voff), "s"(rsrc), "s"(soff), "n"(OFF) : "memory", "scc");
}
#define SB __builtin_amdgcn_sched_barrier(0)
#define FENCE asm volatile("" ::: "memory")
#define PIN(a) asm volatile("" : "+v"(a))
#define WAIT_VM(n) asm volatile("s_waitcnt vmcnt(%0)" :: "n"(n) : "memory")
#define BARRIER asm volatile("s_waitcnt lgkmcnt(0)\n\ts_barrier" ::: "memory")
#define LD4(dst, voff, rsrc, soff, mod) asm volatile("buffer_load_dwordx4 %0, %1, %2, %3 offen " mod : "=v"(dst) : "v"(voff), "s"(rsrc), "s"(soff) : "memory")
#define LD1(dst, voff, rsrc, soff, mod) asm volatile("buffer_load_dword %0, %1, %2, %3 offen " mod : "=v"(dst) : "v"(voff), "s"(rsrc), "s"(soff) : "memory")
#define ST4(src, voff, rsrc, soff, mod) asm volatile("buffer_store_dwordx4 %0, %1, %2, %3 offen " mod "\n\ts_nop 1" :: "v"(src), "v"(voff), "s"(rsrc), "s"(soff) : "memory")
#define ST2(src, voff, rsrc, soff, mod) asm volatile("buffer_store_dwordx2 %0, %1, %2, %3 offen " mod :: "v"(src), "v"(voff), "s"(rsrc), "s"(soff) : "memory")
DI f32x16 mfma(bf16x8 a, bf16x8 b, f32x16 c) { return __builtin_amdgcn_mfma_f32_32x32x16_bf16(a, b, c, 0, 0, 0); }
DI bf16x8 asbf(u32x4 v) { return __builtin_bit_cast(bf16x8, v); }
DI bf16x8 ld_frag(unsigned a) { return *(LDS const bf16x8*)a; }
DI bf16x8 tr_frag(unsigned a0, unsigned a1) {
  s16x4 lo = __builtin_amdgcn_ds_read_tr16_b64_v4i16((LDS s16x4*)a0);
  s16x4 hi = __builtin_amdgcn_ds_read_tr16_b64_v4i16((LDS s16x4*)a1);
  return __builtin_bit_cast(bf16x8, __builtin_shufflevector(lo, hi, 0, 1, 2, 3, 4, 5, 6, 7));
}
DI unsigned pk2(float a, float b) { f32x2 v = {a, b}; return __builtin_bit_cast(unsigned, __builtin_convertvector(v, bf16x2)); }
DI float bflo(unsigned u) { return __uint_as_float(u << 16); }
DI float bfhi(unsigned u) { return __uint_as_float(u & 0xffff0000u); }
DI float rbf(float f) { return bflo(pk2(f, 0.f)); }
constexpr float LOG2E = 1.44269504f, EPS2 = 1e-6f * LOG2E;
DI f32x2 phi_u(float a0, float a1, f32x2 rl) { return __builtin_elementwise_fma((f32x2){a0, a1}, rl, (f32x2){EPS2, EPS2}); }
#define PHI_E(j0) _Pragma("unroll") for (int j_ = (j0); j_ < (j0) + 2; ++j_) pe[j_] = (f32x2){__builtin_amdgcn_fmed3f(__builtin_amdgcn_exp2f(u[j_].x), 0.f, 1.f), \
                                                                                     __builtin_amdgcn_fmed3f(__builtin_amdgcn_exp2f(u[j_].y), 0.f, 1.f)}; SB
#define PHI_B(o) _Pragma("unroll") for (int j_ = 0; j_ < 4; ++j_) u[j_] = (f32x2){fmaxf(u[j_].x, EPS2), fmaxf(u[j_].y, EPS2)}; SB; \
              _Pragma("unroll") for (int j_ = 0; j_ < 4; ++j_) u[j_] = __builtin_elementwise_fma(u[j_], (f32x2){1.f / LOG2E, 1.f / LOG2E}, pe[j_]); SB; \
              o = (u32x4){pk2(u[0].x, u[0].y), pk2(u[1].x, u[1].y), pk2(u[2].x, u[2].y), pk2(u[3].x, u[3].y)}
DI unsigned relu2(unsigned v) {
  typedef short s16x2 __attribute__((ext_vector_type(2)));
  return __builtin_bit_cast(unsigned, __builtin_elementwise_max(__builtin_bit_cast(s16x2, v), (s16x2){0, 0}));
}
DI float mul1(float a, float b) { float r; asm("v_mul_f32_e32 %0, %1, %2" : "=v"(r) : "v"(a), "v"(b)); return r; }
DI float sum8(float v) {
  v += __uint_as_float(__builtin_amdgcn_update_dpp(0u, __float_as_uint(v), 0xB1, 0xf, 0xf, true));
  v += __uint_as_float(__builtin_amdgcn_update_dpp(0u, __float_as_uint(v), 0x4E, 0xf, 0xf, true));
  v += __uint_as_float(__builtin_amdgcn_update_dpp(0u, __float_as_uint(v), 0x141, 0xf, 0xf, true));
  return v;
}
DI void lds_put(unsigned a, unsigned v) { FENCE; *(volatile LDS unsigned*)a = v; FENCE; }
DI void lds_wait(unsigned a, int n, unsigned need, int lane) {
  const unsigned ad = a + (lane & (n - 1)) * 4;
#pragma unroll 1
  for (unsigned sp = 0; sp < LDS_CAP; ++sp) {
    FENCE; const unsigned v = *(volatile LDS unsigned*)ad; FENCE;
    if (__all(v >= need)) break;
    __builtin_amdgcn_s_sleep(1);
  }
}
DI u64 tag_wait(unsigned voff, u32x4 rs, unsigned soff, u64 mask, u32x2 tag) {
  u64 bl = 0;
#pragma unroll 1
  for (unsigned sp = 0; sp < SPIN_CAP; ++sp) {
    u32x2 v;
    asm volatile("buffer_load_dwordx2 %0, %1, %2, %3 offen sc1\n\ts_waitcnt vmcnt(0)" : "=&v"(v) : "v"(voff), "s"(rs), "s"(soff) : "memory");
    bl = __ballot(v.x == tag.x && v.y == tag.y);
    if ((bl & mask) == mask) break;
    __builtin_amdgcn_s_sleep(4);
  }
  return bl;
}

__global__ __launch_bounds__(512, 2) void fused(const u16* __restrict__ x, const u16* __restrict__ wqkv, const u16* __restrict__ w1, char* ws,
                                                const u16* __restrict__ wo, const u16* __restrict__ b1, const u16* __restrict__ w2,
                                                const u16* __restrict__ b2, u16* __restrict__ out) {
  const int bid = blockIdx.x, tid = threadIdx.x, lane = tid & 63;
  unsigned cpf;
  {
    u64 pc; asm volatile("s_getpc_b64 %0" : "=s"(pc));
    LD1(cpf, (unsigned)(tid >> 6) * 4096u + (unsigned)lane * 32768u, mk_rsrc((const void*)(pc & ~255ull), CODE_BYTES), (unsigned)(bid >> 3) * 128u, "");
  }
  const int w = __builtin_amdgcn_readfirstlane(tid >> 6);
  const int xc = bid & 7, ix = bid >> 3, sl = ix & 7, rl = ix >> 3;
  const int r = (xc >> 1) * 4 + rl, s0 = r >= 14 ? 0 : r >> 1;
  const u32x4 rx = mk_rsrc(x, S * D * 2);
  const unsigned vx0 = (unsigned)(r * 128 + (w & 1) * 64 + (lane >> 3)) * 896u + (unsigned)(((lane & 7) ^ (lane >> 4)) << 4), vx1 = (vx0 ^ 64u) + 7168u;
#define XGROUP(n) do { dma16o<(n) * 2048>(lx, vx0, rx, sg * 128 + (n) * 14336); dma16o<(n) * 2048 + 1024>(lx, vx1, rx, sg * 128 + (n) * 14336); } while (0)
  asm volatile("" :: "s"(wqkv), "s"(w1), "s"(ws), "s"(wo), "s"(w2), "s"(b1), "s"(b2), "s"(out), "v"(vx0), "v"(vx1));
  if (w < 2) { const unsigned lx = w * 8192; const int sg = s0; XGROUP(0); XGROUP(1); XGROUP(2); XGROUP(3); }
  const int c = (int)(((xc & 1) ? 0xEFB98765u : 0xDCA43210u) >> (4 * sl)) & 15;
  const bool kvt = sl >= 6, pure = sl == 7;
  const int rt = xc * 4 + rl, ct = sl + 2 * rl - (sl + 2 * rl >= 7 ? 7 : 0), rot2 = 7 * rl + xc - (7 * rl + xc >= 28 ? 28 : 0);
  const int l31 = lane & 31, hh = lane >> 5, q = (lane >> 2) & 3, p = lane & 3, blk = (lane >> 4) & 1;
  const unsigned slot_b = kvt ? 49152u : 40960u;
  const u32x4 rws = mk_rsrc(ws, WS_END);
  const unsigned v16 = lane * 16;
  u32x2 tag;
  const unsigned swz = (unsigned)(((lane & 7) ^ ((lane >> 2) & 4)) << 4);
  f32x16 z16;
#pragma unroll
  for (int e = 0; e < 16; ++e) z16[e] = 0.f;

#define PUT_TILES(base, t) do { _Pragma("unroll") for (int n_ = 0; n_ < 8; ++n_) *(LDS f32x4*)((base) + n_ * 1024 + (v16 ^ (n_ * 32))) = \
      (f32x4){t[n_ >> 2][4 * (n_ & 3)], t[n_ >> 2][4 * (n_ & 3) + 1], t[n_ >> 2][4 * (n_ & 3) + 2], t[n_ >> 2][4 * (n_ & 3) + 3]}; } while (0)
  if (__builtin_expect(w < 4, 1)) {
    const int h = w, hp = h >> 1, hs_ = h & 1;
    const u32x4 rw2 = mk_rsrc(w2, F * D * 2), rwo = mk_rsrc(wo, D * D * 2);
    const unsigned vwo = (unsigned)(lane >> 3) * 896u + ct * 128 + swz;
    const unsigned vw2 = vwo + (unsigned)((h >> 1) * 896 + (h & 1) * 16) * 896u;
    const int hg = (h + xc + rl) & 3;
    u32x4 R[36];
#define ROT(u) ((u) + rot2 >= 28 ? (u) + rot2 - 28 : (u) + rot2)
#define PFLD(n) LD4(R[n], vw2, rw2, ROT((n) < 12 ? 3 + ((n) >> 1) : 10 + ((n) >> 1)) * 28672u + ((n) & 1) * 7168u, "")
    auto fill = [&](int u, int g, int cnt) {
#pragma unroll 1
      for (int e = u + cnt; u < e; ++u, ++g) {
        const unsigned k = ROT(u) * 28672u;
        dma16(g * 8192 + h * 2048, vw2, rw2, k); dma16(g * 8192 + h * 2048 + 1024, vw2, rw2, k + 7168u);
      }
    };
    {
      __builtin_amdgcn_s_setprio(3);
      const bool a1 = c < 10, b1_ = c < 9;
      const u32x4 r0 = mk_rsrc(a1 ? w1 : wqkv, a1 ? D * F * 2 : D * 3 * D * 2), r1 = mk_rsrc(b1_ ? w1 : wqkv, b1_ ? D * F * 2 : D * 3 * D * 2);
      const unsigned p0 = a1 ? 3584u : 2688u, p1 = b1_ ? 3584u : 2688u;
      unsigned V0, V1, V2, V3;
      {
        const unsigned kl = 32 * hs_ + (lane >> 3);
        const unsigned c0 = (a1 ? c : c - 10) * 384, c1 = c == 9 ? 512 : c0;
        const unsigned vk = (32 * hs_ + (lane >> 4)) * 2688 + ((224 * c - 1792) & ~63) + ((q ^ (lane >> 4)) << 6) + p * 16;
        V0 = kvt ? vk : kl * p0 + c0 + swz;
        V1 = kvt ? vk + 4 * 2688 : kl * p1 + c1 + swz + 128;
        V2 = kvt ? V0 + 896 : V1 + 128;
        V3 = V1 + 896;
      }
      int sg = s0 + hp; sg = sg == 7 ? 0 : sg;
      unsigned so = hp * slot_b;
#define GROUP(n) do { const unsigned kk = sg * 64 + 8 * (n), sa = kk * p0, sb_ = kk * p1; \
          if (i || hp) XGROUP(n); \
          dma16o<16384 + (n) * 1024>(lw, V0, r0, sa); \
          dma16o<24576 + (n) * 1024>(lw, V1, r1, sb_); \
          dma16o<32768 + (n) * 1024>(lw, V2, r1, sb_); \
          if (kvt) dma16o<40960 + (n) * 1024>(lw, V3, r1, sb_); } while (0)
      if (hp) BARRIER;
      unsigned t2;
#pragma unroll
      for (int i = 0; i < 4; ++i) {
        const int t = hp + 2 * i;
        if (i == 3 && hp) break;
        if (i >= 2 || (i == 1 && hp)) lds_wait(SY_DONE, 4, t - 2, lane);
        const unsigned lx = so + hs_ * 8192, lw = so + hs_ * 4096;
        GROUP(0); GROUP(1); GROUP(2); GROUP(3);
        if (i == 0 && !hp) BARRIER;
        if (i == 3) lds_put(SY_GO, 1);
        if (i == 1) LD1(t2, lane < (hp ? 0 : hs_ ? 13 : 49) ? lane * 128u : 0x80000000u, hs_ ? rwo : rw2, bid * (hs_ ? 1664u : 6272u), "");
        if (i == 0 || pure) WAIT_VM(i == 1);
        if (!pure && i) {
#pragma unroll
          for (int n = (i == 1 ? 0 : i == 2 ? Q1 : Q2); n < (i == 1 ? Q1 : i == 2 ? Q2 : Q3); ++n) PFLD(n);
        }
        if (i == 2 && hp) break;
        if (i) WAIT_VM(i == 1 ? Q1 + 1 : i == 2 ? Q2 - Q1 : Q3 - Q2);
        if (i == 0) asm volatile("" :: "v"(cpf));
        if (i == 2) asm volatile("" :: "v"(t2));
        lds_put(SY_FULL + h * 4, t + 2);
        so = so == 0 ? 2 * slot_b : so - slot_b;
        sg = sg >= 5 ? sg - 5 : sg + 2;
      }
      tag = *(volatile LDS u32x2*)SY_TAG;
      if (hp) {
        u32x4 xf[8];
        unsigned bi;
        const bool chk = !hs_ || kvt || lane < 32;
        const u32x4 rb1 = mk_rsrc(b1, c < 10 ? F * 2 : 0);
#pragma unroll 1
        for (unsigned sp = 0; sp < SPIN_CAP; ++sp) {
#pragma unroll
          for (int k = 0; k < 7; ++k) LD4(xf[k], v16, rws, WS_CS + c * 14336u + hs_ * 1024u + k * 2048u, "sc1");
          LD4(xf[7], v16, rws, WS_ST + r * 2048u + hs_ * 1024u, "sc1");
          LD1(bi, lane * 4u, rb1, c * 384u + hs_ * 256u, "");
          WAIT_VM(9 + Q2 - Q1); lds_put(SY_FULL + h * 4, 7);
          WAIT_VM(0);
          PIN(xf[0]); PIN(xf[7]); PIN(bi);
          if (kvt && l31 == 30) bi = 0x3f80u;
          unsigned bad = (xf[0].y ^ tag.x) | (xf[0].w ^ tag.x);
          float ax_ = __uint_as_float(xf[0].x), ay_ = __uint_as_float(xf[0].z);
#pragma unroll
          for (int k = 1; k < 7; ++k) {
            PIN(xf[k]);
            ax_ += __uint_as_float(xf[k].x); ay_ += __uint_as_float(xf[k].z); PIN(ax_); PIN(ay_);
            bad |= (xf[k].y ^ tag.x) | (xf[k].w ^ tag.x); PIN(bad);
          }
          const unsigned hi = pk2(ax_, ay_); ax_ -= bflo(hi); PIN(ax_); ay_ -= bfhi(hi);
          const unsigned mi = pk2(ax_, ay_); ax_ -= bflo(mi); PIN(ax_); ay_ -= bfhi(mi);
          const unsigned lo = pk2(ax_, ay_);
          *(LDS u32x4*)(L_CS + hs_ * 1024 + v16) = (u32x4){(hi & 0xffffu) | (mi << 16), (lo & 0xffffu) | (bi << 16),
                                                           (hi >> 16) | (mi & 0xffff0000u), (lo >> 16) | (bi & 0xffff0000u)};
          *(LDS unsigned*)(L_ST + hs_ * 256 + lane * 4) = xf[7].x;
          *(LDS float*)(L_RS + hs_ * 256 + lane * 4) = __builtin_amdgcn_rcpf(bfhi(xf[7].x)) * LOG2E;
          if (__builtin_expect(__all((bad == 0 || !chk) && xf[7].y == tag.x), 1)) break;
          __builtin_amdgcn_s_sleep(8);
        }
        lds_put(SY_FULL + h * 4, 8);
        lds_wait(SY_GO, 1, 1, lane);
        if (!pure) {
#pragma unroll
          for (int n = Q2; n < Q3; ++n) PFLD(n);
        }
        asm volatile("" :: "v"(t2));
      }
      __builtin_amdgcn_s_setprio(0);
    }
    if (!pure) {
#pragma unroll
    for (int n = Q3; n < 36; ++n) PFLD(n);
    const unsigned wb = 49152 + (h ^ 2) * 14336;
    lds_wait(SY_DONE, 4, h < 2 ? 6 : 5, lane);
#pragma unroll 1
    for (int n = 0; n < 14; ++n) dma16(wb + n * 1024, vwo, rwo, hg * 100352u + n * 7168u);
    lds_wait(SY_DONE, 4, 6, lane);
    fill(0, 16, 3);
    WAIT_VM(6);
    bf16x8 WO[4][2][2];
    {
      const unsigned a0 = wb + (4 * hh + q) * 128 + (((q >> 1) & 1) << 6) + blk * 32 + p * 8, a1_ = a0 ^ 64u;
#pragma unroll
      for (int n = 0; n < 4; ++n)
#pragma unroll
        for (int sg = 0; sg < 2; ++sg) {
          const unsigned o = n * 3584 + sg * 2048;
          WO[n][sg][0] = tr_frag(a0 + o, a0 + o + 1024); WO[n][sg][1] = tr_frag(a1_ + o, a1_ + o + 1024);
        }
    }
#pragma unroll
    for (int n = 0; n < 8; ++n) {
      u32x4 t = __builtin_bit_cast(u32x4, WO[n >> 1][1][n & 1]);
      if (hh) { t.z = 0; t.w = 0; }
      WO[n >> 1][1][n & 1] = asbf(t);
    }
#pragma unroll
    for (int n = 0; n < 16; ++n) PIN(WO[n >> 2][(n >> 1) & 1][n & 1]);
    lds_put(SY_WOK + h * 4, 1);
    lds_wait(SY_DONE, 4, 7, lane);
#pragma unroll
    for (int n = 0; n < 12; ++n) { PIN(R[n]); *(LDS u32x4*)(h * 2048 + v16 + (n >> 1) * 8192 + (n & 1) * 1024) = R[n]; }
    WAIT_VM(0); lds_put(SY_FULL2 + h * 4, 1);
#pragma unroll
    for (int n = 12; n < 18; ++n) { PIN(R[n]); *(LDS u32x4*)(57344 + h * 2048 + v16 + (n >> 1) * 8192 + (n & 1) * 1024) = R[n]; }
    lds_wait(SY_DONE, 4, c < 10 ? 8 : 7, lane);
    lds_wait(SY_WOK, 4, 1, lane);
    fill(9, 6, 7);
    lds_wait(SY_DONE, 4, 9, lane);
    WAIT_VM(14);
#pragma unroll
    for (int n = 0; n < 18; ++n) { PIN(R[18 + n]); *(LDS u32x4*)((n < 6 ? 131072 : -24576) + h * 2048 + v16 + (n >> 1) * 8192 + (n & 1) * 1024) = R[18 + n]; }
    WAIT_VM(0); lds_put(SY_RDY + h * 4, 1);
    u32x4 PQ[2][7], KV[8];
    __builtin_amdgcn_s_setprio(3);
    lds_wait(SY_HOK, 1, 1, lane);
    if (*(volatile LDS unsigned*)SY_HOK < 2u) tag_wait(min(lane, 4) * FS, rws, WS_FL + (rt * 32 + 19) * FS, ~0ull, tag);
    const unsigned kfo = WS_KF + (xc * 32 + hg * 8) * FS;
#pragma unroll
    for (int n = 0; n < 7; ++n) {
      LD4(PQ[0][n], v16, rws, WS_PQ + (rt * 56 + hg * 7 + n) * 1024u, "sc1");
      if (n < 6) __builtin_amdgcn_s_sleep(8);
    }
    tag_wait(min(lane, 7) * FS, rws, kfo, ~0ull, tag);
#pragma unroll
    for (int n = 0; n < 8; ++n) LD4(KV[n], v16, rws, WS_KV + hg * 8192u + n * 1024u, "sc1");
    u32x4 PB[2][4];
#define PBS(a) do { _Pragma("unroll") for (int n = 0; n < 7; ++n) PIN(PQ[a][n]); \
      _Pragma("unroll") for (int n = 0; n < 4; ++n) { const int lo = n < 3 ? 2 * n + 1 : 5, hi = n ? 2 * n - 1 : 1; \
        PB[a][n] = (u32x4){PQ[a][lo].x, PQ[a][lo].y, PQ[a][hi].z, PQ[a][hi].w}; PIN(PB[a][n]); } } while (0)
    PBS(0);
    WAIT_VM(4);
#pragma unroll
    for (int n = 0; n < 4; ++n) PIN(KV[n]);
    f32x16 nm[3], as[2][2] = {{z16, z16}, {z16, z16}};
    u32x4 O[2][2]; float rd[2];
#define NUM0(u) nm[(u) % 3] = mfma(asbf(KV[2 * ((u) & 3)]), asbf(PQ[(u) >> 2][2 * ((u) & 3)]), z16)
#define NUM1(u) nm[(u) % 3] = mfma(asbf(KV[2 * ((u) & 3) + 1]), asbf(PB[(u) >> 2][(u) & 3]), nm[(u) % 3])
#define DEN(u) __uint_as_float(__builtin_amdgcn_permlane32_swap(__float_as_uint(nm[(u) % 3][12]), __float_as_uint(nm[(u) % 3][12]), false, false)[1])
#define APP(u, i) as[(u) >> 2][(i) & 1] = mfma(WO[(u) & 3][(i) >> 1][(i) & 1], asbf(O[(u) & 1][(i) >> 1]), as[(u) >> 2][(i) & 1])
#define MULS(a, b) _Pragma("unroll") for (int e = (a); e < (b); ++e) o[e] = mul1(nm[u % 3][e], rd[u & 1])
#define CVTS(a, b) _Pragma("unroll") for (int e = (a); e < (b); ++e) O[u & 1][e >> 2][e & 3] = pk2(o[2 * e], o[2 * e + 1])
    NUM0(0); NUM1(0); NUM0(1); NUM1(1);
    SB;
#pragma unroll
    for (int n = 0; n < 7; ++n) LD4(PQ[1][n], v16, rws, WS_PQ + (rt * 56 + 28 + hg * 7 + n) * 1024u, "sc1");
    __builtin_amdgcn_s_setprio(1);
    rd[0] = __builtin_amdgcn_rcpf(DEN(0) + 1e-6f);
    asm volatile("s_waitcnt vmcnt(7)" : "+v"(rd[0]), "+v"(KV[4]), "+v"(KV[5]), "+v"(KV[6]), "+v"(KV[7]) :: "memory");
    SB;
    unsigned d2 = 0; bool put0 = false;
#pragma unroll
    for (int u = 0; u < 8; ++u) {
      float o[16], dn;
      if (u) APP(u - 1, 0); SB; MULS(0, 6); SB;
      if (u) APP(u - 1, 1); SB; MULS(6, 10); CVTS(0, 2); SB;
      if (u) APP(u - 1, 2); SB; MULS(10, 13); CVTS(2, 4); SB;
      if (u) APP(u - 1, 3); SB; MULS(13, 16); CVTS(4, 6); SB;
      if (u == 2) { WAIT_VM(0); PBS(1); SB; }
      if (u < 6) NUM0(u + 2); SB; if (u < 7) dn = DEN(u + 1) + 1e-6f; SB;
      if (u < 6) NUM1(u + 2); SB; if (u < 7) rd[(u + 1) & 1] = __builtin_amdgcn_rcpf(dn); SB; CVTS(6, 8);
      if (u == 4) d2 = *(volatile LDS unsigned*)(SY_DONE2 + p * 4);
      if (u == 5) { put0 = __all(d2 >= 1u); if (put0) PUT_TILES(49152 + h * 16384, as[0]); }
      SB;
    }
    APP(7, 0); APP(7, 2); APP(7, 1); APP(7, 3);
    if (!put0) { lds_wait(SY_DONE2, 4, 1, lane); PUT_TILES(49152 + h * 16384, as[0]); }
    PUT_TILES(57344 + h * 16384, as[1]);
    BARRIER;
    }
  } else {
  const int cw = w - 4, wl = cw & 1, wh = cw >> 1;
  u32x4 xs[7];
  if (cw == 3) {
    const unsigned va = (unsigned)(r * 128 + c * 8 + (lane >> 3)) * 896u + (lane & 7) * 16;
#pragma unroll
    for (int k = 0; k < 7; ++k) LD4(xs[k], va, rx, k * 128u, "");
  }
  *(LDS unsigned*)(L_SY + lane * 4) = (lane >> 1) == 1; BARRIER;
  __builtin_amdgcn_s_setprio(2);
  bf16x8 ones = asbf((u32x4){0x3f803f80u, 0x3f803f80u, 0x3f803f80u, 0x3f803f80u});
  PIN(ones);
  {
    {
      u64 ep;
      asm volatile("s_load_dwordx2 %0, %1, 0x0\n\ts_waitcnt lgkmcnt(0) vmcnt(0)" : "=&s"(ep) : "s"(ws) : "memory");
      tag.x = (unsigned)ep + 1u; tag.y = (unsigned)(ep >> 32) + 1u;
    }
    asm volatile("" :: "v"(cpf));
    if (cw == 0) *(volatile LDS u32x2*)SY_TAG = tag;
    if (cw == 3) {
      float sq[2] = {0.f, 0.f}, sm[2] = {0.f, 0.f};
      const bf16x2 one2 = __builtin_bit_cast(bf16x2, 0x3f803f80u);
#pragma unroll
      for (int k = 0; k < 7; ++k) {
        PIN(xs[k]);
#pragma unroll
        for (int e = 0; e < 4; ++e) {
          const unsigned u = xs[k][e];
          const bf16x2 v = __builtin_bit_cast(bf16x2, u);
          sq[e & 1] = __builtin_amdgcn_fdot2_f32_bf16(v, v, sq[e & 1], false); sm[e & 1] = __builtin_amdgcn_fdot2_f32_bf16(v, one2, sm[e & 1], false);
        }
      }
      const float m = sum8(sm[0] + sm[1]) * (1.f / D);
      const float var = fmaxf(sum8(sq[0] + sq[1]) * (1.f / D) - m * m, 0.f);
      const unsigned sv = __builtin_amdgcn_ds_bpermute(lane * 32, pk2(-m, __builtin_amdgcn_sqrtf(rbf(rbf(var) + 1e-5f))));
      const u32x4 gr = {sv, tag.x, sv, tag.x};
      if (lane < 8) ST4(gr, v16, rws, WS_ST + (r * 128 + c * 8) * 16, "sc1");
    }
  }
  const unsigned fl_off = WS_FL + ((r * 2 + wl) * 32 + 2 * c + wh) * FS;
  const unsigned fa = SY_FULL + p * 4;
#define CHECK_FULL(v, need) do { if (__builtin_expect(__any((v) < (need)), 0)) lds_wait(SY_FULL, 4, need, lane); FENCE; } while (0)
#define COLSUM(a_, t_, tile) do { const unsigned a = (a_); bf16x8 f[4]; \
    _Pragma("unroll") for (int ks = 0; ks < 4; ++ks) f[ks] = tr_frag(a + ks * 2048, a + ks * 2048 + (t_)); \
    SB; f32x16 cs = z16; \
    _Pragma("unroll") for (int ks = 0; ks < 4; ++ks) cs = mfma(ones, f[ks], cs); \
    const u32x4 gr = {(unsigned)__builtin_amdgcn_ds_bpermute(lane * 8, __float_as_int(cs[0])), tag.x, (unsigned)__builtin_amdgcn_ds_bpermute(lane * 8 + 4, __float_as_int(cs[0])), tag.x}; \
    if (lane < 16) ST4(gr, v16, rws, WS_CS + (c * 7 + s0) * 2048 + (tile) * 256, "sc1"); } while (0)
  unsigned da = SY_DONE + cw * 4;
  PIN(da);
  const unsigned xm = hh ? 0u : ~0u;
  if (!kvt) {
    const unsigned xa = (wl * 64 + l31) * 128 + ((hh ^ ((l31 >> 1) & 7)) << 4);
    const unsigned wr = 16384 + (8 * hh + q) * 128 + blk * 32 + p * 8;
    const unsigned wa0 = wr + wh * 8192 + ((wh ^ (q >> 1)) << 6), wa1 = wr + wh * 16384 + ((wh ^ 1 ^ (q >> 1)) << 6);
    f32x16 acc[2][3] = {{z16, z16, z16}, {z16, z16, z16}};
    bf16x8 X[4][2], W[4][3];
#define RDA(n, ks) do { \
      if ((n) == 0 || (n) == 2) X[ks][(n) >> 1] = ld_frag(ax[ks] + ((n) >> 1) * 4096); \
      else { const unsigned a_ = ((n) == 3 ? a1_ : a0_) + ((n) == 4 ? 8192 : 0) + (ks) * 2048; W[ks][(n) == 1 ? 0 : (n) - 2] = tr_frag(a_, a_ + 512); } } while (0)
#define MMA(ks, m) acc[(m) & 1][(m) >> 1] = mfma(W[ks][(m) >> 1], X[ks][(m) & 1], acc[(m) & 1][(m) >> 1])
    unsigned ax[4] = {xa, xa ^ 32u, xa ^ 64u, xa ^ 96u}, a0_ = wa0, a1_ = wa1;
#pragma unroll
    for (int n = 0; n < 6; ++n) PIN(acc[n & 1][n >> 1]);
    lds_wait(SY_FULL, 4, 1, lane);
    if (r < 14 && !((r & 1) && wl)) {
      const int b = (r & 1) ? 2 : wl;
      COLSUM(b == 1 ? wa1 : wa0 + b * 4096, 512, 3 * wh + b);
    }
#pragma unroll
    for (int n = 0; n < 10; ++n) { RDA(n % 5, n / 5); SB; }
    const int tla = c >= 10 ? 2 : (xc & 1) ? 3 : 4, tlb = c >= 10 ? 2 : 4;
    unsigned tv = 1; int ts = 0;
    PIN(tv);
#pragma unroll 1
    for (;;) {
      unsigned fv; int dl;
#pragma unroll
      for (int m = 0; m < 12; ++m) {
        MMA(m / 6, m % 6); SB;
        if (m % 6 < 5) RDA(m % 6, 2 + m / 6);
        if (m == 0) { dl = ((0x24 >> ts) & 1) ? -81920 : 40960; ++ts; asm volatile("" : "+s"(dl), "+s"(ts)); }
        if (m == 2) { ax[0] += dl; ax[1] += dl; PIN(ax[0]); PIN(ax[1]); }
        if (m == 5) { if (ts <= tla) __builtin_amdgcn_s_sleep(4); fv = *(volatile LDS unsigned*)fa; }
        if (m == 10) *(volatile LDS unsigned*)da = tv;
        if (m == 11) { a0_ += dl; a1_ += dl; ++tv; CHECK_FULL(fv, tv); }
        SB;
      }
      if (__builtin_expect(ts == 7, 0)) break;
#pragma unroll
      for (int m = 0; m < 12; ++m) {
        MMA(2 + m / 6, m % 6); SB;
        if (m % 6 < 5) RDA(m % 6, m / 6);
        if (m == 5) { ax[2] += dl; ax[3] += dl; if (ts <= tlb) __builtin_amdgcn_s_sleep(4); }
        SB;
      }
    }
    unsigned st[2]; u32x2 cv[3];
#pragma unroll
    for (int a = 0; a < 2; ++a) st[a] = *(LDS const unsigned*)(L_ST + (wl * 64 + a * 32 + l31) * 4);
#pragma unroll
    for (int b = 0; b < 3; ++b) cv[b] = *(LDS const u32x2*)(L_CS + ((3 * wh + b) * 32 + l31) * 8);
    SB;
    MMA(2, 0); MMA(2, 1); MMA(3, 0); MMA(3, 1);
    SB;
    bf16x8 xe[2];
#pragma unroll
    for (int a = 0; a < 2; ++a) xe[a] = asbf((u32x4){((st[a] & 0xffffu) | (st[a] << 16)) & xm, st[a] & xm, 0u, 0u});
#define MME(m) acc[(m) & 1][(m) >> 1] = mfma(asbf((u32x4){cv[(m) >> 1].x, cv[(m) >> 1].y, cv[(m) >> 1].x, cv[(m) >> 1].y}), xe[(m) & 1], acc[(m) & 1][(m) >> 1])
    MME(0); MME(1);
    SB;
    const int R0 = r * 4 + wl * 2;
    const unsigned sk = c == 9 ? 0x40000000u : 0u;
#define MM6(b, j) do { if ((j) < 4) MMA(2 + ((j) >> 1), 2 * (b) + ((j) & 1)); else MME(2 * (b) + ((j) & 1)); } while (0)
    if (2 * c + wh < 19) {
      const unsigned ob = WS_H + (R0 * 112 + 12 * c + 6 * wh) * 1024u;
#pragma unroll
      for (int b = 0; b < 3; ++b)
#pragma unroll
        for (int n = 0; n < 4; ++n) {
          const int a = n >> 1, e = 8 * (n & 1);
          float t[8]; u32x4 o;
          if (b < 2) MM6(b + 1, n < 2 ? 2 * n : n + 2);
          SB;
          if (n == 0) asm volatile("s_nop 10" : "+v"(acc[0][b]), "+v"(acc[1][b]));
          asm volatile("v_mul_f32_e64 %0, %8, |%8|\n\tv_mul_f32_e64 %1, %9, |%9|\n\tv_mul_f32_e64 %2, %10, |%10|\n\tv_mul_f32_e64 %3, %11, |%11|\n\t"
                       "v_mul_f32_e64 %4, %12, |%12|\n\tv_mul_f32_e64 %5, %13, |%13|\n\tv_mul_f32_e64 %6, %14, |%14|\n\tv_mul_f32_e64 %7, %15, |%15|"
                       : "=&v"(t[0]), "=&v"(t[1]), "=&v"(t[2]), "=&v"(t[3]), "=&v"(t[4]), "=&v"(t[5]), "=&v"(t[6]), "=&v"(t[7])
                       : "v"(acc[a][b][e]), "v"(acc[a][b][e + 1]), "v"(acc[a][b][e + 2]), "v"(acc[a][b][e + 3]), "v"(acc[a][b][e + 4]), "v"(acc[a][b][e + 5]),
                         "v"(acc[a][b][e + 6]), "v"(acc[a][b][e + 7]));
          SB;
          if (b < 2 && n < 2) MM6(b + 1, 2 * n + 1);
          SB;
#pragma unroll
          for (int j = 0; j < 4; ++j) o[j] = pk2(t[2 * j], t[2 * j + 1]);
#pragma unroll
          for (int j = 0; j < 4; ++j) o[j] = relu2(o[j]);
          ST4(o, v16, rws, ob + a * (112 * 1024u) + (2 * b + (n & 1)) * 1024u + (b == 2 ? sk : 0u), "sc1");
          SB;
        }
    } else {
#pragma unroll
      for (int j = 0; j < 12; ++j) MM6(1 + j / 6, j % 6);
      const unsigned ob = WS_PQ + (R0 * 28 + (c == 9 ? 22 : (c - 10) * 12 + wh * 6)) * 1024u;
      SB;
#pragma unroll
      for (int a = 0; a < 2; ++a) {
        const float rl = __builtin_amdgcn_rcpf(bfhi(st[a])) * LOG2E;
#pragma unroll
        for (int n = 0; n < 6; ++n) {
          f32x2 u[4], pe[4]; u32x4 o;
#pragma unroll
          for (int j = 0; j < 4; ++j) u[j] = phi_u(acc[a][n >> 1][8 * (n & 1) + 2 * j], acc[a][n >> 1][8 * (n & 1) + 2 * j + 1], (f32x2){rl, rl});
          SB; PHI_E(0); PHI_E(2); PHI_B(o);
          ST4(o, v16, rws, ob + a * (28 * 1024u) + n * 1024u + (n < 2 ? sk : 0u), "sc1");
          SB;
        }
      }
    }
  } else {
    const unsigned xa = l31 * 128 + ((hh ^ ((l31 >> 1) & 7)) << 4);
    const int ok_ = (c & 1) * 32 + 56 * cw + 8 * ((4 * blk + p + cw) & 7), ov_ = (c & 1) * 32 + 56 * cw + 32 * blk + 8 * p;
    const unsigned ka = 16384 + hh * 1024 + q * 256 + ((((ok_ >> 6) & 3) ^ q) << 6) + (ok_ & 63);
    const unsigned va = 32768 + hh * 1024 + q * 256 + ((((ov_ >> 6) & 3) ^ q) << 6) + (ov_ & 63) + ((lane & 19) == 19 ? 0x100000 : 0);
    f32x16 acc[4][2] = {{z16, z16}, {z16, z16}, {z16, z16}, {z16, z16}};
    bf16x8 X[4][4], W[4][2];
#define RDK(n, ks) do { \
      if ((n) == 1 || (n) == 5) { const unsigned a_ = ((n) == 1 ? a0_ : a1_) + (ks) * 2048; W[ks][(n) >> 2] = tr_frag(a_, a_ + 8192); } \
      else X[ks][(n) ? (n) - 1 : 0] = ld_frag(ax[ks] + ((n) ? (n) - 1 : 0) * 4096); } while (0)
#define MMK(ks, m) acc[(m) & 3][(m) >> 2] = mfma(X[ks][(m) & 3], W[ks][(m) >> 2], acc[(m) & 3][(m) >> 2])
    unsigned ax[4] = {xa, xa ^ 32u, xa ^ 64u, xa ^ 96u}, a0_ = ka, a1_ = va;
#pragma unroll
    for (int n = 0; n < 8; ++n) PIN(acc[n & 3][n >> 2]);
    lds_wait(SY_FULL, 4, 1, lane);
    if (r < 14) COLSUM((r & 1) ? va : ka, 8192, 2 * cw + (r & 1));
#pragma unroll
    for (int n = 0; n < 12; ++n) { RDK(n % 6, n / 6); SB; }
    unsigned tv = 1; int ts = 0;
    PIN(tv);
#pragma unroll 1
    for (;;) {
      unsigned fv; int dl;
#pragma unroll
      for (int m = 0; m < 16; ++m) {
        MMK(m >> 3, m & 7); SB;
        if ((m & 7) >= 1 && (m & 7) < 7) RDK((m & 7) - 1, 2 + (m >> 3));
        if (m == 0) { dl = ((0x24 >> ts) & 1) ? -98304 : 49152; ++ts; asm volatile("" : "+s"(dl), "+s"(ts)); }
        if (m == 7) { ax[0] += dl; ax[1] += dl; PIN(ax[0]); PIN(ax[1]); }
        if (m == 8) { if (ts <= 2) __builtin_amdgcn_s_sleep(4); fv = *(volatile LDS unsigned*)fa; }
        if (m == 14) *(volatile LDS unsigned*)da = tv;
        if (m == 15) { a0_ += dl; a1_ += dl; ++tv; CHECK_FULL(fv, tv); }
        SB;
      }
      if (__builtin_expect(ts == 7, 0)) break;
#pragma unroll
      for (int m = 0; m < 16; ++m) {
        MMK(2 + (m >> 3), m & 7); SB;
        if ((m & 7) >= 1 && (m & 7) < 7) RDK((m & 7) - 1, m >> 3);
        if (m == 7) { ax[2] += dl; ax[3] += dl; }
        SB;
      }
    }
    unsigned st[4]; u32x2 cv[2]; f32x4 rsv[2][4];
#pragma unroll
    for (int a = 0; a < 4; ++a) st[a] = *(LDS const unsigned*)(L_ST + (a * 32 + l31) * 4);
#pragma unroll
    for (int b = 0; b < 2; ++b) cv[b] = *(LDS const u32x2*)(L_CS + ((2 * cw + b) * 32 + l31) * 8);
    unsigned rsa = L_RS + hh * 16;
    PIN(rsa);
#define MK6(a, j) do { if ((j) < 4) MMK(2 + ((j) >> 1), (a) + 4 * ((j) & 1)); else acc[a][(j) & 1] = mfma(asbf((u32x4){((st[a] & 0xffffu) | (st[a] << 16)) & xm, st[a] & xm, 0u, 0u}), \
        asbf((u32x4){cv[(j) & 1].x, cv[(j) & 1].y, cv[(j) & 1].x, cv[(j) & 1].y}), acc[a][(j) & 1]); } while (0)
#pragma unroll
    for (int n = 0; n < 4; ++n) rsv[0][n] = *(LDS const f32x4*)(rsa + n * 32);
    SB;
#pragma unroll
    for (int j = 0; j < 6; ++j) MK6(0, j);
    SB;
    f32x16 kv = z16;
    u32x4 fk, fv;
#pragma unroll
    for (int gi = 0; gi < 8; ++gi) {
      const int a = gi >> 1, sg = gi & 1, e = 8 * sg;
      if (a < 3 && sg == 0) {
#pragma unroll
        for (int n = 0; n < 4; ++n) rsv[(a + 1) & 1][n] = *(LDS const f32x4*)(rsa + (a + 1) * 128 + n * 32);
      }
      const f32x4 ra_ = rsv[a & 1][2 * sg], rb_ = rsv[a & 1][2 * sg + 1];
      const f32x2 rl[4] = {{ra_[0], ra_[1]}, {ra_[2], ra_[3]}, {rb_[0], rb_[1]}, {rb_[2], rb_[3]}};
      f32x2 u[4], vv[4], pe[4];
#pragma unroll
      for (int j = 0; j < 4; ++j) u[j] = phi_u(acc[a][0][e + 2 * j], acc[a][0][e + 2 * j + 1], rl[j]);
#pragma unroll
      for (int j = 0; j < 4; ++j) vv[j] = (f32x2){acc[a][1][e + 2 * j], acc[a][1][e + 2 * j + 1]} * rl[j];
      SB;
      if (gi) kv = mfma(asbf(fk), asbf(fv), kv);
      SB;
      PHI_E(0);
      if (a < 3) MK6(a + 1, 3 * sg);
      SB;
      PHI_E(2);
      if (a < 3) MK6(a + 1, 3 * sg + 1);
      SB;
      fv = (u32x4){pk2(vv[0].x, vv[0].y), pk2(vv[1].x, vv[1].y), pk2(vv[2].x, vv[2].y), pk2(vv[3].x, vv[3].y)};
      SB;
      if (a < 3) MK6(a + 1, 3 * sg + 2);
      SB;
      PHI_B(fk);
      SB;
    }
    kv = mfma(asbf(fk), asbf(fv), kv);
    kv = kv * (l31 == 28 ? 1.f / 1.4453125f : 1.f / LOG2E);
    const unsigned ko = WS_KVP + (((c - 12) * 4 + cw) * 16 + r) * 4096u;
#pragma unroll
    for (int e = 0; e < 4; ++e) {
      const u32x4 o = {pk2(kv[4 * e], kv[4 * e + 1]), tag.x, pk2(kv[4 * e + 2], kv[4 * e + 3]), tag.x};
      ST4(o, v16, rws, ko + e * 1024u, "sc1");
    }
  }
  lds_put(da, 8);
  bf16x8 WH[9][4];
  unsigned ra[3][2];
#pragma unroll
  for (int cb = 0; cb < 2; ++cb) {
    ra[0][cb] = wh * 4096 + (4 * hh + q) * 128 + ((cb ^ (q >> 1)) << 6) + blk * 32 + p * 8; ra[1][cb] = ra[0][cb] + 65536; ra[2][cb] = ra[0][cb] + 131072;
    PIN(ra[0][cb]); PIN(ra[1][cb]); PIN(ra[2][cb]);
  }
#define RDW(dst, n, reg) do { const unsigned a_ = ra[(reg) >> 3][(n) & 1] + ((reg) & 7) * 8192 + ((n) >> 1) * 2048; dst[n] = tr_frag(a_, a_ + 1024); } while (0)
  if (!kvt) WAIT_VM(0);
  if (lane < 8) ST4(((u32x4){tag.x, tag.y, tag.x, tag.y}), v16, rws, fl_off, "sc1");
  if (!pure) {
    lds_wait(SY_FULL2, 4, 1, lane);
#pragma unroll
    for (int n = 0; n < 36; ++n) RDW(WH[n >> 2], n & 3, (16 + (n >> 2)) % 19);
    lds_put(da, 9);
  }

  if (!pure) {
    const int kh = wh, R = rt * 2 + wl;
    if (cw == 0) {
      if (c < 10) __builtin_amdgcn_s_sleep(12);
      const u64 bl = tag_wait(min(lane, 23) * FS, rws, WS_FL + rt * 32 * FS, 0x7FFFFull, tag);
      lds_put(SY_HOK, (bl & 0xF80000ull) == 0xF80000ull ? 2 : 1);
    } else lds_wait(SY_HOK, 1, 1, lane);
    WAIT_VM(0);
    const unsigned hs = WS_H + (R * 112 + kh * 56) * 1024u;
    constexpr int NH = NHW, E = 28 - NH;
    u32x4 hf[NH][2];
#define HLD(u) do { const unsigned o_ = hs + ROT(u) * 2048u; \
      LD4(hf[(u) % NH][0], v16, rws, o_, "sc1"); LD4(hf[(u) % NH][1], v16, rws, o_, "offset:1024 sc1"); } while (0)
#pragma unroll
    for (int u = 0; u < NH; ++u) HLD(u);
    u32x4 xr[2], br; unsigned sg_[2];
    const unsigned fxo = (unsigned)(rt * 64 + cw * 8 + (lane >> 3)) * 896u + ct * 128 + (lane & 7) * 16;
#define FIN_LD do { LD4(xr[0], fxo, rx, 0u, ""); LD4(xr[1], fxo, rx, 28672u, ""); LD4(br, (lane & 7) * 16u, mk_rsrc(b2, D * 2), ct * 128u, ""); \
      LD1(sg_[0], (unsigned)(lane & ~7) * 2u, rws, WS_ST + rt * 1024u + cw * 128u, "sc1"); LD1(sg_[1], (unsigned)(lane & ~7) * 2u, rws, WS_ST + rt * 1024u + cw * 128u, "offset:512 sc1"); } while (0)
    f32x16 af[2] = {z16, z16};
    bf16x8 W[LA + 1][4];
#pragma unroll
    for (int u = 0; u < 28; ++u) {
      constexpr auto total = [](int u_) { return 2 * NH + 2 * (u_ < E ? u_ : E) + (u_ > E ? 5 : 0); };
      asm volatile("s_waitcnt vmcnt(%2)" : "+v"(hf[u % NH][0]), "+v"(hf[u % NH][1]) : "n"(total(u) - 2 - 2 * u) : "memory");
      if (u == 9 - LA) { lds_wait(SY_RDY, 4, 1, lane); }
#pragma unroll
      for (int n = 0; n < 4; ++n) {
        af[n & 1] = mfma(u < 9 ? WH[u < 9 ? u : 0][n] : W[u % (LA + 1)][n], asbf(hf[u % NH][n >> 1]), af[n & 1]); SB;
        if (u + LA >= 9 && u + LA < 28) RDW(W[(u + LA) % (LA + 1)], n, (u + LA - 3) % 19);
        SB;
      }
      if (u == 16 - LA) lds_put(SY_DONE2 + cw * 4, 1);
      if (u == 20 - LA) lds_put(SY_DONE2 + cw * 4, 2);
      if (u < E) HLD(u + NH);
      else if (u == E) FIN_LD;
    }
    __builtin_amdgcn_s_setprio(0);
    lds_wait(SY_DONE2, 4, 2, lane);
    PUT_TILES(114688 + cw * 8192, af);
    lds_put(SY_DONE2 + cw * 4, 3);
    WAIT_VM(0);
    unsigned a0 = 49152 + (lane & 7) * 1024 + (((cw * 8 + (lane >> 3)) ^ ((lane & 7) * 2)) << 4), a1 = a0 + 65536;
    PIN(a0); PIN(a1); PIN(br); PIN(sg_[0]); PIN(sg_[1]); PIN(xr[0]); PIN(xr[1]);
    float gt[2][8], xv[2][8], bv[8], rs2[2];
#define ALL16(stmt) do { _Pragma("unroll") for (int n = 0; n < 16; ++n) { const int a = n >> 3, e = n & 7; (void)a; (void)e; stmt; } SB; } while (0)
#pragma unroll
    for (int a = 0; a < 2; ++a) { rs2[a] = __builtin_amdgcn_rcpf(bfhi(sg_[a])); rs2[a] *= rs2[a]; PIN(rs2[a]); }
#pragma unroll
    for (int e = 0; e < 8; ++e) { bv[e] = (e & 1) ? bfhi(br[e >> 1]) : bflo(br[e >> 1]); PIN(bv[e]); }
    ALL16(xv[a][e] = (e & 1) ? bfhi(xr[a][e >> 1]) : bflo(xr[a][e >> 1]); PIN(xv[a][e]));
    lds_wait(SY_DONE2, 4, 3, lane);
    {
      f32x4 f[2][2][2];
      float ex[2][8];
#pragma unroll
      for (int n = 0; n < 8; ++n) f[n >> 2][n & 1][(n >> 1) & 1] = *(LDS const f32x4*)(a1 + (n >> 2) * 8192 + (n & 1) * 16384 + ((n >> 1) & 1) * 512);
      SB;
      ALL16(gt[a][e] = f[a][0][e >> 2][e & 3] + f[a][1][e >> 2][e & 3]; PIN(gt[a][e]));
      ALL16(gt[a][e] = fmaf(gt[a][e], rs2[a], bv[e]); PIN(gt[a][e]));
      ALL16(ex[a][e] = gt[a][e] * -LOG2E; PIN(ex[a][e]));
      ALL16(ex[a][e] = __builtin_amdgcn_exp2f(ex[a][e]));
      ALL16(ex[a][e] += 1.f; PIN(ex[a][e]));
      ALL16(ex[a][e] = __builtin_amdgcn_rcpf(ex[a][e]));
      ALL16(gt[a][e] *= ex[a][e]; PIN(gt[a][e]));
    }
    BARRIER;
    f32x4 sp[2][4][2];
#pragma unroll
    for (int n = 0; n < 16; ++n) sp[n >> 3][(n >> 1) & 3][n & 1] = *(LDS const f32x4*)(a0 + (n >> 3) * 8192 + ((n >> 1) & 3) * 16384 + (n & 1) * 512);
    SB;
#pragma unroll
    for (int a = 0; a < 2; ++a) {
      const f32x4 s0_ = (sp[a][0][0] + sp[a][1][0]) + (sp[a][2][0] + sp[a][3][0]), s1_ = (sp[a][0][1] + sp[a][1][1]) + (sp[a][2][1] + sp[a][3][1]);
      u32x4 ov;
#pragma unroll
      for (int e = 0; e < 4; ++e) {
        const f32x4 sv = e < 2 ? s0_ : s1_;
        ov[e] = pk2(fmaf(sv[2 * (e & 1)], gt[a][2 * e], xv[a][2 * e]), fmaf(sv[2 * (e & 1) + 1], gt[a][2 * e + 1], xv[a][2 * e + 1]));
      }
      ST4(ov, fxo, mk_rsrc(out, S * D * 2), a * 28672u, "sc0 sc1");
    }
  }
  }
  if (pure) {
    const int id = xc * 4 + (ix >> 3), hd = id >> 1;
    lds_wait(SY_DONE, 4, 8, lane);
    f32x4 a, b;
#pragma unroll 1
    for (unsigned sp = 0; sp < SPIN_CAP; ++sp) {
      u32x4 pr[4];
#pragma unroll
      for (int n = 0; n < 4; ++n) LD4(pr[n], v16, rws, WS_KVP + (hd * 16 + 2 * w + (n >> 1)) * 4096u + (id & 1) * 2048u + (n & 1) * 1024u, "sc1");
      WAIT_VM(0);
      unsigned bad = 0;
#pragma unroll
      for (int n = 0; n < 4; ++n) { PIN(pr[n]); bad |= (pr[n].y ^ tag.x) | (pr[n].w ^ tag.x); }
#define F4(n) (f32x4){bflo(pr[n].x), bfhi(pr[n].x), bflo(pr[n].z), bfhi(pr[n].z)}
      a = F4(0) + F4(2); b = F4(1) + F4(3);
      if (__all(bad == 0)) break;
      __builtin_amdgcn_s_sleep(4);
    }
    *(LDS f32x4*)(w * 2048 + v16) = a; *(LDS f32x4*)(w * 2048 + 1024 + v16) = b;
    BARRIER;
    if (w == 4) {
      a = *(LDS const f32x4*)v16; b = *(LDS const f32x4*)(1024 + v16);
#pragma unroll
      for (int n = 1; n < 8; ++n) { a += *(LDS const f32x4*)(n * 2048 + v16); b += *(LDS const f32x4*)(n * 2048 + 1024 + v16); }
      u32x4 o = {pk2(a[0], a[1]), pk2(a[2], a[3]), pk2(b[0], b[1]), pk2(b[2], b[3])};
      if ((id & 1) && hh != (hd & 1)) { if (hd & 2) { o.x = 0; o.y = 0; } else { o.z = 0; o.w = 0; } }
      ST4(o, v16, rws, WS_KV + id * 1024u, "sc1");
      WAIT_VM(0);
      ST4(((u32x4){tag.x, tag.y, tag.x, tag.y}), (lane >> 3) * 32 * FS + (lane & 7) * 16, rws, WS_KF + id * FS, "sc1");
    } else if (w == 5 && bid == 255) {
#pragma unroll 1
      for (int n = 0; n < 4; ++n) tag_wait((unsigned)((lane >> 4) * 64 + (lane & 15) * 2) * FS, rws, WS_FL + n * 256 * FS, ~0ull, tag);
      if (lane == 0) ST2(tag, 0u + lane, rws, WS_EP, "sc1");
    }
  }
}

extern "C" void kernel_launch(void* const* d_in, const int* in_sizes, int n_in, void* d_out, int out_size, void* d_ws,
                              size_t ws_size, hipStream_t stream) {
  (void)hipFuncSetAttribute((const void*)fused, hipFuncAttributeMaxDynamicSharedMemorySize, L_END);
  fused<<<256, 512, L_END, stream>>>((const u16*)d_in[0], (const u16*)d_in[1], (const u16*)d_in[4], (char*)d_ws, (const u16*)d_in[2],
                                         (const u16*)d_in[5], (const u16*)d_in[6], (const u16*)d_in[7], (u16*)d_out);
}
```
